```python
import torch

def _softmax(x, axis=-1):
    return torch.softmax(x, dim=axis)

def _gelu(x):
    return torch.nn.functional.gelu(x, approximate='tanh')

def _silu(x):
    return torch.nn.functional.silu(x)

def _sigmoid(x):
    return torch.sigmoid(x)

def _layernorm(x, axis=-1, eps=1e-05):
    m = x.mean(dim=axis, keepdim=True)
    v = x.var(dim=axis, keepdim=True, unbiased=False)
    return (x - m) / torch.sqrt(v + eps)

def _scatter_add(tgt, idx, src, axis=0):
    return tgt.clone().index_add_(axis, idx, src)

def _segment_sum(src, seg, n):
    out = torch.zeros((n,) + src.shape[1:], dtype=src.dtype)
    return out.index_add_(0, seg, src)

def _pad(x, pw):
    flat = tuple((v for pair in reversed(pw) for v in pair))
    return torch.nn.functional.pad(x, flat)
kernel_name = 'adaLN_cond__ssm_conv_h3__mixer_s_mlp__prompt'

def setup_inputs():

    _w = lambda a: (lambda t: t.to(torch.bfloat16) if t.is_floating_point() else t)(torch.as_tensor(a))

    def _A_setup():
        B, S, D = (1, 2048, 640)
        ks = 4
        return {'q': _w((lambda *s: torch.randn(*s, dtype=torch.bfloat16) * 0.05)(B, S, D)), 'k': _w((lambda *s: torch.randn(*s, dtype=torch.bfloat16) * 0.8)(B, S, D)), 'v': _w((lambda *s: torch.randn(*s, dtype=torch.bfloat16) * 0.8)(B, S, D)), 'shift_k': _w((lambda *s: torch.randn(*s, dtype=torch.bfloat16) * 0.8)(D, ks) * 0.5), 'ssm_k': _w((lambda *s: torch.randn(*s, dtype=torch.bfloat16) * 0.8)(D, S) * (1.0 / S ** 0.5)), 'ssm_D': _w((lambda *s: torch.randn(*s, dtype=torch.bfloat16) * 0.8)(D))}
    _A_in = _A_setup()

    def _B_setup():
        B, S, D, F = (1, 2048, 640, 1728)
        return {'x': _w((lambda *s: torch.randn(*s, dtype=torch.bfloat16) * 0.05)(B, S, D)), 'w1': _w((lambda *s: torch.randn(*s, dtype=torch.bfloat16) * 3.2)(D, D) * D ** (-0.5)), 'w2': _w((lambda *s: torch.randn(*s, dtype=torch.bfloat16) * 3.2)(D, D) * D ** (-0.5)), 'w3': _w((lambda *s: torch.randn(*s, dtype=torch.bfloat16) * 3.2)(D, F) * D ** (-0.5)), 'w4': _w((lambda *s: torch.randn(*s, dtype=torch.bfloat16) * 3.2)(F, D) * F ** (-0.5))}
    _B_in = _B_setup()
    return {'A_q': _A_in['q'], 'A_k': _A_in['k'], 'A_v': _A_in['v'], 'A_shift_k': _A_in['shift_k'], 'A_ssm_k': _A_in['ssm_k'], 'A_ssm_D': _A_in['ssm_D'], 'B_x': _B_in['x'], 'B_w1': _B_in['w1'], 'B_w2': _B_in['w2'], 'B_w3': _B_in['w3'], 'B_w4': _B_in['w4'], 'c': _w((lambda *s: torch.randn(*s, dtype=torch.bfloat16) * 0.1)(1, 640) * 8.0), 'w_mod': _w((lambda *s: torch.randn(*s, dtype=torch.bfloat16) * 0.1)(640, 6 * 640) * 640 ** (-0.5) * 8.0)}

def reference(A_q, A_k, A_v, A_shift_k, A_ssm_k, A_ssm_D, B_x, B_w1, B_w2, B_w3, B_w4, c, w_mod):

    def _A_ref(q, k, v, shift_k, ssm_k, ssm_D):
        B, S, D = q.shape
        ks = shift_k.shape[-1]
        k_pad = _pad(k, ((0, 0), (ks - 1, 0), (0, 0)))
        k_shift = torch.zeros_like(k)
        for t in range(ks):
            k_shift = k_shift + k_pad[:, t:t + S, :] * shift_k[:, ks - 1 - t][None, None, :]
        kv = k_shift * v
        L = 2 * S
        kv_pad = _pad(kv, ((0, 0), (0, S), (0, 0)))
        kf_pad = _pad(ssm_k, ((0, 0), (0, S)))
        kv_f = (lambda x, axes=(-2, -1): torch.fft.rfft2(x.float(), dim=axes))(kv_pad[:, None, :, :], axes=(1, 2))
        kf_f = (lambda x, axes=(-2, -1): torch.fft.rfft2(x.float(), dim=axes))(kf_pad[:, None, :], axes=(1, 2))
        kf_f = (lambda x, *d: x.permute(*d))(kf_f, 1, 2, 0)
        y_f = kv_f * kf_f[None, :, :, :]
        y = (lambda x, s, axes=(-2, -1): torch.fft.irfft2(x, s=s, dim=axes).to(torch.bfloat16))(y_f, (1, L), axes=(1, 2))[:, 0, :S, :]
        y = y + kv * ssm_D[None, None, :]
        return q * y

    def _B_ref(x, w1, w2, w3, w4):
        B, S, D = x.shape
        h = _gelu(_layernorm(x, axis=-1) @ w1)
        c = D // 4
        g0 = _pad(h[:, :, 0 * c:1 * c], ((0, 0), (1, 0), (0, 0)))[:, :S, :]
        g1 = _pad(h[:, :, 1 * c:2 * c], ((0, 0), (0, 1), (0, 0)))[:, 1:, :]
        g2 = _pad(h[:, :, 2 * c:3 * c], ((0, 0), (2, 0), (0, 0)))[:, :S, :]
        g3 = _pad(h[:, :, 3 * c:], ((0, 0), (0, 2), (0, 0)))[:, 2:, :]
        hs = torch.concatenate([g0, g1, g2, g3], axis=-1) @ w2
        x1 = x + hs
        h2 = _gelu(_layernorm(x1, axis=-1) @ w3) @ w4
        return x1 + h2
    x = A_q
    mod = (_silu(c) @ w_mod).reshape(c.shape[0], 6, 1, -1)
    sa, ca, ga, sb, cb, gb = (mod[:, 0].clone(), mod[:, 1].clone(), mod[:, 2].clone(), mod[:, 3].clone(), mod[:, 4].clone(), mod[:, 5].clone())
    h = _layernorm(x, axis=-1) * (1 + ca) + sa
    x = x + ga * _A_ref(h, A_k, A_v, A_shift_k, A_ssm_k, A_ssm_D)
    h = _layernorm(x, axis=-1) * (1 + cb) + sb
    x = x + gb * _B_ref(h, B_w1, B_w2, B_w3, B_w4)
    return x
```

```cpp
#include <hip/hip_runtime.h>
#include <hip/hip_bf16.h>
#include <stdint.h>

#define RUN(i)

typedef __hip_bfloat16 bf16;
typedef __attribute__((ext_vector_type(8))) short bf16x8;
typedef __attribute__((ext_vector_type(4))) short s16x4;
typedef __attribute__((ext_vector_type(4))) float f32x4;
typedef __attribute__((ext_vector_type(2))) float f32x2;
typedef __attribute__((ext_vector_type(16))) float f32x16;
typedef __attribute__((ext_vector_type(4))) unsigned u32x4;
typedef __attribute__((ext_vector_type(2))) unsigned u32x2;

static constexpr int S_ = 2048;
static constexpr int D_ = 640;
static constexpr int F_ = 1728;

__device__ __forceinline__ float b2f(bf16 x) { return __bfloat162float(x); }
__device__ __forceinline__ bf16 f2b(float x) { return __float2bfloat16(x); }
__device__ __forceinline__ float rbf(float x) { return __bfloat162float(__float2bfloat16(x)); }
__device__ __forceinline__ float lo2f(unsigned w) { return __uint_as_float(w << 16); }
__device__ __forceinline__ float hi2f(unsigned w) { return __uint_as_float(w & 0xffff0000u); }
__device__ __forceinline__ unsigned pack2(float a, float b) {
  bf16 x = f2b(a), y = f2b(b);
  return (unsigned)(*reinterpret_cast<unsigned short*>(&x)) | ((unsigned)(*reinterpret_cast<unsigned short*>(&y)) << 16);
}

__device__ __forceinline__ float gelu_tanh(float x) {
  const float k0 = 0.7978845608028654f, k1 = 0.044715f;
  const float u2 = -2.f * k0 * (x + k1 * x * x * x);
  return __fdividef(x, 1.f + __expf(u2));
}

__device__ __forceinline__ float row16_sum(float v) {
  v += __builtin_bit_cast(float, __builtin_amdgcn_update_dpp(0, __builtin_bit_cast(int, v), 0xB1, 0xf, 0xf, true));
  v += __builtin_bit_cast(float, __builtin_amdgcn_update_dpp(0, __builtin_bit_cast(int, v), 0x4E, 0xf, 0xf, true));
  v += __builtin_bit_cast(float, __builtin_amdgcn_update_dpp(0, __builtin_bit_cast(int, v), 0x141, 0xf, 0xf, true));
  v += __builtin_bit_cast(float, __builtin_amdgcn_update_dpp(0, __builtin_bit_cast(int, v), 0x140, 0xf, 0xf, true));
  return v;
}
__device__ __forceinline__ float rl(float v, int l) {
  return __builtin_bit_cast(float, __builtin_amdgcn_readlane(__builtin_bit_cast(int, v), l));
}
__device__ __forceinline__ float wave_sum(float v) {
  v = row16_sum(v);
  return (rl(v, 0) + rl(v, 16)) + (rl(v, 32) + rl(v, 48));
}
__device__ __forceinline__ float half_sum(float v) {
  v = row16_sum(v);
  const float lo = rl(v, 0) + rl(v, 16), hi = rl(v, 32) + rl(v, 48);
  return (__lane_id() < 32) ? lo : hi;
}

static constexpr int KV_BLOCKS = (S_ / 64) * (D_ / 64);
static constexpr int MOD_BLOCKS = (6 * D_) / 64;
static constexpr int CS_BLOCKS = F_ / 64;

static constexpr int PF_BLOCKS = 96;
__global__ __launch_bounds__(256) void k_prep(const bf16* __restrict__ k, const bf16* __restrict__ v,
                                              const bf16* __restrict__ shift_k, bf16* __restrict__ kvT,
                                              const bf16* __restrict__ w1, const bf16* __restrict__ w2,
                                              const bf16* __restrict__ w3, const bf16* __restrict__ w4) {
  const int tid = threadIdx.x;
  if (blockIdx.x >= KV_BLOCKS) {
    const int b = blockIdx.x - KV_BLOCKS;
    if (b < 32) {
      unsigned long long pc;
      asm volatile("s_getpc_b64 %0" : "=s"(pc));
      const char* cp = (const char*)(pc & ~127ull);
      unsigned a2 = 0;
      const int l = (b >> 3) * 90 + tid;
      if (tid < 90) a2 = *(const __attribute__((address_space(1))) unsigned*)((const __attribute__((address_space(1))) char*)cp + l * 128);
      asm volatile("" ::"v"(a2));
    }
    const bf16* ws[4] = {w1, w2, w3, w4};
    const int lines[4] = {D_ * D_ / 64, D_ * D_ / 64, D_ * F_ / 64, F_ * D_ / 64};
    unsigned acc = 0;
#pragma unroll
    for (int t = 0; t < 4; ++t)
      for (int l = b * 256 + tid; l < lines[t]; l += PF_BLOCKS * 256)
        acc ^= *reinterpret_cast<const unsigned*>(ws[t] + (size_t)l * 64);
    asm volatile("" ::"v"(acc));
    return;
  }
  {
    const int tb = blockIdx.x / (D_ / 64), cb = blockIdx.x % (D_ / 64);
    const int tr = tid >> 5, tc = tid & 31;
    const int t0 = tb * 64 + tr * 8;
    const int d0 = cb * 64 + tc * 2;
    const u32x4 skA = *reinterpret_cast<const u32x4*>(shift_k + (size_t)d0 * 4);
    float sk0[4] = {lo2f(skA[0]), hi2f(skA[0]), lo2f(skA[1]), hi2f(skA[1])};
    float sk1[4] = {lo2f(skA[2]), hi2f(skA[2]), lo2f(skA[3]), hi2f(skA[3])};
    unsigned kw[11];
#pragma unroll
    for (int i = 0; i < 11; ++i) {
      int t = t0 - 3 + i;
      kw[i] = (t >= 0) ? *reinterpret_cast<const unsigned*>(k + (size_t)t * D_ + d0) : 0u;
    }
    unsigned vw[8];
#pragma unroll
    for (int i = 0; i < 8; ++i) vw[i] = *reinterpret_cast<const unsigned*>(v + (size_t)(t0 + i) * D_ + d0);
    float o0[8], o1[8];
#pragma unroll
    for (int i = 0; i < 8; ++i) {
      float a0 = 0.f, a1 = 0.f;
#pragma unroll
      for (int t = 0; t < 4; ++t) {
        a0 = rbf(a0 + rbf(lo2f(kw[i + t]) * sk0[3 - t]));
        a1 = rbf(a1 + rbf(hi2f(kw[i + t]) * sk1[3 - t]));
      }
      o0[i] = a0 * lo2f(vw[i]);
      o1[i] = a1 * hi2f(vw[i]);
    }
    u32x4 w0, w1;
#pragma unroll
    for (int i = 0; i < 4; ++i) {
      w0[i] = pack2(o0[2 * i], o0[2 * i + 1]);
      w1[i] = pack2(o1[2 * i], o1[2 * i + 1]);
    }
    *reinterpret_cast<u32x4*>(kvT + (size_t)d0 * S_ + t0) = w0;
    *reinterpret_cast<u32x4*>(kvT + (size_t)(d0 + 1) * S_ + t0) = w1;
  }
}

static constexpr int CV_ACOPY_STRIDE = 4160;
static constexpr int CV_PART_BYTES = 4 * 64 * 144;
static constexpr int CV_U_OFF = CV_PART_BYTES;
static constexpr int CV_LDS = CV_U_OFF + 128 * 80;

static constexpr int SIDE_BLOCKS = MOD_BLOCKS + CS_BLOCKS;
__device__ __forceinline__ void side_job(int blk, const bf16* __restrict__ c, const bf16* __restrict__ w_mod,
                                         const bf16* __restrict__ w3, float* __restrict__ mod,
                                         float* __restrict__ colsum, unsigned* __restrict__ zero_page, float* red) {
  const int tid = threadIdx.x;
  const bool is_mod = blk < MOD_BLOCKS;
  const int b = is_mod ? blk : blk - MOD_BLOCKS;
  if (is_mod && b == 0 && tid < 64) zero_page[tid] = 0u;
  const int cg = tid & 7, kg = tid >> 3;
  const int ldw = is_mod ? 6 * D_ : F_;
  const bf16* W = is_mod ? w_mod : w3;
  const int n0 = b * 64 + cg * 8;
  u32x4 w[20];
#pragma unroll
  for (int i = 0; i < 20; ++i) w[i] = *reinterpret_cast<const u32x4*>(W + (size_t)(kg + 32 * i) * ldw + n0);
  float acc[8] = {0.f, 0.f, 0.f, 0.f, 0.f, 0.f, 0.f, 0.f};
#pragma unroll
  for (int i = 0; i < 20; ++i) {
    float sc = 1.f;
    if (is_mod) {
      float cv = b2f(c[kg + 32 * i]);
      sc = rbf(cv / (1.f + __expf(-cv)));
    }
#pragma unroll
    for (int j = 0; j < 4; ++j) {
      acc[2 * j] += sc * lo2f(w[i][j]);
      acc[2 * j + 1] += sc * hi2f(w[i][j]);
    }
  }
#pragma unroll
  for (int j = 0; j < 8; ++j) red[kg * 65 + cg * 8 + j] = acc[j];
  __syncthreads();
  if (tid < 64) {
    float s = 0.f;
#pragma unroll
    for (int g = 0; g < 32; ++g) s += red[g * 65 + tid];
    const int n = b * 64 + tid;
    if (is_mod) {
      const float m = rbf(s);
      mod[n] = m;
      if (n >= D_ && n < 2 * D_) mod[6 * D_ + (n - D_)] = rbf(1.f + m);
      if (n >= 4 * D_ && n < 5 * D_) mod[7 * D_ + (n - 4 * D_)] = rbf(1.f + m);
    } else {
      colsum[n] = s;
    }
  }
}

__global__ __launch_bounds__(256) void k_conv(const bf16* __restrict__ kvT, const bf16* __restrict__ ssm_k,
                                              const bf16* __restrict__ ssm_D, bf16* __restrict__ zT,
                                              const bf16* __restrict__ c, const bf16* __restrict__ w_mod,
                                              const bf16* __restrict__ w3, float* __restrict__ mod,
                                              float* __restrict__ colsum, unsigned* __restrict__ zero_page) {
  __shared__ __attribute__((aligned(16))) char lds[CV_LDS];
  if (blockIdx.x < SIDE_BLOCKS) {
    side_job(blockIdx.x, c, w_mod, w3, mod, colsum, zero_page, reinterpret_cast<float*>(lds));
    return;
  }
  const int d = blockIdx.x - SIDE_BLOCKS;
  const int tid = threadIdx.x, lane = tid & 63, wv = tid >> 6;
  const bf16* urow = kvT + (size_t)d * S_;
  const bf16* krow = ssm_k + (size_t)d * S_;
  {
    u32x4 u = *reinterpret_cast<const u32x4*>(urow + tid * 8);
    int b = tid >> 2, ch = tid & 3;
    *reinterpret_cast<u32x4*>(lds + CV_U_OFF + (64 + b) * 80 + ch * 16) = u;
    u32x4 z4 = {0u, 0u, 0u, 0u};
    *reinterpret_cast<u32x4*>(lds + CV_U_OFF + b * 80 + ch * 16) = z4;
  }
  for (int ci = tid; ci < 260; ci += 256) {
    const int Y0 = ci * 8;
    const int B = 2048 - Y0;
    u32x4 lo = {0u, 0u, 0u, 0u}, hi = {0u, 0u, 0u, 0u};
    if (B - 8 >= 0 && B - 8 < 2048) lo = *reinterpret_cast<const u32x4*>(krow + (B - 8));
    if (B >= 0 && B < 2048) hi = *reinterpret_cast<const u32x4*>(krow + B);
    unsigned w[8] = {lo[0], lo[1], lo[2], lo[3], hi[0], hi[1], hi[2], hi[3]};
#pragma unroll
    for (int m = 0; m < 8; ++m) {
      u32x4 o;
      const int a = m >> 1;
#pragma unroll
      for (int t = 0; t < 4; ++t) {
        if ((m & 1) == 0) o[t] = (w[a - t + 4] & 0xffffu) | (w[a - t + 3] & 0xffff0000u);
        else o[t] = (w[a - t + 4] >> 16) | (w[a - t + 4] << 16);
      }
      *reinterpret_cast<u32x4*>(lds + m * CV_ACOPY_STRIDE + Y0 * 2) = o;
    }
  }
  __syncthreads();

  const int r = lane & 31, h = lane >> 5;
  const int aoff = (r & 7) * CV_ACOPY_STRIDE + (2048 + 8 * h - 8 * (r >> 3)) * 2;
  const int boff = CV_U_OFF + (64 + r) * 80 + 16 * h;
  f32x16 acc0 = {0.f}, acc1 = {0.f};
#pragma unroll
  for (int it = 0; it < 16; ++it) {
    const int p = wv + 4 * it;
#pragma unroll
    for (int c = 0; c < 2; ++c) {
      const int c0 = 16 * c;
      bf16x8 af = *reinterpret_cast<const bf16x8*>(lds + aoff - 64 * p + 2 * c0);
      bf16x8 b1 = *reinterpret_cast<const bf16x8*>(lds + boff + 32 * 80 - 80 * p + 2 * c0);
      acc1 = __builtin_amdgcn_mfma_f32_32x32x16_bf16(af, b1, acc1, 0, 0, 0);
      if (it < 8) {
        bf16x8 b0 = *reinterpret_cast<const bf16x8*>(lds + boff - 80 * p + 2 * c0);
        acc0 = __builtin_amdgcn_mfma_f32_32x32x16_bf16(af, b0, acc0, 0, 0, 0);
      }
    }
  }
  __syncthreads();
  {
    char* pb = lds + wv * (64 * 144);
#pragma unroll
    for (int g = 0; g < 4; ++g) {
      f32x4 v0 = {acc0[4 * g], acc0[4 * g + 1], acc0[4 * g + 2], acc0[4 * g + 3]};
      f32x4 v1 = {acc1[4 * g], acc1[4 * g + 1], acc1[4 * g + 2], acc1[4 * g + 3]};
      *reinterpret_cast<f32x4*>(pb + r * 144 + (8 * g + 4 * h) * 4) = v0;
      *reinterpret_cast<f32x4*>(pb + (32 + r) * 144 + (8 * g + 4 * h) * 4) = v1;
    }
  }
  __syncthreads();
  {
    const int i = tid >> 2, r0 = (tid & 3) * 8;
    float y[8];
#pragma unroll
    for (int j = 0; j < 8; ++j) y[j] = 0.f;
#pragma unroll
    for (int w = 0; w < 4; ++w) {
      f32x4 a = *reinterpret_cast<const f32x4*>(lds + w * (64 * 144) + i * 144 + r0 * 4);
      f32x4 b = *reinterpret_cast<const f32x4*>(lds + w * (64 * 144) + i * 144 + r0 * 4 + 16);
#pragma unroll
      for (int j = 0; j < 4; ++j) { y[j] += a[j]; y[4 + j] += b[j]; }
    }
    u32x4 u = *reinterpret_cast<const u32x4*>(lds + CV_U_OFF + (64 + i) * 80 + r0 * 2);
    const float Dv = b2f(ssm_D[d]);
    u32x4 o;
#pragma unroll
    for (int j = 0; j < 4; ++j) {
      float z0 = rbf(y[2 * j]) + rbf(lo2f(u[j]) * Dv);
      float z1 = rbf(y[2 * j + 1]) + rbf(hi2f(u[j]) * Dv);
      o[j] = pack2(z0, z1);
    }
    *reinterpret_cast<u32x4*>(zT + (size_t)d * S_ + tid * 8) = o;
  }
}

__device__ __forceinline__ void ln10(const float* in, float* out) {
  float s = 0.f;
#pragma unroll
  for (int i = 0; i < 10; ++i) s += in[i];
  s = wave_sum(s);
  const float mean = s * (1.f / (float)D_);
  float q = 0.f;
#pragma unroll
  for (int i = 0; i < 10; ++i) { float dd = in[i] - mean; q += dd * dd; }
  q = wave_sum(q);
  const float var = rbf(q * (1.f / (float)D_));
  const float m = rbf(mean);
  const float den = rbf(sqrtf(rbf(var + 1e-5f)));
  const float rinv = 1.f / den;
#pragma unroll
  for (int i = 0; i < 10; ++i) out[i] = rbf(rbf(in[i] - m) * rinv);
}

__global__ __launch_bounds__(512) void k_row(const bf16* __restrict__ q, const bf16* __restrict__ zT,
                                             const float* __restrict__ mod, bf16* __restrict__ xo,
                                             bf16* __restrict__ hp, bf16* __restrict__ a1) {
  __shared__ __attribute__((aligned(16))) unsigned short zs[D_][8];
  const int tid = threadIdx.x, lane = tid & 63, wv = tid >> 6;
  const int tg = (blockIdx.x & 7) * (S_ / 64) + (blockIdx.x >> 3);
  const int s0 = tg * 8;
  for (int ch = tid; ch < D_; ch += 512)
    *reinterpret_cast<u32x4*>(&zs[ch][0]) = *reinterpret_cast<const u32x4*>(zT + (size_t)ch * S_ + s0);
  const int s = s0 + wv;
  float xq[10], t[10], hh[10];
#pragma unroll
  for (int j = 0; j < 5; ++j) {
    unsigned w = *reinterpret_cast<const unsigned*>(q + (size_t)s * D_ + 2 * (lane + 64 * j));
    xq[2 * j] = lo2f(w);
    xq[2 * j + 1] = hi2f(w);
  }
  f32x2 msa[5], mca[5], mga[5], msb[5], mcb[5];
#pragma unroll
  for (int j = 0; j < 5; ++j) {
    const int d = 2 * (lane + 64 * j);
    msa[j] = *reinterpret_cast<const f32x2*>(mod + 0 * D_ + d);
    mca[j] = *reinterpret_cast<const f32x2*>(mod + 6 * D_ + d);
    mga[j] = *reinterpret_cast<const f32x2*>(mod + 2 * D_ + d);
    msb[j] = *reinterpret_cast<const f32x2*>(mod + 3 * D_ + d);
    mcb[j] = *reinterpret_cast<const f32x2*>(mod + 7 * D_ + d);
  }
  ln10(xq, t);
  __syncthreads();
#pragma unroll
  for (int j = 0; j < 5; ++j) {
#pragma unroll
    for (int e = 0; e < 2; ++e) {
      const int i = 2 * j + e;
      const int d = 2 * (lane + 64 * j) + e;
      const float hv = rbf(rbf(t[i] * mca[j][e]) + msa[j][e]);
      const float zv = __uint_as_float((unsigned)zs[d][wv] << 16);
      const float ao = rbf(hv * zv);
      xq[i] = rbf(xq[i] + rbf(mga[j][e] * ao));
    }
    *reinterpret_cast<unsigned*>(xo + (size_t)s * D_ + 2 * (lane + 64 * j)) = pack2(xq[2 * j], xq[2 * j + 1]);
  }
  ln10(xq, t);
#pragma unroll
  for (int j = 0; j < 5; ++j) {
#pragma unroll
    for (int e = 0; e < 2; ++e) {
      const int i = 2 * j + e;
      hh[i] = rbf(rbf(t[i] * mcb[j][e]) + msb[j][e]);
    }
    *reinterpret_cast<unsigned*>(hp + (size_t)s * D_ + 2 * (lane + 64 * j)) = pack2(hh[2 * j], hh[2 * j + 1]);
  }
  ln10(hh, t);
#pragma unroll
  for (int j = 0; j < 5; ++j)
    *reinterpret_cast<unsigned*>(a1 + (size_t)s * D_ + 2 * (lane + 64 * j)) = pack2(t[2 * j], t[2 * j + 1]);
}

#define NLW_ 4
static constexpr int NLW = NLW_;
static constexpr int NCW = 8;
static constexpr int GTHR = (NCW + NLW) * 64;
static constexpr int STAT_TILES = 8;

template <int BM, int BN, int BNS, int K, int N, int MODE, int NST>
__global__ __launch_bounds__(GTHR) void k_gemm(const bf16* __restrict__ A, const bf16* __restrict__ B,
                                               bf16* __restrict__ out, const bf16* __restrict__ res,
                                               const bf16* __restrict__ xres, const float* __restrict__ mod,
                                               f32x2* __restrict__ stats, const float* __restrict__ colsum,
                                               const bf16* __restrict__ zero_page) {
  constexpr int MT = BM / 64, NT = BN / 16;
  constexpr int ACH = BM * 8, BCW = BN / 8, BCH = 64 * BCW;
  constexpr int LT = NLW * 64;
  constexpr int TCH = ACH + BCH;
  constexpr int IPW = (TCH + LT - 1) / LT;
  constexpr int STAGE = TCH * 16;
  constexpr int RLAST = TCH - (IPW - 1) * LT;
  constexpr int NKT = K / 64;
  constexpr int CST = BN * 2 + 16;
  constexpr int RED_OFF = ((BM * CST + 255) / 256) * 256;
  constexpr int LN_OFF = NST * STAGE;
  static_assert(K % 64 == 0, "K");
  static_assert(RED_OFF + BM * BN * 4 <= NST * STAGE, "lds red");
  extern __shared__ __attribute__((aligned(16))) char lds[];
  const int tid = threadIdx.x, lane = tid & 63;
  const int wv = __builtin_amdgcn_readfirstlane(tid >> 6);
  constexpr int NTN = (N + BNS - 1) / BNS;
  constexpr int NTM = S_ / BM;
  static_assert(NTM % 8 == 0, "xcd map");
  const int xcd = blockIdx.x & 7, jj = blockIdx.x >> 3;
  const int tm = xcd * (NTM / 8) + jj / NTN, tn = jj % NTN;
  const int m0 = tm * BM, n0 = tn * BNS;

  constexpr int PPR = BNS / 4;
  static_assert(PPR <= 32, "ppr");
  constexpr int NPASS = (BM + 2 * (NCW + NLW) - 1) / (2 * (NCW + NLW));
  const int hl = lane & 31;
  u32x2 pre_r[NPASS], pre_x[NPASS];
#pragma unroll
  for (int ps = 0; ps < NPASS; ++ps) {
    pre_r[ps] = u32x2{0u, 0u};
    pre_x[ps] = u32x2{0u, 0u};
    const int r = wv * 2 + (lane >> 5) + ps * 2 * (NCW + NLW);
    const int n = n0 + hl * 4;
    if (r < BM && hl < PPR && n < N) {
      const size_t o = (size_t)(m0 + r) * N + n;
      if (MODE == 1 || MODE == 2) pre_r[ps] = *reinterpret_cast<const u32x2*>(res + o);
      if (MODE == 2) pre_x[ps] = *reinterpret_cast<const u32x2*>(xres + o);
    }
  }
  f32x4 pre_gb = {0.f, 0.f, 0.f, 0.f};
  if (MODE == 2 && hl < PPR && n0 + hl * 4 < N) pre_gb = *reinterpret_cast<const f32x4*>(mod + 5 * D_ + n0 + hl * 4);

  if (wv >= NCW) {
    const int lt = tid - NCW * 64;
    const bool has_last = (wv - NCW) * 64 < RLAST;
    const char* srcp[IPW];
    int inc[IPW];
#pragma unroll
    for (int i = 0; i < IPW; ++i) {
      const int c = i * LT + lt;
      if (c < ACH) {
        const int row = c >> 3, pc = c & 7;
        const int lch = pc ^ ((row >> 1) & 7);
        srcp[i] = (const char*)(A + (size_t)(m0 + row) * K + lch * 8);
        inc[i] = 128;
      } else if (c < TCH) {
        const int cb = c - ACH;
        const int krow = cb / BCW, nch = cb % BCW;
        const int nn = n0 + nch * 8;
        const bool ok = nn < N;
        srcp[i] = ok ? (const char*)(B + (size_t)krow * N + nn) : (const char*)zero_page;
        inc[i] = ok ? 64 * N * 2 : 0;
      } else {
        srcp[i] = (const char*)zero_page;
        inc[i] = 0;
      }
    }
    auto issue = [&](int kt, int slot) {
#pragma unroll
      for (int i = 0; i < IPW; ++i) {
        const char* src = srcp[i];
        if (MODE == 1 && i * LT < ACH) {
          const int c = i * LT + lt;
          if (c < ACH) {
            const int row = c >> 3, pc = c & 7;
            const int lch = pc ^ ((row >> 1) & 7);
            const int kcol = kt * 64 + lch * 8;
            const int g = kcol / 160;
            const int sh = (g == 0) ? -1 : (g == 1) ? 1 : (g == 2) ? -2 : 2;
            const int gr = m0 + row + sh;
            src = (gr >= 0 && gr < S_) ? src + (long)sh * (K * 2) : (const char*)zero_page;
          }
        }
        if (i < IPW - 1 || lt < RLAST)
          __builtin_amdgcn_global_load_lds((const unsigned*)src, (unsigned*)(lds + slot * STAGE + (i * LT + lt) * 16), 16, 0, 0);
        srcp[i] += inc[i];
      }
    };
#pragma unroll
    for (int st = 0; st < NST - 1; ++st) issue(st, st);
    for (int kt = 0; kt < NKT; ++kt) {
      if (kt + NST - 1 <= NKT) {
        if (has_last) asm volatile("s_waitcnt vmcnt(%0)" ::"n"((NST - 2) * IPW) : "memory");
        else asm volatile("s_waitcnt vmcnt(%0)" ::"n"((NST - 2) * (IPW - 1)) : "memory");
      } else {
        asm volatile("s_waitcnt vmcnt(0)" ::: "memory");
      }
      __builtin_amdgcn_s_barrier();
      asm volatile("" ::: "memory");
      if (kt + NST - 1 < NKT) issue(kt + NST - 1, (kt + NST - 1) % NST);
    }
    __builtin_amdgcn_s_barrier();
    __builtin_amdgcn_s_barrier();
  } else {
    if (MODE == 3 && tid < BM) {
      float mt[STAT_TILES], m2 = 0.f, ms = 0.f;
#pragma unroll
      for (int t = 0; t < STAT_TILES; ++t) {
        f32x2 v = stats[(size_t)(m0 + tid) * STAT_TILES + t];
        mt[t] = v[0];
        m2 += v[1];
        ms += v[0];
      }
      const float mean = ms * (1.f / STAT_TILES);
#pragma unroll
      for (int t = 0; t < STAT_TILES; ++t) { const float dd = mt[t] - mean; m2 += 80.f * dd * dd; }
      const float var = rbf(m2 * (1.f / (float)D_));
      const float den = rbf(sqrtf(rbf(var + 1e-5f)));
      f32x2 o = {rbf(mean), 1.f / den};
      *reinterpret_cast<f32x2*>(lds + LN_OFF + tid * 8) = o;
    }
    f32x4 acc[MT][NT];
#pragma unroll
    for (int a = 0; a < MT; ++a)
#pragma unroll
      for (int b = 0; b < NT; ++b) acc[a][b] = f32x4{0.f, 0.f, 0.f, 0.f};
    f32x4 pre_cs[NT];
    if (MODE == 3) {
#pragma unroll
      for (int j = 0; j < NT; ++j) {
        const int n = n0 + j * 16 + 4 * (lane >> 4);
        pre_cs[j] = (n + 3 < N) ? *reinterpret_cast<const f32x4*>(colsum + n) : f32x4{0.f, 0.f, 0.f, 0.f};
      }
    }
    const int mg = wv & 3, ks = wv >> 2;
    const int wrow0 = mg * (16 * MT);
    const int g = lane >> 4, qq = (lane & 15) >> 2, pp = lane & 3;
    int aoff[MT];
#pragma unroll
    for (int a = 0; a < MT; ++a) {
      const int row = wrow0 + a * 16 + (lane & 15);
      aoff[a] = row * 128 + (((ks * 4 + g) ^ ((row >> 1) & 7)) << 4);
    }
    const int boff = ACH * 16 + (ks * 32 + 8 * g + qq) * (BN * 2) + 4 * pp * 2;
    for (int kt = 0; kt < NKT; ++kt) {
      __builtin_amdgcn_s_barrier();
      asm volatile("" ::: "memory");
      const unsigned sbase = (unsigned)(size_t)(lds + (kt % NST) * STAGE);
      f32x4 afr[MT];
      s16x4 blo[NT], bhi[NT];
#pragma unroll
      for (int a = 0; a < MT; ++a) asm volatile("ds_read_b128 %0, %1" : "=v"(afr[a]) : "v"(sbase + aoff[a]) : "memory");
#pragma unroll
      for (int j = 0; j < NT; ++j) {
        asm volatile("ds_read_b64_tr_b16 %0, %1 offset:%2" : "=v"(blo[j]) : "v"(sbase + boff), "n"(j * 32) : "memory");
        asm volatile("ds_read_b64_tr_b16 %0, %1 offset:%2" : "=v"(bhi[j]) : "v"(sbase + boff), "n"(j * 32 + 4 * BN * 2) : "memory");
      }
      asm volatile("s_waitcnt lgkmcnt(0)" ::: "memory");
#pragma unroll
      for (int a = 0; a < MT; ++a) asm volatile("" : "+v"(afr[a]));
#pragma unroll
      for (int j = 0; j < NT; ++j) asm volatile("" : "+v"(blo[j]), "+v"(bhi[j]));
#pragma unroll
      for (int j = 0; j < NT; ++j) {
        bf16x8 bfr = {blo[j][0], blo[j][1], blo[j][2], blo[j][3], bhi[j][0], bhi[j][1], bhi[j][2], bhi[j][3]};
#pragma unroll
        for (int a = 0; a < MT; ++a)
          acc[a][j] = __builtin_amdgcn_mfma_f32_16x16x32_bf16(bfr, __builtin_bit_cast(bf16x8, afr[a]), acc[a][j], 0, 0, 0);
      }
    }
    constexpr int NH = (NT + 1) / 2;
    __builtin_amdgcn_s_barrier();
#pragma unroll
    for (int a = 0; a < MT; ++a)
#pragma unroll
      for (int j = 0; j < NT; ++j)
        if ((j >= NH) != (ks == 1))
          *reinterpret_cast<f32x4*>(lds + RED_OFF + (((mg * MT + a) * NT + j) * 64 + lane) * 16) = acc[a][j];
    __builtin_amdgcn_s_barrier();
#pragma unroll
    for (int a = 0; a < MT; ++a) {
      const int r = wrow0 + a * 16 + (lane & 15);
      float lm = 0.f, lr = 1.f;
      if (MODE == 3) {
        f32x2 v = *reinterpret_cast<const f32x2*>(lds + LN_OFF + r * 8);
        lm = v[0];
        lr = v[1];
      }
#pragma unroll
      for (int j = 0; j < NT; ++j) {
        if ((j >= NH) == (ks == 1)) {
          f32x4 o = *reinterpret_cast<const f32x4*>(lds + RED_OFF + (((mg * MT + a) * NT + j) * 64 + lane) * 16);
          o += acc[a][j];
          if (MODE == 3) {
#pragma unroll
            for (int e = 0; e < 4; ++e) o[e] = (o[e] - lm * pre_cs[j][e]) * lr;
          }
          u32x2 w;
          w[0] = pack2(o[0], o[1]);
          w[1] = pack2(o[2], o[3]);
          *reinterpret_cast<u32x2*>(lds + r * CST + (j * 16 + 4 * g) * 2) = w;
        }
      }
    }
  }
  __syncthreads();
#pragma unroll
  for (int ps = 0; ps < NPASS; ++ps) {
    const int r = wv * 2 + (lane >> 5) + ps * 2 * (NCW + NLW);
    if (r >= BM) break;
    const int c4 = hl * 4;
    const int n = n0 + c4;
    const bool act = hl < PPR && n < N;
    float v[4] = {0.f, 0.f, 0.f, 0.f};
    const size_t o = (size_t)(m0 + r) * N + n;
    if (act) {
      const u32x2 w = *reinterpret_cast<const u32x2*>(lds + r * CST + c4 * 2);
      float cv[4] = {lo2f(w[0]), hi2f(w[0]), lo2f(w[1]), hi2f(w[1])};
      if (MODE == 0 || MODE == 3) {
#pragma unroll
        for (int e = 0; e < 4; ++e) v[e] = gelu_tanh(cv[e]);
      } else if (MODE == 1) {
        const u32x2 rw = pre_r[ps];
        float rv[4] = {lo2f(rw[0]), hi2f(rw[0]), lo2f(rw[1]), hi2f(rw[1])};
#pragma unroll
        for (int e = 0; e < 4; ++e) v[e] = rbf(rv[e] + cv[e]);
      } else {
        const u32x2 rw = pre_r[ps], xw = pre_x[ps];
        float rv[4] = {lo2f(rw[0]), hi2f(rw[0]), lo2f(rw[1]), hi2f(rw[1])};
        float xv[4] = {lo2f(xw[0]), hi2f(xw[0]), lo2f(xw[1]), hi2f(xw[1])};
#pragma unroll
        for (int e = 0; e < 4; ++e) {
          float bo = rbf(rv[e] + cv[e]);
          v[e] = xv[e] + rbf(pre_gb[e] * bo);
        }
      }
      u32x2 ow;
      ow[0] = pack2(v[0], v[1]);
      ow[1] = pack2(v[2], v[3]);
      *reinterpret_cast<u32x2*>(out + o) = ow;
    }
    if (MODE == 1) {
      const float s = half_sum(v[0] + v[1] + v[2] + v[3]);
      const float mean = s * (1.f / BNS);
      float q = 0.f;
      if (act) {
#pragma unroll
        for (int e = 0; e < 4; ++e) { const float dd = v[e] - mean; q += dd * dd; }
      }
      q = half_sum(q);
      if (hl == 0) {
        f32x2 st = {mean, q};
        stats[(size_t)(m0 + r) * STAT_TILES + tn] = st;
      }
    }
  }
}

template <int BM, int BN, int BNS, int K, int N, int MODE, int NST>
static void launch_gemm(const bf16* A, const bf16* B, bf16* out, const bf16* res, const bf16* xres, const float* mod,
                        f32x2* stats, const float* colsum, const bf16* zero_page, hipStream_t stream) {
  constexpr int ACH = BM * 8, BCH = 64 * (BN / 8);
  constexpr int STAGE = (ACH + BCH) * 16;
  constexpr int LDS = STAGE * NST + BM * 8;
  static_assert(LDS <= 160 * 1024, "lds");
  constexpr int NTN = (N + BNS - 1) / BNS;
  auto kern = k_gemm<BM, BN, BNS, K, N, MODE, NST>;
  (void)hipFuncSetAttribute((const void*)kern, hipFuncAttributeMaxDynamicSharedMemorySize, LDS);
  kern<<<(S_ / BM) * NTN, GTHR, LDS, stream>>>(A, B, out, res, xres, mod, stats, colsum, zero_page);
}

extern "C" void kernel_launch(void* const* d_in, const int* in_sizes, int n_in, void* d_out, int out_size,
                              void* d_ws, size_t ws_size, hipStream_t stream) {
  const bf16* A_q = (const bf16*)d_in[0];
  const bf16* A_k = (const bf16*)d_in[1];
  const bf16* A_v = (const bf16*)d_in[2];
  const bf16* A_shift_k = (const bf16*)d_in[3];
  const bf16* A_ssm_k = (const bf16*)d_in[4];
  const bf16* A_ssm_D = (const bf16*)d_in[5];
  const bf16* w1 = (const bf16*)d_in[7];
  const bf16* w2 = (const bf16*)d_in[8];
  const bf16* w3 = (const bf16*)d_in[9];
  const bf16* w4 = (const bf16*)d_in[10];
  const bf16* c = (const bf16*)d_in[11];
  const bf16* w_mod = (const bf16*)d_in[12];
  bf16* out = (bf16*)d_out;

  char* ws = (char*)d_ws;
  size_t off = 0;
  auto alloc = [&](size_t bytes) { char* p = ws + off; off += (bytes + 255) & ~(size_t)255; return p; };
  unsigned* zero_page = (unsigned*)alloc(256);
  float* mod = (float*)alloc(8 * D_ * 4);
  float* colsum = (float*)alloc(F_ * 4);
  f32x2* stats = (f32x2*)alloc((size_t)S_ * STAT_TILES * 8);
  bf16* kvT = (bf16*)alloc((size_t)S_ * D_ * 2);
  bf16* zT = (bf16*)alloc((size_t)S_ * D_ * 2);
  bf16* xo = (bf16*)alloc((size_t)S_ * D_ * 2);
  bf16* hp = (bf16*)alloc((size_t)S_ * D_ * 2);
  bf16* a1 = (bf16*)alloc((size_t)S_ * D_ * 2);
  bf16* h1 = (bf16*)alloc((size_t)S_ * D_ * 2);
  bf16* x1 = (bf16*)alloc((size_t)S_ * D_ * 2);
  bf16* H = (bf16*)alloc((size_t)S_ * F_ * 2);
  const bf16* zp = (const bf16*)zero_page;

  RUN(0) k_prep<<<KV_BLOCKS + PF_BLOCKS, 256, 0, stream>>>(A_k, A_v, A_shift_k, kvT, w1, w2, w3, w4);
  RUN(1) k_conv<<<D_ + SIDE_BLOCKS, 256, 0, stream>>>(kvT, A_ssm_k, A_ssm_D, zT, c, w_mod, w3, mod, colsum, zero_page);
  RUN(2) k_row<<<S_ / 8, 512, 0, stream>>>(A_q, zT, mod, xo, hp, a1);
  RUN(3) launch_gemm<64, 80, 80, D_, D_, 0, 5>(a1, w1, h1, nullptr, nullptr, mod, stats, colsum, zp, stream);
  RUN(4) launch_gemm<64, 80, 80, D_, D_, 1, 5>(h1, w2, x1, hp, nullptr, mod, stats, colsum, zp, stream);
  RUN(6) launch_gemm<128, 112, 108, D_, F_, 3, 4>(x1, w3, H, nullptr, nullptr, mod, stats, colsum, zp, stream);
  RUN(7) launch_gemm<64, 80, 80, F_, D_, 2, 5>(H, w4, out, x1, xo, mod, stats, colsum, zp, stream);
}
```

```cpp
#include <hip/hip_runtime.h>
#include <stdint.h>

typedef unsigned v4u __attribute__((ext_vector_type(4)));
typedef unsigned v2u __attribute__((ext_vector_type(2)));
typedef float f4 __attribute__((ext_vector_type(4)));
typedef float f2 __attribute__((ext_vector_type(2)));
typedef short s8v __attribute__((ext_vector_type(8)));
typedef short s4v __attribute__((ext_vector_type(4)));
typedef __bf16 bf2 __attribute__((ext_vector_type(2)));
typedef __bf16 bf8 __attribute__((ext_vector_type(8)));
typedef __amdgpu_buffer_rsrc_t rsrc_t;
#define LAS __attribute__((address_space(3)))
#define GAS __attribute__((address_space(1)))
#define DI __device__ __forceinline__

static constexpr int S_ = 2048, D_ = 640, F_ = 1728;
static constexpr unsigned OOB = 0x80000000u;

static constexpr unsigned O_EPOCH = 0;
static constexpr unsigned O_FP = 1024;
static constexpr unsigned O_FR = O_FP + 16 * 128;
static constexpr unsigned O_F1 = O_FR + 32 * 128;
static constexpr unsigned O_F2 = O_F1 + 32 * 128;
static constexpr unsigned O_F3 = O_F2 + 16 * 128;
static constexpr unsigned O_CNT = O_F3 + 16 * 128;
static constexpr unsigned O_FW = O_CNT + 64 * 128;
static constexpr unsigned O_MOD = 65536;
static constexpr unsigned O_GB = O_MOD + 8192;
static constexpr unsigned O_STAT = O_MOD + 16384;
static constexpr unsigned O_KVT = O_STAT + 131072;
static constexpr unsigned SD2 = S_ * D_ * 2;
static constexpr unsigned O_ZT = O_KVT + SD2;
static constexpr unsigned O_A1 = O_ZT + SD2;
static constexpr unsigned O_HP = O_A1 + SD2;
static constexpr unsigned O_X = O_HP + SD2;
static constexpr unsigned O_H1 = O_X + SD2;
static constexpr unsigned O_X1 = O_H1 + SD2;
static constexpr unsigned O_H = O_X1 + SD2;
static constexpr unsigned O_W1 = O_H + 128 * 56 * 1024;
static constexpr unsigned O_W2 = O_W1 + 800 * 1024;
static constexpr unsigned O_W3 = O_W2 + 800 * 1024;
static constexpr unsigned O_W4 = O_W3 + 2240 * 1024;
static constexpr unsigned O_END = O_W4 + 2240 * 1024;
static constexpr unsigned O_DBG = O_END;

static constexpr int CS = 4160;
static constexpr int UOFF = 8 * CS;
static constexpr int WR = 8 * CS + 4608;
static constexpr int LDS_BYTES = 4 * WR;
static constexpr int CSUM_OFF = 140 * 1024;
static constexpr unsigned SPIN_CAP = 200000;

DI float lo2f(unsigned w) { return __builtin_bit_cast(float, w << 16); }
DI float hi2f(unsigned w) { return __builtin_bit_cast(float, w & 0xffff0000u); }
DI f2 up2(unsigned w) { return f2{lo2f(w), hi2f(w)}; }
DI unsigned pk2(float a, float b) { return __builtin_bit_cast(unsigned, __builtin_convertvector(f2{a, b}, bf2)); }
DI f4 mfma(v4u a, v4u b, f4 c) {
  return __builtin_amdgcn_mfma_f32_16x16x32_bf16(__builtin_bit_cast(bf8, a), __builtin_bit_cast(bf8, b), c, 0, 0, 0);
}
DI rsrc_t mkrs(const void* p, unsigned bytes) { return __builtin_amdgcn_make_buffer_rsrc((void*)p, 0, bytes, 0x00020000); }
DI v4u ld128(rsrc_t r, unsigned v, unsigned s) { return __builtin_amdgcn_raw_buffer_load_b128(r, v, s, 0); }
DI v2u ld64(rsrc_t r, unsigned v, unsigned s) { return __builtin_amdgcn_raw_buffer_load_b64(r, v, s, 0); }
DI unsigned ld32(rsrc_t r, unsigned v, unsigned s) { return __builtin_amdgcn_raw_buffer_load_b32(r, v, s, 0); }
DI v4u hld128(rsrc_t r, unsigned v, unsigned s) { return __builtin_amdgcn_raw_buffer_load_b128(r, v, s, 16); }
DI v2u hld64(rsrc_t r, unsigned v, unsigned s) { return __builtin_amdgcn_raw_buffer_load_b64(r, v, s, 16); }
DI void hst128(v4u x, rsrc_t r, unsigned v, unsigned s) { __builtin_amdgcn_raw_buffer_store_b128(x, r, v, s, 16); }
DI void hst64(v2u x, rsrc_t r, unsigned v, unsigned s) { __builtin_amdgcn_raw_buffer_store_b64(x, r, v, s, 16); }
DI void hst32(unsigned x, rsrc_t r, unsigned v, unsigned s) { __builtin_amdgcn_raw_buffer_store_b32(x, r, v, s, 16); }
#define DRAIN() asm volatile("s_waitcnt vmcnt(0)" ::: "memory")
#define WGBAR() asm volatile("s_waitcnt lgkmcnt(0)\n\ts_barrier" ::: "memory")
#define PIN() __builtin_amdgcn_sched_barrier(0)

template <int CTRL> DI float dpp(float x) {
  return __builtin_bit_cast(float, __builtin_amdgcn_mov_dpp(__builtin_bit_cast(int, x), CTRL, 0xf, 0xf, true));
}
DI float xrow_sum(float x) {
  auto s = __builtin_amdgcn_permlane16_swap(__builtin_bit_cast(unsigned, x), __builtin_bit_cast(unsigned, x), false, false);
  x = __builtin_bit_cast(float, (unsigned)s[0]) + __builtin_bit_cast(float, (unsigned)s[1]);
  auto t = __builtin_amdgcn_permlane32_swap(__builtin_bit_cast(unsigned, x), __builtin_bit_cast(unsigned, x), false, false);
  return __builtin_bit_cast(float, (unsigned)t[0]) + __builtin_bit_cast(float, (unsigned)t[1]);
}
DI float wave_sum(float v) {
  v += dpp<0xB1>(v);
  v += dpp<0x4E>(v);
  v += dpp<0x141>(v);
  v += dpp<0x128>(v);
  return xrow_sum(v);
}
DI f2 gelu2(f2 x) {
  const float c0 = -2.f * 0.7978845608028654f * 1.4426950408889634f, c1 = c0 * 0.044715f;
  const f2 a = x * (x * x * c1 + c0);
  const f2 e = f2{__builtin_amdgcn_exp2f(a.x), __builtin_amdgcn_exp2f(a.y)} + 1.f;
  return x * f2{__builtin_amdgcn_rcpf(e.x), __builtin_amdgcn_rcpf(e.y)};
}
DI void poll64(rsrc_t ws, unsigned voff, v2u want) {
  const bool idle = voff == OOB;
  for (unsigned spin = 0; spin < SPIN_CAP; ++spin) {
    asm volatile("" ::: "memory");
    const v2u v = hld64(ws, voff, 0);
    if (__all(idle || (v.x == want.x && v.y == want.y))) break;
  }
  __builtin_amdgcn_fence(__ATOMIC_ACQUIRE, "wavefront");
}

DI bool wflags_ok(v4u a, int lane, v2u want) {
  return __all(lane >= 48 || (a.x == want.x && a.y == want.y && a.z == want.x && a.w == want.y));
}
DI void pollw(rsrc_t ws, unsigned voff, int lane, v2u want) {
  for (unsigned spin = 0; spin < SPIN_CAP; ++spin) {
    asm volatile("" ::: "memory");
    if (wflags_ok(hld128(ws, voff, 0), lane, want)) break;
    __builtin_amdgcn_s_sleep(4);
  }
  __builtin_amdgcn_fence(__ATOMIC_ACQUIRE, "wavefront");
}
DI float rbf(float x) { return lo2f(pk2(x, 0.f)); }
DI void ln_scal(f2 s, f2 ss, f2& mean, f2& r) {
  const f2 m = s * (1.f / D_);
  const f2 var = ss * (1.f / D_) - m * m;
  const f2 v = up2(pk2(fmaxf(var.x, 0.f), fmaxf(var.y, 0.f))) + 1e-5f;
  const f2 vr = up2(pk2(v.x, v.y));
  const f2 den = up2(pk2(__builtin_amdgcn_sqrtf(vr.x), __builtin_amdgcn_sqrtf(vr.y)));
  mean = up2(pk2(m.x, m.y));
  r = f2{__builtin_amdgcn_rcpf(den.x), __builtin_amdgcn_rcpf(den.y)};
}
template <int N> DI void gelu_pack(f2 (&x)[N], unsigned* o) {
  const float c0 = -2.f * 0.7978845608028654f * 1.4426950408889634f, c1 = c0 * 0.044715f;
  f2 a[N];
#pragma unroll
  for (int i = 0; i < N; ++i) a[i] = x[i] * x[i] * c1 + c0;
  PIN();
#pragma unroll
  for (int i = 0; i < N; ++i) a[i] *= x[i];
  PIN();
#pragma unroll
  for (int i = 0; i < N; ++i) a[i] = f2{__builtin_amdgcn_exp2f(a[i].x), __builtin_amdgcn_exp2f(a[i].y)};
  PIN();
#pragma unroll
  for (int i = 0; i < N; ++i) a[i] += 1.f;
  PIN();
#pragma unroll
  for (int i = 0; i < N; ++i) a[i] = f2{__builtin_amdgcn_rcpf(a[i].x), __builtin_amdgcn_rcpf(a[i].y)};
  PIN();
#pragma unroll
  for (int i = 0; i < N; ++i) a[i] *= x[i];
  PIN();
#pragma unroll
  for (int i = 0; i < N; ++i) o[i] = pk2(a[i].x, a[i].y);
}

#define STAMP(i)

struct Unit { rsrc_t rs; unsigned pitch, colb, rowA, rowB, nc, dst, odd, clim; };
DI Unit unit_desc(int u, const void* w1, const void* w2, const void* w3, const void* w4) {
  Unit t;
  if (u < 640) {
    const bool is3 = u >= 320;
    const int uu = is3 ? u - 320 : (u >= 160 ? u - 160 : u);
    const int grp = uu / 20, ks = uu % 20, P = ks / 5, e = ks % 5;
    t.rs = mkrs(is3 ? w3 : (u >= 160 ? w2 : w1), is3 ? D_ * F_ * 2 : D_ * D_ * 2);
    t.pitch = is3 ? F_ * 2 : D_ * 2;
    t.nc = is3 ? 14 : 10;
    t.colb = grp * t.nc * 16;
    t.odd = e == 4;
    t.rowA = e == 4 ? 160 * P + 64 : 160 * P + 80 * (e >> 1) + 32 * (e & 1);
    t.rowB = e == 4 ? 160 * P + 144 : t.rowA + 16;
    t.dst = is3 ? O_W3 + uu * 7168 : (u >= 160 ? O_W2 : O_W1) + uu * 5120;
  } else {
    const int uu = u - 640, grp = uu / 56, ks = uu % 56, Q = ks / 7, e = ks % 7;
    t.rs = mkrs(w4, F_ * D_ * 2);
    t.pitch = D_ * 2;
    t.nc = 10;
    t.colb = grp * 160;
    t.odd = e == 6;
    t.rowA = e == 6 ? 224 * Q + 96 : 224 * Q + 112 * (e / 3) + 32 * (e % 3);
    t.rowB = e == 6 ? 224 * Q + 208 : t.rowA + 16;
    t.dst = O_W4 + uu * 5120;
  }
  t.clim = t.pitch;
  return t;
}
static constexpr int RT_PITCH = 240;
DI void unit_load(const Unit& t, int lane, v4u (&r)[7]) {
  const unsigned magic = t.nc == 10 ? 6554u : 4682u;
#pragma unroll
  for (int i = 0; i < 7; ++i) {
    const unsigned c = i * 64 + lane, row = (c * magic) >> 16, col = c - row * t.nc;
    const unsigned cb = t.colb + col * 16;
    const unsigned srow = row < 16 ? t.rowA + row : t.rowB + row - 16;
    r[i] = ld128(t.rs, (row < 32 && cb < t.clim) ? srow * t.pitch + cb : OOB, 0);
  }
}
DI void unit_store(const Unit& t, int lane, const v4u (&r)[7], LAS char* L, rsrc_t ws) {
  const unsigned magic = t.nc == 10 ? 6554u : 4682u;
#pragma unroll
  for (int i = 0; i < 7; ++i) {
    const unsigned c = i * 64 + lane, row = (c * magic) >> 16, col = c - row * t.nc;
    *(LAS v4u*)(L + row * RT_PITCH + col * 16) = r[i];
  }
  asm volatile("s_waitcnt lgkmcnt(0)" ::: "memory");
  const int g = lane >> 4, q = (lane & 15) >> 2, p = lane & 3;
  const unsigned rlo = (t.odd ? 4 * g : 8 * g) + q, rhi = (t.odd ? 16 + 4 * g : 8 * g + 4) + q;
  const unsigned np = t.nc == 10 ? 2 : 3;
#pragma unroll
  for (int f = 0; f < 7; ++f) {
    const bool last = (unsigned)f == 2 * np;
    const unsigned col = last ? 32 * np + 4 * p : 32 * (f >> 1) + 8 * p + 4 * (f & 1);
    const s4v lo = __builtin_amdgcn_ds_read_tr16_b64_v4i16((LAS s4v*)(L + rlo * RT_PITCH + col * 2));
    const s4v hi = __builtin_amdgcn_ds_read_tr16_b64_v4i16((LAS s4v*)(L + rhi * RT_PITCH + col * 2));
    const v2u a = __builtin_bit_cast(v2u, lo), b = __builtin_bit_cast(v2u, hi);
    hst128(v4u{a.x, a.y, b.x, b.y}, ws, (unsigned)f <= 2 * np ? lane * 16 + f * 1024 : OOB, t.dst);
  }
  asm volatile("s_waitcnt lgkmcnt(0)" ::: "memory");
}

DI void red_put(LAS char* L, int m, int slot, int lane, const f4 (&acc)[5]) {
#pragma unroll
  for (int t = 0; t < 5; ++t) *(LAS f4*)(L + ((m * 3 + slot) * 5 + t) * 1024 + lane * 16) = acc[t];
}
template <int KS, int WIN, class Extra>
DI void gemm_ks(rsrc_t ws, unsigned abase, unsigned mstride, const v4u* b, LAS char* L, int w, int lane, f4 (&acc)[5], Extra extra) {
  constexpr int NA = 4 * KS;
  v4u a[WIN];
  unsigned so[4];
#pragma unroll
  for (int i = 0; i < 4; ++i) so[i] = abase + ((w + 1 + i) & 3) * mstride;
#pragma unroll
  for (int n = 0; n < WIN; ++n) a[n] = hld128(ws, lane * 16 + (n % KS) * 1024, so[n / KS]);
  PIN();
#pragma unroll
  for (int n = 0; n < NA; ++n) {
    const int i = n / KS, k = n % KS;
#pragma unroll
    for (int t = 0; t < 5; ++t) acc[t] = mfma(b[k * 5 + t], a[n % WIN], k == 0 ? f4{0.f, 0.f, 0.f, 0.f} : acc[t]);
    PIN();
    if (n + WIN < NA) a[n % WIN] = hld128(ws, lane * 16 + ((n + WIN) % KS) * 1024, so[(n + WIN) / KS]);
    else extra(n + WIN - NA);
    if (k == KS - 1 && i < 3) red_put(L, (w + 1 + i) & 3, i, lane, acc);
    PIN();
  }
  WGBAR();
  f4 p[15];
#pragma unroll
  for (int s = 0; s < 15; ++s) p[s] = *(LAS f4*)(L + (w * 15 + s) * 1024 + lane * 16);
  PIN();
#pragma unroll
  for (int t = 0; t < 5; ++t) acc[t] += (p[t] + p[5 + t]) + p[10 + t];
}

#define WAITV(n) asm volatile("s_waitcnt vmcnt(" #n ")" ::: "memory")
__global__ __launch_bounds__(256, 1) void fused(const void* __restrict__ gq, const void* __restrict__ gk,
                                                const void* __restrict__ gv, const void* __restrict__ gsk,
                                                const void* __restrict__ gssm, const void* __restrict__ gD,
                                                const void* __restrict__ gw1, const void* __restrict__ gw2,
                                                const void* __restrict__ gw3, const void* __restrict__ gw4,
                                                const void* __restrict__ gc, const void* __restrict__ gwm,
                                                void* __restrict__ gout, void* __restrict__ gws) {
  extern __shared__ __attribute__((aligned(16))) char smem[];
  LAS char* L = (LAS char*)smem;
  STAMP(0);
  const int tid = threadIdx.x, lane = tid & 63;
  const int w = __builtin_amdgcn_readfirstlane(tid >> 6);
  const int b = blockIdx.x, x = b & 7, j = b >> 3;
  const int tm = 4 * x + (j >> 3), tn = j & 7;
  const int rg = 2 * x + (j >> 4), cg = j & 15;
  const bool conv_wg = j < 20;
  const rsrc_t ws = mkrs(gws, O_END);
  const int g = lane >> 4, l15 = lane & 15;

  v2u kw[11], vw[8];
  v4u skw[2];
  const int prun = 4 * w + lane / 10, pquad = lane % 10;
  const int pT0 = 128 * (j >> 1) + 8 * prun, pC0 = 80 * x + 40 * (j & 1) + 4 * pquad;
  {
    const rsrc_t rk = mkrs(gk, SD2), rv = mkrs(gv, SD2), rs = mkrs(gsk, D_ * 8);
    const unsigned o = lane < 40 ? (unsigned)(pT0 * D_ + pC0) * 2 : OOB;
#pragma unroll
    for (int i = 0; i < 11; ++i) kw[i] = ld64(rk, o + (i - 3) * D_ * 2, 0);
#pragma unroll
    for (int i = 0; i < 8; ++i) vw[i] = ld64(rv, o + i * D_ * 2, 0);
    skw[0] = ld128(rs, pC0 * 8, 0);
    skw[1] = ld128(rs, pC0 * 8 + 16, 0);
  }
  const v2u ep = hld64(ws, O_EPOCH, 0);
  {
    unsigned long long pc;
    unsigned len;
    asm volatile("s_getpc_b64 %0\n.Lfused_pc:\n\ts_mov_b32 %1, .Lfused_end-.Lfused_pc" : "=s"(pc), "=s"(len));
    const unsigned pf = ld32(mkrs((const void*)pc, len), (((j & 1) * 4 + w) * 64 + lane) * 128, 0);
    asm volatile("" ::"v"(pf));
  }
  PIN();
  {
    f2 wt[4][2];
#pragma unroll
    for (int pr = 0; pr < 2; ++pr) {
      wt[0][pr] = f2{lo2f(skw[pr].x), lo2f(skw[pr].z)};
      wt[1][pr] = f2{hi2f(skw[pr].x), hi2f(skw[pr].z)};
      wt[2][pr] = f2{lo2f(skw[pr].y), lo2f(skw[pr].w)};
      wt[3][pr] = f2{hi2f(skw[pr].y), hi2f(skw[pr].w)};
    }
#pragma unroll
    for (int pr = 0; pr < 2; ++pr) {
      f2 kf[11], o[8];
#pragma unroll
      for (int i = 0; i < 11; ++i) kf[i] = up2(kw[i][pr]);
#pragma unroll
      for (int i = 0; i < 8; ++i)
        o[i] = (kf[i + 3] * wt[0][pr] + kf[i + 2] * wt[1][pr] + kf[i + 1] * wt[2][pr] + kf[i] * wt[3][pr]) * up2(vw[i][pr]);
      const v4u c0 = {pk2(o[7].x, o[6].x), pk2(o[5].x, o[4].x), pk2(o[3].x, o[2].x), pk2(o[1].x, o[0].x)};
      const v4u c1 = {pk2(o[7].y, o[6].y), pk2(o[5].y, o[4].y), pk2(o[3].y, o[2].y), pk2(o[1].y, o[0].y)};
      const unsigned so = lane < 40 ? (unsigned)((pC0 + 2 * pr) * S_ + pT0) * 2 : OOB;
      hst128(c0, ws, so, O_KVT);
      hst128(c1, ws, so + S_ * 2, O_KVT);
    }
  }
  STAMP(1);
  asm volatile("" ::: "memory");
  PIN();
  const int hw = ((j - 20) * 8 + x) * 4 + w;
  const int u0 = conv_wg ? 320 + (j * 8 + x) * 4 + w : (hw < 320 ? hw : 640 + hw);
  const Unit un = unit_desc(u0, gw1, gw2, gw3, gw4);
  v4u ru[7];
  const int ch = 80 * x + 4 * j + w;
  v4u kr[4][2];
  {
    const rsrc_t rK = mkrs((const char*)gssm + (size_t)(conv_wg ? ch : 0) * 4096, conv_wg ? 4096 : 0);
#pragma unroll
    for (int i = 0; i < 4; ++i) {
      kr[i][0] = ld128(rK, (unsigned)((i * 64 + lane - 1) * 16), 0);
      kr[i][1] = ld128(rK, (unsigned)((i * 64 + lane) * 16), 0);
    }
  }
  unit_load(un, lane, ru);
  const int rt0 = 64 * tm + 8 * tn + 2 * w;
  const int rcm = 80 * (lane >> 3) + 8 * (lane & 7), rco = 80 * (lane >> 3) + 64 + 2 * (lane & 7);
  v4u qm[2];
  unsigned qo[2];
  {
    const rsrc_t rq = mkrs(gq, SD2);
#pragma unroll
    for (int t = 0; t < 2; ++t) {
      qm[t] = ld128(rq, rcm * 2, (rt0 + t) * D_ * 2);
      qo[t] = ld32(rq, rco * 2, (rt0 + t) * D_ * 2);
    }
  }
  PIN();
  v2u tag;
  tag.x = __builtin_amdgcn_readfirstlane(ep.x) * 0x9E3779B1u + 0x7F4A7C15u;
  tag.y = (__builtin_amdgcn_readfirstlane(ep.y) ^ tag.x) * 0x85EBCA77u + 0x2545F491u;
  tag.x &= 0xffffff00u;
  WAITV(19);
  WGBAR();
  if (w == 0) hst64(tag, ws, lane == 0 ? O_FP + (x * 2 + (j & 1)) * 128 + (j >> 1) * 8 : OOB, 0);
  STAMP(2);

  v4u b1[25], b2[25];
  const unsigned sb1 = O_W1 + (tn * 100 + w * 25) * 1024, sb2 = O_W2 + (tn * 100 + w * 25) * 1024;
  if (conv_wg) {
    LAS char* T = L + w * WR;
    unit_store(un, lane, ru, T, ws);
#pragma unroll
    for (int i = 0; i < 4; ++i) {
      const unsigned d[8] = {kr[i][0].x, kr[i][0].y, kr[i][0].z, kr[i][0].w, kr[i][1].x, kr[i][1].y, kr[i][1].z, kr[i][1].w};
      unsigned a[7];
#pragma unroll
      for (int e = 0; e < 7; ++e) a[e] = __builtin_amdgcn_alignbit(d[e + 1], d[e], 16);
#pragma unroll
      for (int m = 0; m < 8; ++m) {
        const int h = (m + 1) >> 1;
        const v4u o = (m & 1) ? v4u{d[h], d[h + 1], d[h + 2], d[h + 3]} : v4u{a[h], a[h + 1], a[h + 2], a[h + 3]};
        *(LAS v4u*)(T + m * CS + (i * 64 + lane + 1) * 16) = o;
      }
    }
    if (lane < 8) *(LAS v4u*)(T + lane * CS) = v4u{0u, 0u, 0u, 0u};
    if (lane < 32) *(LAS v4u*)(T + UOFF + lane * 16) = v4u{0u, 0u, 0u, 0u};
    const unsigned dvb = ((const GAS unsigned short*)gD)[ch];
    STAMP(3);
    if (w == 0) poll64(ws, lane < 16 ? O_FP + (x * 2 + (j >= 10)) * 128 + lane * 8 : OOB, tag);
    WGBAR();
    STAMP(4);
    v4u ur[4];
#pragma unroll
    for (int i = 0; i < 4; ++i) ur[i] = hld128(ws, lane * 16 + i * 1024, O_KVT + ch * 4096);
    const v4u wf = hld128(ws, O_FW + x * 1024 + lane * 16, 0);
    const LAS char* pa = T + (l15 & 7) * CS + (3 - g + (l15 >> 3)) * 16;
    const LAS char* pv = T + UOFF + 512 + (l15 - 1) * 32 + g * 16;
    v4u fa[2][8], fv[2][8];
#pragma unroll
    for (int a = 0; a < 8; ++a) fa[0][a] = *(const LAS v4u*)(pa + 8 * a * 64);
    PIN();
#pragma unroll
    for (int i = 0; i < 4; ++i) *(LAS v4u*)(T + UOFF + 512 + i * 1024 + lane * 16) = ur[i];
    asm volatile("s_waitcnt lgkmcnt(0)" ::: "memory");
#pragma unroll
    for (int a = 0; a < 8; ++a) fv[0][a] = *(const LAS v4u*)(pv + a * 512);
    const bool wok = wflags_ok(wf, lane, tag);
    __builtin_amdgcn_fence(__ATOMIC_ACQUIRE, "wavefront");
    const unsigned boff = wok ? lane * 16 : OOB;
    STAMP(5);
    f4 acc[8];
    {
      const int jd = 8 * g - 9 - l15;
      v4u ad;
#pragma unroll
      for (int e = 0; e < 4; ++e) ad[e] = jd == 2 * e ? dvb : (jd == 2 * e + 1 ? dvb << 16 : 0u);
#pragma unroll
      for (int q = 0; q < 8; ++q) acc[q] = mfma(ad, fv[0][q], f4{0.f, 0.f, 0.f, 0.f});
    }
    PIN();
#pragma unroll
    for (int g0 = 0; g0 < 8; ++g0) {
      int n = 0;
#pragma unroll
      for (int a = 0; a < 8; ++a)
#pragma unroll
        for (int q = a; q < 8; ++q) {
          acc[q] = mfma(fa[g0 & 1][a], fv[g0 & 1][q - a], acc[q]);
          ++n;
          if ((n & 1) == 0) {
            const int e = n / 2 - 1;
            if (g0 < 7 && e < 16) {
              if (e < 8) fv[(g0 + 1) & 1][e] = *(const LAS v4u*)(pv + e * 512 - (g0 + 1) * 64);
              else fa[(g0 + 1) & 1][e - 8] = *(const LAS v4u*)(pa + (g0 + 1 + 8 * (e - 8)) * 64);
            }
            PIN();
          }
          if (n % 5 == 0) {
            const int i = g0 * 7 + n / 5 - 1;
            if (i < 25) b1[i] = hld128(ws, boff, sb1 + i * 1024);
            else if (i < 50) b2[i - 25] = hld128(ws, boff, sb2 + (i - 25) * 1024);
            PIN();
          }
        }
    }
    STAMP(6);
#pragma unroll
    for (int q = 0; q < 8; q += 2) {
      const auto s0 = __builtin_amdgcn_permlane16_swap(pk2(acc[q][0], acc[q][1]), pk2(acc[q + 1][0], acc[q + 1][1]), false, false);
      const auto s1 = __builtin_amdgcn_permlane16_swap(pk2(acc[q][2], acc[q][3]), pk2(acc[q + 1][2], acc[q + 1][3]), false, false);
      hst128(v4u{s0[0], s1[0], s0[1], s1[1]}, ws, (256 * (g & 1) + 16 * l15 + 8 * (g >> 1)) * 2 + q * 512, O_ZT + ch * 4096);
    }
    if (!wok) {
      pollw(ws, O_FW + x * 1024 + lane * 16, lane, tag);
#pragma unroll
      for (int i = 0; i < 25; ++i) b1[i] = hld128(ws, lane * 16, sb1 + i * 1024);
#pragma unroll
      for (int i = 0; i < 25; ++i) b2[i] = hld128(ws, lane * 16, sb2 + i * 1024);
    }
  } else {
    const int hb = (j - 20) * 8 + x;
    unit_store(un, lane, ru, L + w * WR, ws);
    if (j == 20 && w == 0) hst64(tag, ws, lane < 8 ? O_CNT + (lane * 8 + x) * 128 : OOB, 0);
    DRAIN();
    const rsrc_t rm = mkrs(gwm, D_ * 6 * D_ * 2);
    const int mc = tid % 5, mr = tid / 5;
    v4u wm[13];
    unsigned short cv[13];
    WGBAR();
    if (w == 0) hst64(tag, ws, lane < 8 ? O_FW + lane * 1024 + hb * 8 : OOB, 0);
    STAMP(3);
#pragma unroll
    for (int i = 0; i < 13; ++i) {
      const int row = mr + 51 * i;
      const bool ok = tid < 255 && row < D_;
      wm[i] = ld128(rm, ok ? (unsigned)(row * 6 * D_ + hb * 40 + mc * 8) * 2 : OOB, 0);
      cv[i] = ((const GAS unsigned short*)gc)[ok ? row : 0];
    }
    if (hb < 16) {
      const Unit u2 = unit_desc(1024 + hw, gw1, gw2, gw3, gw4);
      v4u r2[7];
      unit_load(u2, lane, r2);
      unit_store(u2, lane, r2, L + w * WR, ws);
    }
    f2 a[4] = {f2{0.f, 0.f}, f2{0.f, 0.f}, f2{0.f, 0.f}, f2{0.f, 0.f}};
#pragma unroll
    for (int i = 0; i < 13; ++i) {
      const float c = lo2f(cv[i]);
      const float s = rbf(c * __builtin_amdgcn_rcpf(1.f + __builtin_amdgcn_exp2f(-1.4426950408889634f * c)));
#pragma unroll
      for (int e = 0; e < 4; ++e) a[e] += up2(wm[i][e]) * s;
    }
    WGBAR();
    *(LAS f4*)(L + tid * 32) = f4{a[0].x, a[0].y, a[1].x, a[1].y};
    *(LAS f4*)(L + tid * 32 + 16) = f4{a[2].x, a[2].y, a[3].x, a[3].y};
    WGBAR();
    if (tid < 40) {
      float s = 0.f;
#pragma unroll
      for (int r = 0; r < 51; ++r) s += *(LAS float*)(L + r * 160 + tid * 4);
      const int n = hb * 40 + tid, v = n / D_, c = n % D_;
      s = rbf(s);
      if (v == 1 || v == 4) s = rbf(1.f + s);
      if (v == 5) {
        hst32(__builtin_bit_cast(unsigned, s), ws, c * 4, O_GB);
      } else {
        const int grp = c / 80, o = c % 80;
        const int ln = grp * 8 + (o < 64 ? o >> 3 : (o - 64) >> 1), e = v * 10 + (o < 64 ? o & 7 : 8 + (o & 1));
        __builtin_amdgcn_raw_buffer_store_b16((unsigned short)(__builtin_bit_cast(unsigned, s) >> 16), ws,
                                              (e >> 3) * 1024 + ln * 16 + (e & 7) * 2, O_MOD, 16);
      }
    }
    STAMP(4);
    pollw(ws, O_FW + x * 1024 + lane * 16, lane, tag);
    STAMP(5);
#pragma unroll
    for (int i = 0; i < 25; ++i) b1[i] = hld128(ws, lane * 16, sb1 + i * 1024);
#pragma unroll
    for (int i = 0; i < 25; ++i) b2[i] = hld128(ws, lane * 16, sb2 + i * 1024);
    STAMP(6);
  }
  DRAIN();
  WGBAR();
  STAMP(7);
  if (w == 0 && lane < 8)
    __hip_atomic_fetch_add((GAS unsigned*)((GAS char*)gws + O_CNT + (lane * 8 + x) * 128), 1u, __ATOMIC_RELAXED, __HIP_MEMORY_SCOPE_AGENT);
  f2 qv[10], n1[10];
  {
#pragma unroll
    for (int i = 0; i < 4; ++i) {
      qv[2 * i] = f2{lo2f(qm[0][i]), lo2f(qm[1][i])};
      qv[2 * i + 1] = f2{hi2f(qm[0][i]), hi2f(qm[1][i])};
    }
    qv[8] = f2{lo2f(qo[0]), lo2f(qo[1])};
    qv[9] = f2{hi2f(qo[0]), hi2f(qo[1])};
    f2 s = {0.f, 0.f}, ss = {0.f, 0.f};
#pragma unroll
    for (int i = 0; i < 10; ++i) { s += qv[i]; ss += qv[i] * qv[i]; }
    f2 mean, r;
    ln_scal(f2{wave_sum(s.x), wave_sum(s.y)}, f2{wave_sum(ss.x), wave_sum(ss.y)}, mean, r);
#pragma unroll
    for (int i = 0; i < 10; ++i) n1[i] = (qv[i] - mean) * r;
  }
  PIN();
  if (w == 0) {
    poll64(ws, lane < 8 ? O_CNT + (x * 8 + lane) * 128 : OOB, v2u{tag.x + 32, tag.y});
    if (b == 0) hst64(tag, ws, lane == 0 ? O_EPOCH : OOB, 0);
  }
  WGBAR();
  STAMP(8);

  v4u b3[35], b4[70];
  const unsigned sb3 = O_W3 + (cg * 140 + w * 35) * 1024, sb4 = O_W4 + (tn * 280 + w * 70) * 1024;
#define LB3(lo, hi) _Pragma("unroll") for (int i_ = lo; i_ < hi; ++i_) b3[i_] = hld128(ws, lane * 16, sb3 + i_ * 1024)
#define LB4(lo, hi) _Pragma("unroll") for (int i_ = lo; i_ < hi; ++i_) b4[i_] = hld128(ws, lane * 16, sb4 + i_ * 1024)
  {
    v4u zc[3];
#pragma unroll
    for (int i = 0; i < 3; ++i) zc[i] = hld128(ws, tid + 256 * i < D_ ? (tid + 256 * i) * 4096 : OOB, O_ZT + (64 * tm + 8 * tn) * 2);
    v4u mp[7];
#pragma unroll
    for (int i = 0; i < 7; ++i) mp[i] = hld128(ws, lane * 16 + i * 1024, O_MOD);
    LB3(0, 4);
    PIN();
#pragma unroll
    for (int i = 0; i < 3; ++i)
#pragma unroll
      for (int e = 0; e < 4; ++e) *(LAS unsigned*)(L + e * 3072 + (tid + 256 * i) * 4) = zc[i][e];
    WGBAR();
    const v4u z0 = *(LAS v4u*)(L + w * 3072 + rcm * 4), z1 = *(LAS v4u*)(L + w * 3072 + rcm * 4 + 16);
    const v2u z2 = *(LAS v2u*)(L + w * 3072 + rco * 4);
    LB3(4, 8);
    auto md = [&](int v, int i) { const int e = v * 10 + i; const unsigned u = mp[e >> 3][(e & 7) >> 1]; return (e & 1) ? hi2f(u) : lo2f(u); };
    f2 xv[10], s = {0.f, 0.f}, ss = {0.f, 0.f};
#pragma unroll
    for (int i = 0; i < 10; ++i) {
      const f2 z = up2(i < 4 ? z0[i & 3] : (i < 8 ? z1[i & 3] : z2[i & 1]));
      const f2 h = n1[i] * md(1, i) + md(0, i);
      xv[i] = qv[i] + h * z * md(2, i);
      s += xv[i];
      ss += xv[i] * xv[i];
    }
    const int sm = (5 * g + 2 * ((lane >> 3) & 1) + ((lane & 7) >> 2)) * 4 + (lane & 3), so = (5 * g + 4) * 4 + ((lane & 7) >> 1);
    LAS char* im = L + 16384 + sm * 128;
    LAS char* io = L + 16384 + so * 128 + 8 * ((lane >> 3) & 1) + 4 * (lane & 1);
    auto put = [&](const f2(&v)[10], int img) {
      *(LAS v4u*)(im + img * 10240 + (((2 * w) ^ (sm & 7)) << 4)) = v4u{pk2(v[0].x, v[1].x), pk2(v[2].x, v[3].x), pk2(v[4].x, v[5].x), pk2(v[6].x, v[7].x)};
      *(LAS v4u*)(im + img * 10240 + (((2 * w + 1) ^ (sm & 7)) << 4)) = v4u{pk2(v[0].y, v[1].y), pk2(v[2].y, v[3].y), pk2(v[4].y, v[5].y), pk2(v[6].y, v[7].y)};
      *(LAS unsigned*)(io + img * 10240 + (((2 * w) ^ (so & 7)) << 4)) = pk2(v[8].x, v[9].x);
      *(LAS unsigned*)(io + img * 10240 + (((2 * w + 1) ^ (so & 7)) << 4)) = pk2(v[8].y, v[9].y);
    };
    f2 mean, r;
    ln_scal(f2{wave_sum(s.x), wave_sum(s.y)}, f2{wave_sum(ss.x), wave_sum(ss.y)}, mean, r);
    put(xv, 2);
    LB3(8, 12);
    s = f2{0.f, 0.f};
    ss = f2{0.f, 0.f};
#pragma unroll
    for (int i = 0; i < 10; ++i) {
      xv[i] = (xv[i] - mean) * r * md(4, i) + md(3, i);
      s += xv[i];
      ss += xv[i] * xv[i];
    }
    ln_scal(f2{wave_sum(s.x), wave_sum(s.y)}, f2{wave_sum(ss.x), wave_sum(ss.y)}, mean, r);
    put(xv, 1);
#pragma unroll
    for (int i = 0; i < 10; ++i) xv[i] = (xv[i] - mean) * r;
    put(xv, 0);
    WGBAR();
    v4u oc[8];
#pragma unroll
    for (int i = 0; i < 8; ++i) oc[i] = *(LAS v4u*)(L + 16384 + (i * 256 + tid) * 16);
#pragma unroll
    for (int i = 0; i < 8; ++i) {
      const unsigned c = i * 256 + tid, img = c / 640, cc = c - img * 640, sl = cc >> 3, tk = (cc ^ sl) & 7;
      hst128(oc[i], ws, c < 1920 ? img * SD2 + (sl >> 2) * 1024 + (tk + 16 * (sl & 3)) * 16 : OOB,
             O_A1 + ((4 * tm + (tn >> 1)) * 20) * 1024 + (tn & 1) * 128);
    }
    asm volatile("" ::: "memory");
    PIN();
    LB3(12, 18);
    PIN();
  }
  STAMP(9);
  WAITV(11);
  WGBAR();
  if (w == 0) {
    hst64(tag, ws, lane == 0 ? O_FR + tm * 128 + tn * 8 : OOB, 0);
    poll64(ws, lane < 8 ? O_FR + tm * 128 + lane * 8 : OOB, tag);
  }
  WGBAR();
  STAMP(10);

  const unsigned own_s = ((4 * tm + w) * 20 + 5 * (tn >> 1)) * 1024;
  const unsigned own_m = 2 * (tn & 1) * 1024 + lane * 16, own_o = 4096 + lane * 16 + 8 * (tn & 1);
  {
    f4 acc[5];
    gemm_ks<5, 12>(ws, O_A1 + (4 * tm * 20 + 5 * w) * 1024, 20 * 1024, b1, L, w, lane, acc,
                   [&](int n) { b3[18 + n] = hld128(ws, lane * 16, sb3 + (18 + n) * 1024); });
    f2 v[10];
    unsigned o[10];
#pragma unroll
    for (int t = 0; t < 5; ++t) {
      v[2 * t] = f2{acc[t][0], acc[t][1]};
      v[2 * t + 1] = f2{acc[t][2], acc[t][3]};
    }
    gelu_pack<10>(v, o);
    const int sh = (tn >> 1) == 0 ? 1 : ((tn >> 1) == 1 ? -1 : ((tn >> 1) == 2 ? 2 : -2));
    const int t = 64 * tm + 16 * w + l15, t2 = t + sh;
    const unsigned d = (t2 >= 0 && t2 < S_) ? ((t2 >> 4) * 20 + 5 * (tn >> 1)) * 1024 + ((t2 & 15) + 16 * g) * 16 : OOB;
    hst128(v4u{o[0], o[1], o[2], o[3]}, ws, d + 2 * (tn & 1) * 1024, O_H1);
    hst128(v4u{o[4], o[5], o[6], o[7]}, ws, d + 2 * (tn & 1) * 1024 + 1024, O_H1);
    hst64(v2u{o[8], o[9]}, ws, d + 4096 + 8 * (tn & 1), O_H1);
    if ((tm == 0 && w == 0 && sh > 0) || (tm == 31 && w == 3 && sh < 0)) {
      const bool z = t - sh < 0 || t - sh >= S_;
      hst128(v4u{0u, 0u, 0u, 0u}, ws, z ? own_m : OOB, O_H1 + own_s);
      hst128(v4u{0u, 0u, 0u, 0u}, ws, z ? own_m + 1024 : OOB, O_H1 + own_s);
      hst64(v2u{0u, 0u}, ws, z ? own_o : OOB, O_H1 + own_s);
    }
    asm volatile("" ::: "memory");
    PIN();
    LB3(30, 35);
    LB4(0, 3);
    PIN();
    STAMP(11);
    WAITV(8);
    WGBAR();
    if (w == 0) {
      const int nb = tm + (sh > 0 ? 1 : -1);
      unsigned fo = OOB;
      if (lane == 0) fo = O_F1 + tm * 128 + tn * 8;
      if (lane == 1 && nb >= 0 && nb < 32) fo = O_F1 + nb * 128 + 64 + tn * 8;
      hst64(tag, ws, fo, 0);
      const bool up = ((lane >> 1) & 1) == 0;
      const bool need = lane < 8 || (lane < 16 && (up ? tm > 0 : tm < 31));
      poll64(ws, need ? O_F1 + tm * 128 + lane * 8 : OOB, tag);
    }
    WGBAR();
    STAMP(12);
  }
  v4u xr[2];
  v2u xro;
  unsigned x1p[10];
  {
    v4u hr[2];
    v2u hro;
    f4 acc[5];
    gemm_ks<5, 12>(ws, O_H1 + (4 * tm * 20 + 5 * w) * 1024, 20 * 1024, b2, L, w, lane, acc, [&](int n) {
      if (n == 0) hr[0] = hld128(ws, own_m, O_HP + own_s);
      else if (n == 1) hr[1] = hld128(ws, own_m + 1024, O_HP + own_s);
      else if (n == 2) hro = hld64(ws, own_o, O_HP + own_s);
      else if (n == 3) xr[0] = hld128(ws, own_m, O_X + own_s);
      else if (n == 4) xr[1] = hld128(ws, own_m + 1024, O_X + own_s);
      else if (n == 5) xro = hld64(ws, own_o, O_X + own_s);
      else b4[n - 3] = hld128(ws, lane * 16, sb4 + (n - 3) * 1024);
    });
    f2 s = {0.f, 0.f}, ss = {0.f, 0.f};
#pragma unroll
    for (int t = 0; t < 5; ++t) {
      const unsigned h0 = t < 4 ? hr[t >> 1][2 * (t & 1)] : hro.x, h1 = t < 4 ? hr[t >> 1][2 * (t & 1) + 1] : hro.y;
      const f2 p = f2{acc[t][0], acc[t][1]} + up2(h0), q = f2{acc[t][2], acc[t][3]} + up2(h1);
      x1p[2 * t] = pk2(p.x, p.y);
      x1p[2 * t + 1] = pk2(q.x, q.y);
    }
    hst128(v4u{x1p[0], x1p[1], x1p[2], x1p[3]}, ws, own_m, O_X1 + own_s);
    hst128(v4u{x1p[4], x1p[5], x1p[6], x1p[7]}, ws, own_m + 1024, O_X1 + own_s);
    hst64(v2u{x1p[8], x1p[9]}, ws, own_o, O_X1 + own_s);
#pragma unroll
    for (int i = 0; i < 10; ++i) {
      const f2 pr = up2(x1p[i]);
      s += pr;
      ss += pr * pr;
    }
    const float s1 = xrow_sum(s.x + s.y), s2 = xrow_sum(ss.x + ss.y);
    hst64(v2u{__builtin_bit_cast(unsigned, s1), __builtin_bit_cast(unsigned, s2)}, ws, lane < 16 ? lane * 8 : OOB,
          O_STAT + ((4 * tm + w) * 8 + tn) * 128);
    asm volatile("" ::: "memory");
    PIN();
    LB4(9, 15);
    PIN();
    STAMP(13);
    WAITV(6);
    WGBAR();
    if (w == 0) hst64(tag, ws, lane == 0 ? O_F2 + (tm >> 1) * 128 + ((tm & 1) * 8 + tn) * 8 : OOB, 0);
    const v4u ones = {0x3f803f80u, 0x3f803f80u, 0x3f803f80u, 0x3f803f80u};
    f4 c[7];
#pragma unroll
    for (int i = 0; i < 35; ++i) {
      *(LAS v4u*)(L + (w * 35 + i) * 1024 + lane * 16) = b3[i];
      c[i % 7] = mfma(b3[i], ones, i < 7 ? f4{0.f, 0.f, 0.f, 0.f} : c[i % 7]);
      PIN();
    }
    if (l15 == 0) {
#pragma unroll
      for (int t = 0; t < 7; ++t) *(LAS f4*)(L + CSUM_OFF + ((w * 7 + t) * 4 + g) * 16) = c[t];
    }
    if (w == 0) poll64(ws, lane < 16 ? O_F2 + rg * 128 + lane * 8 : OOB, tag);
    WGBAR();
    STAMP(14);
  }
  {
    constexpr int W3 = 6;
    v4u a[W3][2];
    const unsigned sa = O_X1 + (8 * rg + 2 * w) * 20 * 1024;
#pragma unroll
    for (int k = 0; k < W3; ++k)
#pragma unroll
      for (int i = 0; i < 2; ++i) a[k][i] = hld128(ws, lane * 16, sa + (i * 20 + k) * 1024);
    v4u bf[2][7];
#pragma unroll
    for (int t = 0; t < 7; ++t) bf[0][t] = *(LAS v4u*)(L + t * 1024 + lane * 16);
    PIN();
    v2u sv[2][2];
    f4 acc[2][7];
#pragma unroll
    for (int k = 0; k < 20; ++k) {
#pragma unroll
      for (int t = 0; t < 7; ++t) {
        acc[0][t] = mfma(bf[k & 1][t], a[k % W3][0], k == 0 ? f4{0.f, 0.f, 0.f, 0.f} : acc[0][t]);
        acc[1][t] = mfma(bf[k & 1][t], a[k % W3][1], k == 0 ? f4{0.f, 0.f, 0.f, 0.f} : acc[1][t]);
        if (k < 19) bf[(k + 1) & 1][t] = *(LAS v4u*)(L + ((k + 1) * 7 + t) * 1024 + lane * 16);
        if (t == 3) b4[15 + k] = hld128(ws, lane * 16, sb4 + (15 + k) * 1024);
        PIN();
      }
      if (k + W3 < 20) {
#pragma unroll
        for (int i = 0; i < 2; ++i) a[k % W3][i] = hld128(ws, lane * 16, sa + (i * 20 + k + W3) * 1024);
      } else if (k + W3 < 22) {
        const int i = k + W3 - 20;
        sv[i][0] = hld64(ws, l15 * 8 + g * 256, O_STAT + (8 * rg + 2 * w + i) * 1024);
        sv[i][1] = hld64(ws, l15 * 8 + g * 256 + 128, O_STAT + (8 * rg + 2 * w + i) * 1024);
      } else {
        const int i = 35 + 2 * (k + W3 - 22);
        b4[i] = hld128(ws, lane * 16, sb4 + i * 1024);
        b4[i + 1] = hld128(ws, lane * 16, sb4 + (i + 1) * 1024);
      }
      PIN();
    }
    STAMP(15);
    f4 cs[7];
#pragma unroll
    for (int t = 0; t < 7; ++t) {
      cs[t] = *(LAS f4*)(L + CSUM_OFF + (t * 4 + g) * 16);
#pragma unroll
      for (int v = 1; v < 4; ++v) cs[t] += *(LAS f4*)(L + CSUM_OFF + ((v * 7 + t) * 4 + g) * 16);
    }
    f2 mean, r;
    {
      const f2 p0 = __builtin_bit_cast(f2, sv[0][0]) + __builtin_bit_cast(f2, sv[0][1]);
      const f2 p1 = __builtin_bit_cast(f2, sv[1][0]) + __builtin_bit_cast(f2, sv[1][1]);
      ln_scal(f2{xrow_sum(p0.x), xrow_sum(p1.x)}, f2{xrow_sum(p0.y), xrow_sum(p1.y)}, mean, r);
    }
#pragma unroll
    for (int i = 0; i < 2; ++i) {
      const float ri = r[i], mr = -mean[i] * r[i];
      f2 v[14];
      unsigned o[14];
#pragma unroll
      for (int t = 0; t < 7; ++t) {
        v[2 * t] = f2{acc[i][t][0], acc[i][t][1]} * ri + f2{cs[t][0], cs[t][1]} * mr;
        v[2 * t + 1] = f2{acc[i][t][2], acc[i][t][3]} * ri + f2{cs[t][2], cs[t][3]} * mr;
      }
      PIN();
      if (i == 0) { LB4(43, 53); } else { LB4(53, 62); }
      PIN();
      gelu_pack<14>(v, o);
      const unsigned hs = O_H + ((8 * rg + 2 * w + i) * 56 + 7 * (cg >> 1)) * 1024;
#pragma unroll
      for (int e = 0; e < 3; ++e)
        hst128(v4u{o[4 * e], o[4 * e + 1], o[4 * e + 2], o[4 * e + 3]}, ws, (3 * (cg & 1) + e) * 1024 + lane * 16, hs);
      hst64(v2u{o[12], o[13]}, ws, 6144 + lane * 16 + 8 * (cg & 1), hs);
    }
    asm volatile("" ::: "memory");
    PIN();
    LB4(62, 70);
    PIN();
    STAMP(16);
    WAITV(8);
    WGBAR();
    if (w == 0) {
      hst64(tag, ws, lane == 0 ? O_F3 + rg * 128 + cg * 8 : OOB, 0);
      poll64(ws, lane < 16 ? O_F3 + (tm >> 1) * 128 + lane * 8 : OOB, tag);
    }
    WGBAR();
    STAMP(17);
  }
  {
    v4u gb[5];
    f4 acc[5];
    gemm_ks<14, 14>(ws, O_H + (4 * tm * 56 + 14 * w) * 1024, 56 * 1024, b4, L, w, lane, acc, [&](int n) {
      if (n < 4) gb[n] = hld128(ws, (80 * tn + 32 * (n >> 1) + 8 * g + 4 * (n & 1)) * 4, O_GB);
      else if (n == 4) gb[4] = hld128(ws, (80 * tn + 64 + 4 * g) * 4, O_GB);
    });
    unsigned o[10];
#pragma unroll
    for (int t = 0; t < 5; ++t) {
      const unsigned x0 = t < 4 ? xr[t >> 1][2 * (t & 1)] : xro.x, x1 = t < 4 ? xr[t >> 1][2 * (t & 1) + 1] : xro.y;
      const f4 gv4 = __builtin_bit_cast(f4, gb[t]);
      const f2 p = (f2{acc[t][0], acc[t][1]} + up2(x1p[2 * t])) * f2{gv4[0], gv4[1]} + up2(x0);
      const f2 q = (f2{acc[t][2], acc[t][3]} + up2(x1p[2 * t + 1])) * f2{gv4[2], gv4[3]} + up2(x1);
      o[2 * t] = pk2(p.x, p.y);
      o[2 * t + 1] = pk2(q.x, q.y);
    }
    const rsrc_t ro = mkrs(gout, SD2);
    const unsigned d = ((64 * tm + 16 * w + l15) * D_ + 80 * tn) * 2;
    __builtin_amdgcn_raw_buffer_store_b128(v4u{o[0], o[1], o[2], o[3]}, ro, d + 16 * g, 0, 0);
    __builtin_amdgcn_raw_buffer_store_b128(v4u{o[4], o[5], o[6], o[7]}, ro, d + 64 + 16 * g, 0, 0);
    __builtin_amdgcn_raw_buffer_store_b64(v2u{o[8], o[9]}, ro, d + 128 + 8 * g, 0, 0);
  }
  asm volatile(".Lfused_end:" ::: "memory");
}

extern "C" void kernel_launch(void* const* d_in, const int* in_sizes, int n_in, void* d_out, int out_size,
                              void* d_ws, size_t ws_size, hipStream_t stream) {
  (void)hipFuncSetAttribute((const void*)fused, hipFuncAttributeMaxDynamicSharedMemorySize, LDS_BYTES);
  fused<<<256, 256, LDS_BYTES, stream>>>(d_in[0], d_in[1], d_in[2], d_in[3], d_in[4], d_in[5], d_in[7], d_in[8], d_in[9],
                                         d_in[10], d_in[11], d_in[12], d_out, d_ws);
}
```

```cpp
#include <hip/hip_runtime.h>
#include <stdint.h>

typedef unsigned v4u __attribute__((ext_vector_type(4)));
typedef unsigned v2u __attribute__((ext_vector_type(2)));
typedef float f4 __attribute__((ext_vector_type(4)));
typedef float f2 __attribute__((ext_vector_type(2)));
typedef short s8v __attribute__((ext_vector_type(8)));
typedef short s4v __attribute__((ext_vector_type(4)));
typedef __bf16 bf2 __attribute__((ext_vector_type(2)));
typedef __bf16 bf8 __attribute__((ext_vector_type(8)));
typedef __amdgpu_buffer_rsrc_t rsrc_t;
#define LAS __attribute__((address_space(3)))
#define GAS __attribute__((address_space(1)))
#define DI __device__ __forceinline__

static constexpr int S_ = 2048, D_ = 640, F_ = 1728;
static constexpr unsigned OOB = 0x80000000u;

static constexpr unsigned O_EPOCH = 0;
static constexpr unsigned O_FP = 1024;
static constexpr unsigned O_FR = O_FP + 16 * 128;
static constexpr unsigned O_F1 = O_FR + 32 * 128;
static constexpr unsigned O_F2 = O_F1 + 32 * 128;
static constexpr unsigned O_F3 = O_F2 + 16 * 128;
static constexpr unsigned O_CNT = O_F3 + 16 * 128;
static constexpr unsigned O_FW = O_CNT + 64 * 128;
static constexpr unsigned O_MOD = 65536;
static constexpr unsigned O_STAT = O_MOD + 16384;
static constexpr unsigned O_KVT = O_STAT + 131072;
static constexpr unsigned SD2 = S_ * D_ * 2;
static constexpr unsigned O_ZT = O_KVT + SD2;
static constexpr unsigned O_A1 = O_ZT + SD2;
static constexpr unsigned O_HP = O_A1 + SD2;
static constexpr unsigned O_X = O_HP + SD2;
static constexpr unsigned O_H1 = O_X + SD2;
static constexpr unsigned O_X1 = O_H1 + SD2;
static constexpr unsigned O_H = O_X1 + SD2;
static constexpr unsigned O_W1 = O_H + 128 * 56 * 1024;
static constexpr unsigned O_W2 = O_W1 + 800 * 1024;
static constexpr unsigned O_W3 = O_W2 + 800 * 1024;
static constexpr unsigned O_W4 = O_W3 + 2240 * 1024;
static constexpr unsigned O_END = O_W4 + 2240 * 1024;
static constexpr unsigned O_DBG = O_END;

static constexpr int CS = 4160;
static constexpr int UOFF = 8 * CS;
static constexpr int WR = 8 * CS + 4608;
static constexpr int LDS_BYTES = 160 * 1024;
static constexpr int RED_OFF = 100 * 1024;
static constexpr int CSUM_OFF = 140 * 1024;
static constexpr unsigned SPIN_CAP = 200000;

DI float lo2f(unsigned w) { return __builtin_bit_cast(float, w << 16); }
DI float hi2f(unsigned w) { return __builtin_bit_cast(float, w & 0xffff0000u); }
DI f2 up2(unsigned w) { return f2{lo2f(w), hi2f(w)}; }
DI unsigned pk2(float a, float b) { return __builtin_bit_cast(unsigned, __builtin_convertvector(f2{a, b}, bf2)); }
DI f4 mfma(v4u a, v4u b, f4 c) {
  return __builtin_amdgcn_mfma_f32_16x16x32_bf16(__builtin_bit_cast(bf8, a), __builtin_bit_cast(bf8, b), c, 0, 0, 0);
}
DI void mfma_v(f4& c, v4u a, v4u b) { asm volatile("v_mfma_f32_16x16x32_bf16 %0, %1, %2, %0" : "+v"(c) : "v"(a), "v"(b)); }
#define MFENCE() asm volatile("s_nop 7\n\ts_nop 1" ::: "memory")
DI rsrc_t mkrs(const void* p, unsigned bytes) { return __builtin_amdgcn_make_buffer_rsrc((void*)p, 0, bytes, 0x00020000); }
#define INAUX 2
DI v4u ld128(rsrc_t r, unsigned v, unsigned s) { return __builtin_amdgcn_raw_buffer_load_b128(r, v, s, INAUX); }
DI v2u ld64(rsrc_t r, unsigned v, unsigned s) { return __builtin_amdgcn_raw_buffer_load_b64(r, v, s, INAUX); }
DI unsigned ld32(rsrc_t r, unsigned v, unsigned s) { return __builtin_amdgcn_raw_buffer_load_b32(r, v, s, INAUX); }
DI v4u hld128(rsrc_t r, unsigned v, unsigned s) { return __builtin_amdgcn_raw_buffer_load_b128(r, v, s, 16); }
DI v2u hld64(rsrc_t r, unsigned v, unsigned s) { return __builtin_amdgcn_raw_buffer_load_b64(r, v, s, 16); }
DI void hst128(v4u x, rsrc_t r, unsigned v, unsigned s) { __builtin_amdgcn_raw_buffer_store_b128(x, r, v + s, 0, 16); }
DI void hst64(v2u x, rsrc_t r, unsigned v, unsigned s) { __builtin_amdgcn_raw_buffer_store_b64(x, r, v, s, 16); }
DI void hst32(unsigned x, rsrc_t r, unsigned v, unsigned s) { __builtin_amdgcn_raw_buffer_store_b32(x, r, v, s, 16); }
#define DRAIN() do { asm volatile("s_waitcnt vmcnt(0)" ::: "memory"); __builtin_amdgcn_s_waitcnt(0x0F70); } while (0)
#define WGBAR() asm volatile("s_waitcnt lgkmcnt(0)\n\ts_barrier" ::: "memory")
#define PIN() __builtin_amdgcn_sched_barrier(0)

template <int CTRL> DI float dpp(float x) {
  return __builtin_bit_cast(float, __builtin_amdgcn_mov_dpp(__builtin_bit_cast(int, x), CTRL, 0xf, 0xf, true));
}
DI float xrow_sum(float x) {
  auto s = __builtin_amdgcn_permlane16_swap(__builtin_bit_cast(unsigned, x), __builtin_bit_cast(unsigned, x), false, false);
  x = __builtin_bit_cast(float, (unsigned)s[0]) + __builtin_bit_cast(float, (unsigned)s[1]);
  auto t = __builtin_amdgcn_permlane32_swap(__builtin_bit_cast(unsigned, x), __builtin_bit_cast(unsigned, x), false, false);
  return __builtin_bit_cast(float, (unsigned)t[0]) + __builtin_bit_cast(float, (unsigned)t[1]);
}
DI float wave_sum(float v) {
  v += dpp<0xB1>(v);
  v += dpp<0x4E>(v);
  v += dpp<0x141>(v);
  v += dpp<0x128>(v);
  return xrow_sum(v);
}
DI void poll64(rsrc_t ws, unsigned voff, v2u want) {
  const bool idle = voff == OOB;
  for (unsigned spin = 0; spin < SPIN_CAP; ++spin) {
    asm volatile("" ::: "memory");
    const v2u v = hld64(ws, voff, 0);
    if (__all(idle || (v.x == want.x && v.y == want.y))) break;
  }
  __builtin_amdgcn_fence(__ATOMIC_ACQUIRE, "wavefront");
}

DI bool wflags_ok(v4u a, int lane, v2u want) {
  return __all(lane >= 48 || (a.x == want.x && a.y == want.y && a.z == want.x && a.w == want.y));
}
DI void pollw(rsrc_t ws, unsigned voff, int lane, v2u want) {
  for (unsigned spin = 0; spin < SPIN_CAP; ++spin) {
    asm volatile("" ::: "memory");
    if (wflags_ok(hld128(ws, voff, 0), lane, want)) break;
    __builtin_amdgcn_s_sleep(4);
  }
  __builtin_amdgcn_fence(__ATOMIC_ACQUIRE, "wavefront");
}
DI float rbf(float x) { return lo2f(pk2(x, 0.f)); }
DI void ln_scal(f2 s, f2 ss, f2& mean, f2& r, f2* pden = nullptr) {
  const f2 m = s * (1.f / D_);
  const f2 var = ss * (1.f / D_) - m * m;
  const f2 v = up2(pk2(fmaxf(var.x, 0.f), fmaxf(var.y, 0.f))) + 1e-5f;
  const f2 vr = up2(pk2(v.x, v.y));
  const f2 den = up2(pk2(__builtin_amdgcn_sqrtf(vr.x), __builtin_amdgcn_sqrtf(vr.y)));
  mean = up2(pk2(m.x, m.y));
  if (pden) *pden = den;
  r = f2{__builtin_amdgcn_rcpf(den.x), __builtin_amdgcn_rcpf(den.y)};
}
template <int N, class Mid> DI void gelu_pack(const f2 (&x)[N], float r, float den, unsigned* o, Mid mid) {
  const float c0 = -2.f * 0.7978845608028654f * 1.4426950408889634f * r, c1 = c0 * 0.044715f * r * r;
  f2 a[N];
#pragma unroll
  for (int i = 0; i < N; ++i) a[i] = x[i] * x[i] * c1 + c0;
  mid(0);
  PIN();
#pragma unroll
  for (int i = 0; i < N; ++i) a[i] *= x[i];
  mid(1);
  PIN();
#pragma unroll
  for (int i = 0; i < N; ++i) a[i] = f2{__builtin_amdgcn_exp2f(a[i].x), __builtin_amdgcn_exp2f(a[i].y)};
  mid(2);
  PIN();
#pragma unroll
  for (int i = 0; i < N; ++i) a[i] = a[i] * den + den;
  PIN();
#pragma unroll
  for (int i = 0; i < N; ++i) a[i] = f2{__builtin_amdgcn_rcpf(a[i].x), __builtin_amdgcn_rcpf(a[i].y)};
  mid(3);
  PIN();
#pragma unroll
  for (int i = 0; i < N; ++i) a[i] *= x[i];
  PIN();
#pragma unroll
  for (int i = 0; i < N; ++i) o[i] = pk2(a[i].x, a[i].y);
}

#define STAMP(i)
#define STARG
#define STPASS(n)

struct Job { rsrc_t rs; unsigned pitch, src, dst, wide; };
static constexpr int RT_PITCH = 912, RT_BYTES = 32 * RT_PITCH;
DI Job job_desc(int job, int w, const void* w1, const void* w2, const void* w3, const void* w4) {
  Job t;
  const bool is4 = job >= 160, is3 = !is4 && job >= 80;
  const int jj = job - (is4 ? 160 : (is3 ? 80 : (job >= 40 ? 40 : 0)));
  const int gq = is3 ? jj & 3 : jj & 1, ks = is3 ? jj >> 2 : jj >> 1;
  t.wide = is3 ? (gq == 3 ? 2 : 1) : 0;
  t.pitch = is3 ? F_ * 2 : D_ * 2;
  t.rs = mkrs(is4 ? w4 : (is3 ? w3 : (job >= 40 ? w2 : w1)), (is3 || is4) ? D_ * F_ * 2 : D_ * D_ * 2);
  t.src = (32 * ks + 8 * w) * t.pitch + gq * (is3 ? 896 : 640);
  t.dst = (is4 ? O_W4 : (is3 ? O_W3 : (job >= 40 ? O_W2 : O_W1))) + ((gq * 4 + w) * (is4 ? 56 : 20) + ks) * (is3 ? 7168 : 5120);
  return t;
}
DI void job_load(const Job& t, int lane, v4u (&r)[7]) {
  const unsigned nc = t.wide ? 56 : 40, magic = t.wide ? 1171u : 1639u, clim = t.wide == 2 ? 48 : 64;
#pragma unroll
  for (int i = 0; i < 7; ++i) {
    const unsigned c = i * 64 + lane, row = (c * magic) >> 16, col = c - row * nc;
    r[i] = ld128(t.rs, (row < 8 && col < clim) ? row * t.pitch + col * 16 + t.src : OOB, 0);
  }
}
DI void job_put(const Job& t, int w, int lane, const v4u (&r)[7], LAS char* L) {
  const unsigned nc = t.wide ? 56 : 40, magic = t.wide ? 1171u : 1639u;
#pragma unroll
  for (int i = 0; i < 7; ++i) {
    const unsigned c = i * 64 + lane, row = (c * magic) >> 16, col = c - row * nc;
    if (i < 5 || t.wide) *(LAS v4u*)(L + (8 * w + row) * RT_PITCH + col * 16) = r[i];
  }
}
DI void job_emit(const Job& t, int w, int lane, LAS char* L, rsrc_t ws) {
  const int g = lane >> 4, q = (lane & 15) >> 2, p = lane & 3;
  LAS char* lo = L + (8 * g + q) * RT_PITCH + w * (t.wide ? 224 : 160);
  auto frag = [&](unsigned cb, unsigned f) {
    const v2u a = __builtin_bit_cast(v2u, __builtin_amdgcn_ds_read_tr16_b64_v4i16((LAS s4v*)(lo + cb)));
    const v2u c = __builtin_bit_cast(v2u, __builtin_amdgcn_ds_read_tr16_b64_v4i16((LAS s4v*)(lo + cb + 4 * RT_PITCH)));
    hst128(v4u{a.x, a.y, c.x, c.y}, ws, lane * 16 + f * 1024, t.dst);
  };
  const unsigned pb = 32 * (w & 1) + 16 * p;
#pragma unroll
  for (int f = 0; f < 4; ++f) frag(pb + 64 * (f >> 1) + 8 * (f & 1), f);
  if (t.wide) {
    frag(pb + 128, 4);
    frag(pb + 136, 5);
    frag(((w & 1) ? 0 : 192) + 8 * p, 6);
  } else {
    frag(((w & 1) ? 0 : 128) + 8 * p, 4);
  }
}

template <int KS, int WIN, class Extra, class Tail>
DI void gemm_ks(rsrc_t ws, unsigned abase, unsigned mstride, const v4u* b, LAS char* L, int w, int lane, f4 (&acc)[5], Extra extra, Tail tail STARG) {
  constexpr int NA = 4 * KS, RING = WIN + 1;
  v4u a[RING];
  unsigned so[4];
#pragma unroll
  for (int i = 0; i < 4; ++i) so[i] = abase + ((w + 1 + i) & 3) * mstride;
#pragma unroll
  for (int n = 0; n < WIN; ++n) {
    a[n] = hld128(ws, lane * 16 + (n % KS) * 1024, so[n / KS]);
    PIN();
  }
  f4 prev[5];
#pragma unroll
  for (int n = 0; n < NA; ++n) {
    const int i = n / KS, k = n % KS;
#pragma unroll
    for (int t = 0; t < 5; ++t) {
      acc[t] = mfma(b[k * 5 + t], a[n % RING], k == 0 ? f4{0.f, 0.f, 0.f, 0.f} : acc[t]);
      if (t == 1) {
        if (n + WIN < NA) a[(n + WIN) % RING] = hld128(ws, lane * 16 + ((n + WIN) % KS) * 1024, so[(n + WIN) / KS]);
        else extra(n + WIN - NA);
      }
      if (k == 0 && i > 0) *(LAS f4*)(L + RED_OFF + ((((w + i) & 3) * 3 + i - 1) * 5 + t) * 1024 + lane * 16) = prev[t];
      if (n < NA - 1) PIN();
    }
    if (n == 0) STAMP(sb);
    if (k == KS - 1) {
#pragma unroll
      for (int t = 0; t < 5; ++t) prev[t] = acc[t];
    }
  }
  STAMP(sb + 1);
  WGBAR();
  STAMP(sb + 2);
  f4 p[15];
#pragma unroll
  for (int s = 0; s < 15; ++s) p[s] = *(LAS f4*)(L + RED_OFF + (w * 15 + s) * 1024 + lane * 16);
  tail();
  PIN();
#pragma unroll
  for (int t = 0; t < 5; ++t) acc[t] += (p[t] + p[5 + t]) + p[10 + t];
}

#define YOUNG() do { asm volatile("; YOUNG" ::: "memory"); PIN(); } while (0)
#define WAITV(n) asm volatile("s_waitcnt vmcnt(" #n ") ; WAITV" ::: "memory")
__global__ __launch_bounds__(256, 1) void fused(const void*, const void*, const void*, const void*, const void*, const void*,
                                                const void*, const void*, const void*, const void*, const void*, const void*,
                                                void*, void*) {
  extern __shared__ __attribute__((aligned(16))) char smem[];
  LAS char* L = (LAS char*)smem;
  STAMP(0);
  unsigned pf = 0;
  if (blockIdx.x >= 160) {
    unsigned long long pc;
    unsigned off = (((blockIdx.x >> 3) & 1) * 256 + threadIdx.x) * 128;
    asm volatile("s_getpc_b64 %1\n.Lfused_pc:\n\tv_min_u32 %2, .Lfused_end-.Lfused_pc, %2\n\tglobal_load_dword %0, %2, %1"
                 : "=&v"(pf), "=&s"(pc), "+v"(off));
  }
  PIN();
  typedef void* const __attribute__((address_space(4))) * karg_t;
  const karg_t ka = (karg_t)__builtin_amdgcn_kernarg_segment_ptr();
  void *gq = ka[0], *gk = ka[1], *gv = ka[2], *gsk = ka[3], *gssm = ka[4], *gD = ka[5], *gw1 = ka[6], *gw2 = ka[7], *gw3 = ka[8],
       *gw4 = ka[9], *gc = ka[10], *gwm = ka[11], *gout = ka[12], *gws = ka[13];
  asm volatile("" ::"s"(gq), "s"(gk), "s"(gv), "s"(gsk), "s"(gssm), "s"(gD), "s"(gw1), "s"(gw2), "s"(gw3), "s"(gw4),
               "s"(gc), "s"(gwm), "s"(gout), "s"(gws));
  const int tid = threadIdx.x, lane = tid & 63;
  const int w = __builtin_amdgcn_readfirstlane(tid >> 6);
  const int b = blockIdx.x, x = b & 7, j = b >> 3;
  const int tm = 4 * x + (j >> 3), tn = j & 7;
  const int rg = 2 * x + (j >> 4), cg = j & 15;
  const bool conv_wg = j < 20;
  const rsrc_t ws = mkrs(gws, O_END);
  const int g = lane >> 4, l15 = lane & 15;

  const int rt0 = 64 * tm + 8 * tn + 2 * w;
  const int rcm = 8 * lane, rco = 512 + 2 * lane;
  v4u qm[2];
  unsigned qo[2];
  auto load_q = [&]() {
    const rsrc_t rq = mkrs(gq, SD2);
#pragma unroll
    for (int t = 0; t < 2; ++t) {
      qm[t] = ld128(rq, rcm * 2, (rt0 + t) * D_ * 2);
      qo[t] = ld32(rq, rco * 2, (rt0 + t) * D_ * 2);
    }
  };
  v2u tag;
  auto make_tag = [&](v2u ep) {
    tag.x = __builtin_amdgcn_readfirstlane(ep.x) * 0x9E3779B1u + 0x7F4A7C15u;
    tag.y = (__builtin_amdgcn_readfirstlane(ep.y) ^ tag.x) * 0x85EBCA77u + 0x2545F491u;
    tag.x &= 0xffffff00u;
  };
  v4u b1[25], b2[25];
  const unsigned sb1 = O_W1 + (tn * 100 + w * 25) * 1024, sb2 = O_W2 + (tn * 100 + w * 25) * 1024;
  auto zero_h1 = [&](int i) {
    hst128(v4u{0u, 0u, 0u, 0u}, ws, (i < 2 || tid < 128) ? tid * 16 : OOB, O_H1 + b * 10240 + i * 4096);
  };
  bool wok = false;
  if (conv_wg) {
    const int cgp = j < 16 ? x : 8 + (x >> 2), ci = j < 16 ? j : (x & 3) * 4 + (j - 16);
    const int pT0 = 128 * ci + 32 * w + 8 * ((lane >> 3) & 3) + 4 * (lane >> 5), pC0 = 64 * cgp + 8 * (lane & 7);
    const int ch = 64 * cgp + 4 * ci + w;
    v4u kw[7], vw[4], skw[4], kr[4];
    {
      const rsrc_t rk = mkrs(gk, SD2), rv = mkrs(gv, SD2), rs = mkrs(gsk, D_ * 8);
      const unsigned o = (unsigned)(pT0 * D_ + pC0) * 2;
#pragma unroll
      for (int i = 0; i < 7; ++i) kw[i] = ld128(rk, o + (i - 3) * D_ * 2, 0);
#pragma unroll
      for (int i = 0; i < 4; ++i) vw[i] = ld128(rv, o + i * D_ * 2, 0);
#pragma unroll
      for (int i = 0; i < 4; ++i) skw[i] = __builtin_amdgcn_raw_buffer_load_b128(rs, pC0 * 8 + i * 16, 0, 0);
      PIN();
      asm volatile("s_waitcnt vmcnt(4)" ::: "memory");
      const rsrc_t rK = mkrs((const char*)gssm + (size_t)ch * 4096, 4096);
#pragma unroll
      for (int i = 0; i < 4; ++i) kr[i] = ld128(rK, lane * 16 + i * 1024, 0);
    }
    const v2u ep = hld64(ws, lane == 0 ? O_EPOCH : O_FP + x * 256 + (j >= 16) * 128, 0);
    const unsigned dvb = __builtin_amdgcn_raw_buffer_load_b16(mkrs(gD, D_ * 2), ch * 2, 0, 0);
    PIN();
    {
      unsigned pe[8][2];
#pragma unroll
      for (int pr = 0; pr < 4; ++pr) {
        const f2 w0 = {lo2f(skw[pr].x), lo2f(skw[pr].z)}, w1 = {hi2f(skw[pr].x), hi2f(skw[pr].z)};
        const f2 w2 = {lo2f(skw[pr].y), lo2f(skw[pr].w)}, w3 = {hi2f(skw[pr].y), hi2f(skw[pr].w)};
        f2 kf[7], o[4];
#pragma unroll
        for (int i = 0; i < 7; ++i) kf[i] = up2(kw[i][pr]);
#pragma unroll
        for (int i = 0; i < 4; ++i) o[i] = (kf[i + 3] * w0 + kf[i + 2] * w1 + kf[i + 1] * w2 + kf[i] * w3) * up2(vw[i][pr]);
        pe[2 * pr][0] = pk2(o[3].x, o[2].x);
        pe[2 * pr][1] = pk2(o[1].x, o[0].x);
        pe[2 * pr + 1][0] = pk2(o[3].y, o[2].y);
        pe[2 * pr + 1][1] = pk2(o[1].y, o[0].y);
      }
      const unsigned so = (unsigned)((pC0 + 4 * (lane >> 5)) * S_ + (pT0 & ~7)) * 2;
#pragma unroll
      for (int e = 0; e < 4; ++e) {
        const auto s0 = __builtin_amdgcn_permlane32_swap(pe[e][0], pe[e + 4][0], false, false);
        const auto s1 = __builtin_amdgcn_permlane32_swap(pe[e][1], pe[e + 4][1], false, false);
        hst128(v4u{s0[1], s1[1], s0[0], s1[0]}, ws, so, O_KVT + e * S_ * 2);
      }
    }
    STAMP(1);
    PIN();
    LAS char* T = L + w * WR;
    if (lane < 8) *(LAS v4u*)(T + lane * CS) = v4u{0u, 0u, 0u, 0u};
#pragma unroll
    for (int i = 0; i < 4; ++i) *(LAS v4u*)(T + 7 * CS + (i * 64 + lane + 1) * 16) = kr[i];
    asm volatile("s_waitcnt lgkmcnt(0)" ::: "memory");
    v4u kl[4];
#pragma unroll
    for (int i = 0; i < 4; ++i) kl[i] = *(LAS v4u*)(T + 7 * CS + (i * 64 + lane) * 16);
    auto tbuild = [&](int i) {
      const unsigned d[8] = {kl[i].x, kl[i].y, kl[i].z, kl[i].w, kr[i].x, kr[i].y, kr[i].z, kr[i].w};
      unsigned a[7];
#pragma unroll
      for (int e = 0; e < 7; ++e) a[e] = __builtin_amdgcn_alignbit(d[e + 1], d[e], 16);
#pragma unroll
      for (int m = 0; m < 7; ++m) {
        const int h = (m + 1) >> 1;
        const v4u o = (m & 1) ? v4u{d[h], d[h + 1], d[h + 2], d[h + 3]} : v4u{a[h], a[h + 1], a[h + 2], a[h + 3]};
        *(LAS v4u*)(T + m * CS + (i * 64 + lane + 1) * 16) = o;
      }
    };
    tbuild(0);
    make_tag(ep);
    DRAIN();
    asm volatile("s_barrier" ::: "memory");
    if (w == 0) {
      unsigned o = OOB;
      if (j < 16 && lane == 0) o = O_FP + x * 256 + ci * 8;
      if (j >= 16 && lane < 4) o = O_FP + ((x & 4) + lane) * 256 + 128 + ci * 8;
      hst64(tag, ws, o, 0);
    }
    STAMP(2);
    tbuild(1);
    tbuild(2);
    tbuild(3);
    if (lane < 32) *(LAS v4u*)(T + UOFF + lane * 16) = v4u{0u, 0u, 0u, 0u};
    STAMP(3);
    if (w == 0) poll64(ws, lane < 16 ? O_FP + x * 256 + (j >= 16) * 128 + lane * 8 : OOB, tag);
    WGBAR();
    STAMP(4);
    const v4u wf = hld128(ws, O_FW + x * 1024 + lane * 16, 0);
    v4u ur[4];
#pragma unroll
    for (int i = 0; i < 4; ++i) ur[i] = hld128(ws, lane * 16 + i * 1024, O_KVT + ch * 4096);
    const LAS char* pa = T + (l15 & 7) * CS + (3 - g + (l15 >> 3)) * 16;
    const LAS char* pv = T + UOFF + 512 + (l15 - 1) * 32 + g * 16;
    v4u fa[2][8], fv[2][8];
#pragma unroll
    for (int a = 0; a < 8; ++a) fa[0][a] = *(const LAS v4u*)(pa + 8 * a * 64);
    f4 acc[8];
#pragma unroll
    for (int q = 0; q < 8; ++q) {
      acc[q] = f4{0.f, 0.f, 0.f, 0.f};
      asm volatile("" : "+v"(acc[q]));
    }
    v4u ad;
    {
      const int jd = 8 * g - 9 - l15;
#pragma unroll
      for (int e = 0; e < 4; ++e) ad[e] = jd == 2 * e ? dvb : (jd == 2 * e + 1 ? dvb << 16 : 0u);
      asm volatile("" : "+v"(ad));
    }
    PIN();
#pragma unroll
    for (int i = 0; i < 4; ++i) *(LAS v4u*)(T + UOFF + 512 + i * 1024 + lane * 16) = ur[i];
    asm volatile("s_waitcnt lgkmcnt(0)" ::: "memory");
#pragma unroll
    for (int a = 0; a < 8; ++a) fv[0][a] = *(const LAS v4u*)(pv + a * 512);
    wok = wflags_ok(wf, lane, tag);
    __builtin_amdgcn_fence(__ATOMIC_ACQUIRE, "wavefront");
    const unsigned boff = wok ? lane * 16 : OOB;
    STAMP(5);
#pragma unroll
    for (int q = 0; q < 8; ++q) mfma_v(acc[q], ad, fv[0][q]);
    PIN();
#pragma unroll
    for (int g0 = 0; g0 < 8; ++g0) {
      int n = 0;
      if (g0 == 0) load_q();
#pragma unroll
      for (int a = 0; a < 8; ++a)
#pragma unroll
        for (int q = a; q < 8; ++q) {
          mfma_v(acc[q], fa[g0 & 1][a], fv[g0 & 1][q - a]);
          ++n;
          if ((n & 1) == 0) {
            const int e = n / 2 - 1;
            if (g0 < 7 && e < 16) {
              if (e < 8) fv[(g0 + 1) & 1][e] = *(const LAS v4u*)(pv + e * 512 - (g0 + 1) * 64);
              else fa[(g0 + 1) & 1][e - 8] = *(const LAS v4u*)(pa + (g0 + 1 + 8 * (e - 8)) * 64);
            }
            PIN();
          }
          if (n % 5 == 0) {
            const int i = g0 * 7 + n / 5 - 13;
            if (i < 0) {
              if (i < -9) zero_h1(i + 12);
            } else if (i < 25) b1[i] = hld128(ws, boff, sb1 + i * 1024);
            else if (i < 50) b2[i - 25] = hld128(ws, boff, sb2 + (i - 25) * 1024);
            PIN();
          }
        }
    }
    MFENCE();
    PIN();
    STAMP(6);
#pragma unroll
    for (int q = 0; q < 8; q += 2) {
      const auto s0 = __builtin_amdgcn_permlane16_swap(pk2(acc[q][0], acc[q][1]), pk2(acc[q + 1][0], acc[q + 1][1]), false, false);
      const auto s1 = __builtin_amdgcn_permlane16_swap(pk2(acc[q][2], acc[q][3]), pk2(acc[q + 1][2], acc[q + 1][3]), false, false);
      *(LAS v4u*)(T + 64 * w + (32 * (q + (g & 1)) + 2 * l15 + (g >> 1)) * 16) = v4u{s0[0], s1[0], s0[1], s1[1]};
    }
    WGBAR();
    {
      v4u zo[4];
#pragma unroll
      for (int i = 0; i < 4; ++i) zo[i] = *(LAS v4u*)(L + (lane & 3) * (WR + 64) + (64 * w + 16 * i + (lane >> 2)) * 16);
#pragma unroll
      for (int i = 0; i < 4; ++i) hst128(zo[i], ws, (lane >> 2) * 10240 + (lane & 3) * 16, O_ZT + (64 * w + 16 * i) * 10240 + (ch - w) * 16);
    }
    YOUNG();
    STAMP(37);
#pragma unroll
    for (int i = 19; i < 25; ++i) b2[i] = hld128(ws, boff, sb2 + i * 1024);
    PIN();
    WAITV(6);
  } else {
    const int hb = (j - 20) * 8 + x;
    const Job j0 = job_desc(hb, w, gw1, gw2, gw3, gw4);
    v4u r0[7];
    job_load(j0, lane, r0);
    const v2u ep = hld64(ws, O_EPOCH, 0);
    auto first_job = [&]() {
      job_put(j0, w, lane, r0, L);
      make_tag(ep);
      WGBAR();
      job_emit(j0, w, lane, L, ws);
      if (j == 20 && w == 0) hst128(v4u{tag.x, tag.y, 0u, 0u}, ws, (lane >> 3) * 1024 + (lane & 7) * 16, O_CNT + x * 128);
      asm volatile("" ::"v"(pf) : "memory");
      STAMP(1);
    };
    auto first_pub = [&]() {
      WGBAR();
      if (w == 0) hst64(tag, ws, lane < 8 ? O_FW + lane * 1024 + hb * 8 : OOB, 0);
      STAMP(2);
    };
    const v2u cw = __builtin_amdgcn_raw_buffer_load_b64(mkrs(gc, D_ * 2), tid * 8, 0, 0);
    PIN();
    Job jb[4];
#pragma unroll
    for (int i = 0; i < 4; ++i) jb[i] = job_desc(hb < 60 ? 240 + (hb & 31) : 96 + (hb - 60) + 36 * i, w, gw1, gw2, gw3, gw4);
    LAS float* sc = (LAS float*)(L + 4 * RT_BYTES);
    {
      const float c4[4] = {lo2f(cw.x), hi2f(cw.x), lo2f(cw.y), hi2f(cw.y)};
      f4 sv;
#pragma unroll
      for (int e = 0; e < 4; ++e) sv[e] = rbf(c4[e] * __builtin_amdgcn_rcpf(1.f + __builtin_amdgcn_exp2f(-1.4426950408889634f * c4[e])));
      *(LAS f4*)(sc + tid * 4) = sv;
    }
    first_job();
    if (hb < 60) {
      const rsrc_t rm = mkrs(gwm, D_ * 6 * D_ * 2);
      v4u wm[20], r1[7];
      YOUNG();
#pragma unroll
      for (int i = 0; i < 8; ++i) wm[i] = ld128(rm, (tid >> 3) * (6 * D_ * 2) + (tid & 7) * 16, hb * 128 + i * 32 * (6 * D_ * 2));
      PIN();
      WAITV(8);
      first_pub();
#pragma unroll
      for (int i = 8; i < 20; ++i) wm[i] = ld128(rm, (tid >> 3) * (6 * D_ * 2) + (tid & 7) * 16, hb * 128 + i * 32 * (6 * D_ * 2));
      const Job& j1 = jb[0];
      if (hb < 32) job_load(j1, lane, r1);
      load_q();
      float sl[20];
#pragma unroll
      for (int i = 0; i < 20; ++i) sl[i] = sc[(tid >> 3) + 32 * i];
      PIN();
      f2 a[4] = {f2{0.f, 0.f}, f2{0.f, 0.f}, f2{0.f, 0.f}, f2{0.f, 0.f}};
#pragma unroll
      for (int i = 0; i < 20; ++i)
#pragma unroll
        for (int e = 0; e < 4; ++e) a[e] += up2(wm[i][e]) * sl[i];
      LAS char* mred = L + 4 * RT_BYTES + 4096;
      *(LAS f4*)(mred + (tid >> 3) * 272 + (tid & 7) * 32) = f4{a[0].x, a[0].y, a[1].x, a[1].y};
      *(LAS f4*)(mred + (tid >> 3) * 272 + (tid & 7) * 32 + 16) = f4{a[2].x, a[2].y, a[3].x, a[3].y};
      if (hb < 32) job_put(j1, w, lane, r1, L);
      WGBAR();
      STAMP(3);
      {
        const int col = tid >> 2, part = tid & 3;
        float v[8];
#pragma unroll
        for (int r = 0; r < 8; ++r) v[r] = *(LAS float*)(mred + (part * 8 + r) * 272 + col * 4);
        float s = ((v[0] + v[1]) + (v[2] + v[3])) + ((v[4] + v[5]) + (v[6] + v[7]));
        s += dpp<0xB1>(s);
        s += dpp<0x4E>(s);
        s = rbf(s);
        if ((hb >= 10 && hb < 20) || (hb >= 40 && hb < 50)) s = rbf(1.f + s);
        if (part == 0) *((LAS unsigned short*)sc + 8 + col) = (unsigned short)(__builtin_bit_cast(unsigned, s) >> 16);
      }
      WGBAR();
      if (w == 0) hst128(*(LAS v4u*)((LAS char*)sc + 16 + (lane & 7) * 16), ws, lane < 8 ? lane * 16 : OOB, O_MOD + hb * 128);
      if (hb < 32) job_emit(j1, w, lane, L, ws);
    } else {
      v4u rr[4][7];
      YOUNG();
      job_load(jb[0], lane, rr[0]);
      PIN();
      WAITV(5);
      first_pub();
#pragma unroll
      for (int i = 1; i < 4; ++i) job_load(jb[i], lane, rr[i]);
      load_q();
      PIN();
#pragma unroll
      for (int i = 0; i < 2; ++i) job_put(jb[i], w, lane, rr[i], L + i * RT_BYTES);
      WGBAR();
#pragma unroll
      for (int i = 0; i < 2; ++i) job_emit(jb[i], w, lane, L + i * RT_BYTES, ws);
#pragma unroll
      for (int i = 2; i < 4; ++i) job_put(jb[i], w, lane, rr[i], L + i * RT_BYTES);
      WGBAR();
      STAMP(3);
#pragma unroll
      for (int i = 2; i < 4; ++i) job_emit(jb[i], w, lane, L + i * RT_BYTES, ws);
    }
    zero_h1(0);
    zero_h1(1);
    zero_h1(2);
    if (hb >= 48 && hb < 60) hst128(v4u{0u, 0u, 0u, 0u}, ws, w == 0 ? lane * 16 : OOB, O_FR + (hb - 48) * 1024);
    STAMP(4);
    DRAIN();
  }
  if (!wok) pollw(ws, O_FW + x * 1024 + lane * 16, lane, tag);
  WGBAR();
  STAMP(7);
  if (w == 0 && lane < 8)
    __hip_atomic_fetch_add((GAS unsigned*)((GAS char*)gws + O_CNT + (lane * 8 + x) * 128), 1u, __ATOMIC_RELAXED, __HIP_MEMORY_SCOPE_AGENT);
  if (!wok) {
#pragma unroll
    for (int i = 0; i < 25; ++i) b1[i] = hld128(ws, lane * 16, sb1 + i * 1024);
#pragma unroll
    for (int i = 0; i < 25; ++i) b2[i] = hld128(ws, lane * 16, sb2 + i * 1024);
  }
  f2 qv[10], n1[10];
  {
#pragma unroll
    for (int i = 0; i < 4; ++i) {
      qv[2 * i] = f2{lo2f(qm[0][i]), lo2f(qm[1][i])};
      qv[2 * i + 1] = f2{hi2f(qm[0][i]), hi2f(qm[1][i])};
    }
    qv[8] = f2{lo2f(qo[0]), lo2f(qo[1])};
    qv[9] = f2{hi2f(qo[0]), hi2f(qo[1])};
    f2 s = {0.f, 0.f}, ss = {0.f, 0.f};
#pragma unroll
    for (int i = 0; i < 10; ++i) { s += qv[i]; ss += qv[i] * qv[i]; }
    f2 mean, r;
    ln_scal(f2{wave_sum(s.x), wave_sum(s.y)}, f2{wave_sum(ss.x), wave_sum(ss.y)}, mean, r);
#pragma unroll
    for (int i = 0; i < 10; ++i) n1[i] = (qv[i] - mean) * r;
  }
  v4u b3[35], b4[70];
  const unsigned sb3 = O_W3 + (cg * 140 + w * 35) * 1024, sb4 = O_W4 + (tn * 280 + w * 70) * 1024;
#define LB3(lo, hi) _Pragma("unroll") for (int i_ = lo; i_ < hi; ++i_) b3[i_] = hld128(ws, lane * 16, sb3 + i_ * 1024)
#define LB4(lo, hi) _Pragma("unroll") for (int i_ = lo; i_ < hi; ++i_) b4[i_] = hld128(ws, lane * 16, sb4 + i_ * 1024)
  unsigned z3off = tid < 128 ? tid * 16 + 8192 : OOB;
  unsigned im0 = 16384 + lane * 128 + (((2 * w) ^ (lane & 7)) << 4);
  unsigned io0 = 16384 + (64 + (lane >> 2)) * 128 + (((2 * w) ^ ((lane >> 2) & 7)) << 4) + 4 * (lane & 3);
  unsigned roff = (tid >> 5) * 1024 + (((tid ^ (tid >> 3)) & 7) + 16 * ((tid >> 3) & 3)) * 16, roff3 = tid < 128 ? roff : OOB;
  const unsigned own_m = ((4 * tm + w) * 20 + 5 * (tn >> 1) + 3 * (tn & 1)) * 1024;
  const unsigned own_s = ((4 * tm + w) * 20 + 5 * (tn >> 1) + 2) * 1024 + (tn & 1) * 512;
  unsigned own_r = (l15 + 16 * (g >> 1)) * 16 + 8 * (g & 1), own_w = (g & 1) ? OOB : (l15 + 16 * (g >> 1)) * 16;
  const int sh = (tn >> 1) == 0 ? 1 : ((tn >> 1) == 1 ? -1 : ((tn >> 1) == 2 ? 2 : -2));
  unsigned h1m, h1s, f1pub = OOB, f1wait;
  {
    const int t2 = 64 * tm + 16 * w + l15 + sh;
    const bool ok = t2 >= 0 && t2 < S_;
    const unsigned d = ((t2 >> 4) * 20 + 5 * (tn >> 1)) * 1024 + (t2 & 15) * 16;
    h1m = ok ? d + 3072 * (tn & 1) + 256 * g : OOB;
    h1s = (ok && !(g & 1)) ? d + 2048 + 512 * (tn & 1) + 256 * (g >> 1) : OOB;
    const int nb = tm + (sh > 0 ? 1 : -1);
    if (lane == 0) f1pub = O_F1 + tm * 128 + tn * 8;
    if (lane == 1 && nb >= 0 && nb < 32) f1pub = O_F1 + nb * 128 + 64 + tn * 8;
    const bool up = ((lane >> 1) & 1) == 0;
    f1wait = (lane < 8 || (lane < 16 && (up ? tm > 0 : tm < 31))) ? O_F1 + tm * 128 + lane * 8 : OOB;
  }
  asm volatile("" : "+v"(z3off), "+v"(im0), "+v"(io0), "+v"(roff), "+v"(roff3), "+v"(own_r), "+v"(own_w), "+v"(h1m), "+v"(h1s), "+v"(f1pub), "+v"(f1wait));
  PIN();
  if (w == 0) {
    poll64(ws, lane < 8 ? O_CNT + (x * 8 + lane) * 128 : OOB, v2u{tag.x + 32, tag.y});
    if (b == 0) hst64(tag, ws, lane == 0 ? O_EPOCH : OOB, 0);
  }
  WGBAR();
  STAMP(8);

  {
    v4u zc[3];
#pragma unroll
    for (int i = 0; i < 3; ++i) zc[i] = hld128(ws, i < 2 ? tid * 16 + i * 4096 : z3off, O_ZT + (8 * tm + tn) * 10240);
    v4u mm[5];
    unsigned mo[5];
#pragma unroll
    for (int i = 0; i < 5; ++i) {
      mm[i] = hld128(ws, lane * 16, O_MOD + i * D_ * 2);
      mo[i] = __builtin_amdgcn_raw_buffer_load_b32(ws, lane * 4 + 1024, O_MOD + i * D_ * 2, 16);
    }
    PIN();
#pragma unroll
    for (int i = 0; i < 3; ++i)
#pragma unroll
      for (int e = 0; e < 4; ++e) *(LAS unsigned*)(L + e * 3072 + (tid + 256 * i) * 4) = zc[i][e];
    LB3(0, 5);
    STAMP(9);
    WGBAR();
    STAMP(10);
    const v4u z0 = *(LAS v4u*)(L + w * 3072 + rcm * 4), z1 = *(LAS v4u*)(L + w * 3072 + rcm * 4 + 16);
    const v2u z2 = *(LAS v2u*)(L + w * 3072 + rco * 4);
    auto md = [&](int v, int i) { const unsigned u = i < 8 ? mm[v][(i >> 1) & 3] : mo[v]; return (i & 1) ? hi2f(u) : lo2f(u); };
    f2 xv[10], s = {0.f, 0.f}, ss = {0.f, 0.f};
#pragma unroll
    for (int i = 0; i < 10; ++i) {
      const f2 z = up2(i < 4 ? z0[i & 3] : (i < 8 ? z1[i & 3] : z2[i & 1]));
      const f2 h = n1[i] * md(1, i) + md(0, i);
      xv[i] = qv[i] + h * z * md(2, i);
      s += xv[i];
      ss += xv[i] * xv[i];
    }
    PIN();
    LB3(5, 10);
    PIN();
    auto put = [&](const f2(&v)[10], int img) {
      *(LAS v4u*)(L + im0 + img * 10240) = v4u{pk2(v[0].x, v[1].x), pk2(v[2].x, v[3].x), pk2(v[4].x, v[5].x), pk2(v[6].x, v[7].x)};
      *(LAS v4u*)(L + (im0 ^ 16) + img * 10240) = v4u{pk2(v[0].y, v[1].y), pk2(v[2].y, v[3].y), pk2(v[4].y, v[5].y), pk2(v[6].y, v[7].y)};
      *(LAS unsigned*)(L + io0 + img * 10240) = pk2(v[8].x, v[9].x);
      *(LAS unsigned*)(L + (io0 ^ 16) + img * 10240) = pk2(v[8].y, v[9].y);
    };
    f2 mean, r;
    ln_scal(f2{wave_sum(s.x), wave_sum(s.y)}, f2{wave_sum(ss.x), wave_sum(ss.y)}, mean, r);
    put(xv, 2);
    s = f2{0.f, 0.f};
    ss = f2{0.f, 0.f};
    const f2 mr = -mean * r;
#pragma unroll
    for (int i = 0; i < 10; ++i) {
      xv[i] = (xv[i] * r + mr) * md(4, i) + md(3, i);
      s += xv[i];
      ss += xv[i] * xv[i];
    }
    PIN();
    LB3(10, 14);
    PIN();
    ln_scal(f2{wave_sum(s.x), wave_sum(s.y)}, f2{wave_sum(ss.x), wave_sum(ss.y)}, mean, r);
    put(xv, 1);
    const f2 mr3 = -mean * r;
#pragma unroll
    for (int i = 0; i < 10; ++i) xv[i] = xv[i] * r + mr3;
    put(xv, 0);
    STAMP(11);
    WGBAR();
    v4u oc[9];
#pragma unroll
    for (int i = 0; i < 9; ++i) oc[i] = *(LAS v4u*)(L + 16384 + (i / 3) * 10240 + (i % 3) * 4096 + tid * 16);
    const unsigned rs0 = ((4 * tm + (tn >> 1)) * 20) * 1024 + (tn & 1) * 128;
#pragma unroll
    for (int i = 0; i < 9; ++i) {
      hst128(oc[i], ws, (i % 3) < 2 ? roff : roff3, O_A1 + (i / 3) * SD2 + (i % 3) * 8192 + rs0);
      if (i == 2) YOUNG();
    }
    PIN();
  }
  STAMP(12);
  WAITV(6);
  WGBAR();
  STAMP(13);
  if (w == 0) {
    hst64(tag, ws, lane == 0 ? O_FR + tm * 128 + tn * 8 : OOB, 0);
    poll64(ws, lane < 8 ? O_FR + tm * 128 + lane * 8 : OOB, tag);
  }
  WGBAR();
  STAMP(14);

  f4 csp[7];
  {
    f4 acc[5];
    gemm_ks<5, 12>(ws, O_A1 + (4 * tm * 20 + 5 * w) * 1024, 20 * 1024, b1, L, w, lane, acc,
                   [&](int n) { b3[14 + n] = hld128(ws, lane * 16, sb3 + (14 + n) * 1024); }, [&]() { LB3(26, 35); } STPASS(15));
    STAMP(18);
    f2 v[10];
    unsigned o[10];
#pragma unroll
    for (int t = 0; t < 5; ++t) {
      v[2 * t] = f2{acc[t][0], acc[t][1]};
      v[2 * t + 1] = f2{acc[t][2], acc[t][3]};
    }
    gelu_pack<10>(v, 1.f, 1.f, o, [](int) {});
    const auto s0 = __builtin_amdgcn_permlane16_swap(o[8], o[8], false, false);
    const auto s1 = __builtin_amdgcn_permlane16_swap(o[9], o[9], false, false);
    hst128(v4u{o[0], o[1], o[2], o[3]}, ws, h1m, O_H1);
    hst128(v4u{o[4], o[5], o[6], o[7]}, ws, h1m + 1024, O_H1);
    hst128(v4u{s0[0], s1[0], s0[1], s1[1]}, ws, h1s, O_H1);
    STAMP(19);
    DRAIN();
    WGBAR();
    STAMP(20);
    if (w == 0) hst64(tag, ws, f1pub, 0);
    const v4u ones = {0x3f803f80u, 0x3f803f80u, 0x3f803f80u, 0x3f803f80u};
#pragma unroll
    for (int i = 0; i < 35; ++i) {
      if (i < 20) *(LAS v4u*)(L + (w * 25 + i) * 1024 + lane * 16) = b3[i];
      csp[i % 7] = mfma(b3[i], ones, i < 7 ? f4{0.f, 0.f, 0.f, 0.f} : csp[i % 7]);
      PIN();
    }
    if (w == 0) poll64(ws, f1wait, tag);
    WGBAR();
    STAMP(21);
  }
  v4u xr[2];
  v2u xro;
  unsigned x1p[10];
  {
    v4u hr[2];
    v2u hro;
    f4 acc[5];
    gemm_ks<5, 12>(ws, O_H1 + (4 * tm * 20 + 5 * w) * 1024, 20 * 1024, b2, L, w, lane, acc, [&](int n) {
      if (n == 0) hr[0] = hld128(ws, lane * 16, O_HP + own_m);
      else if (n == 1) hr[1] = hld128(ws, lane * 16 + 1024, O_HP + own_m);
      else if (n == 2) hro = hld64(ws, own_r, O_HP + own_s);
      else if (n == 3) xr[0] = hld128(ws, lane * 16, O_X + own_m);
      else if (n == 4) xr[1] = hld128(ws, lane * 16 + 1024, O_X + own_m);
      else if (n == 5) xro = hld64(ws, own_r, O_X + own_s);
      else b4[n - 6] = hld128(ws, lane * 16, sb4 + (n - 6) * 1024);
    }, [&]() { LB4(6, 12); } STPASS(22));
    f2 s = {0.f, 0.f}, ss = {0.f, 0.f};
#pragma unroll
    for (int t = 0; t < 5; ++t) {
      const unsigned h0 = t < 4 ? hr[t >> 1][2 * (t & 1)] : hro.x, h1 = t < 4 ? hr[t >> 1][2 * (t & 1) + 1] : hro.y;
      const f2 p = f2{acc[t][0], acc[t][1]} + up2(h0), q = f2{acc[t][2], acc[t][3]} + up2(h1);
      x1p[2 * t] = pk2(p.x, p.y);
      x1p[2 * t + 1] = pk2(q.x, q.y);
    }
    const auto s0 = __builtin_amdgcn_permlane16_swap(x1p[8], x1p[8], false, false);
    const auto s1 = __builtin_amdgcn_permlane16_swap(x1p[9], x1p[9], false, false);
    hst128(v4u{x1p[0], x1p[1], x1p[2], x1p[3]}, ws, lane * 16, O_X1 + own_m);
    hst128(v4u{x1p[4], x1p[5], x1p[6], x1p[7]}, ws, lane * 16 + 1024, O_X1 + own_m);
    hst128(v4u{s0[0], s1[0], s0[1], s1[1]}, ws, own_w, O_X1 + own_s);
#pragma unroll
    for (int i = 0; i < 10; ++i) {
      const f2 pr = up2(x1p[i]);
      s += pr;
      ss += pr * pr;
    }
    const float t1 = xrow_sum(s.x + s.y), t2 = xrow_sum(ss.x + ss.y);
    hst64(v2u{__builtin_bit_cast(unsigned, t1), __builtin_bit_cast(unsigned, t2)}, ws, lane < 16 ? lane * 8 : OOB,
          O_STAT + ((4 * tm + w) * 8 + tn) * 128);
    STAMP(25);
    DRAIN();
    WGBAR();
    STAMP(26);
    if (w == 0) hst64(tag, ws, lane == 0 ? O_F2 + (tm >> 1) * 128 + ((tm & 1) * 8 + tn) * 8 : OOB, 0);
#pragma unroll
    for (int i = 20; i < 35; ++i) *(LAS v4u*)(L + (i < 25 ? w * 25 + i : 75 + w * 10 + i) * 1024 + lane * 16) = b3[i];
    if (l15 == 0) {
#pragma unroll
      for (int t = 0; t < 7; ++t) *(LAS f4*)(L + CSUM_OFF + ((w * 7 + t) * 4 + g) * 16) = csp[t];
    }
    if (w == 0) poll64(ws, lane < 16 ? O_F2 + rg * 128 + lane * 8 : OOB, tag);
    WGBAR();
    STAMP(27);
  }
  {
    constexpr int W3 = 6, R3 = W3 + 1;
    v4u a[R3][2];
    const unsigned sa = O_X1 + (8 * rg + 2 * w) * 20 * 1024;
    v2u sv[2][2];
#pragma unroll
    for (int i = 0; i < 2; ++i) {
      sv[i][0] = hld64(ws, l15 * 8 + g * 256, O_STAT + (8 * rg + 2 * w + i) * 1024);
      sv[i][1] = hld64(ws, l15 * 8 + g * 256 + 128, O_STAT + (8 * rg + 2 * w + i) * 1024);
    }
#pragma unroll
    for (int k = 0; k < W3; ++k)
#pragma unroll
      for (int i = 0; i < 2; ++i) {
        a[k][i] = hld128(ws, lane * 16, sa + (i * 20 + k) * 1024);
        PIN();
      }
    f4 acc[2][7];
    f2 r, den;
    {
      f4 cp[28];
#pragma unroll
      for (int t = 0; t < 28; ++t) cp[t] = *(LAS f4*)(L + CSUM_OFF + (t * 4 + g) * 16);
      PIN();
      f2 mean;
      const f2 p0 = __builtin_bit_cast(f2, sv[0][0]) + __builtin_bit_cast(f2, sv[0][1]);
      const f2 p1 = __builtin_bit_cast(f2, sv[1][0]) + __builtin_bit_cast(f2, sv[1][1]);
#pragma unroll
      for (int t = 0; t < 7; ++t) cp[t] = (cp[t] + cp[7 + t]) + (cp[14 + t] + cp[21 + t]);
      ln_scal(f2{xrow_sum(p0.x), xrow_sum(p1.x)}, f2{xrow_sum(p0.y), xrow_sum(p1.y)}, mean, r, &den);
#pragma unroll
      for (int t = 0; t < 7; ++t) {
        acc[0][t] = cp[t] * -mean.x;
        acc[1][t] = cp[t] * -mean.y;
      }
    }
    auto bslot = [](int k, int t) { const int v = k / 5, i = (k % 5) * 7 + t; return (i < 25 ? v * 25 + i : 75 + v * 10 + i) * 1024; };
    const LAS char* Lb = L + lane * 16;
    const LAS char* Lb2 = L + lane * 16 + 65536;
    const LAS char* Lb3 = L + lane * 16 + 131072;
    auto rdb = [&](int k, int t) { const int o = bslot(k, t); return *(const LAS v4u*)(o < 65536 ? Lb + o : (o < 131072 ? Lb2 + (o - 65536) : Lb3 + (o - 131072))); };
    v4u bf[2][7];
#pragma unroll
    for (int t = 0; t < 7; ++t) bf[0][t] = rdb(0, t);
    PIN();
#pragma unroll
    for (int k = 0; k < 20; ++k) {
      __builtin_amdgcn_s_waitcnt(0xC07F);
#pragma unroll
      for (int t = 0; t < 7; ++t) {
        mfma_v(acc[0][t], bf[k & 1][t], a[k % R3][0]);
        if (k < 19) bf[(k + 1) & 1][t] = rdb(k + 1, t);
        PIN();
      }
#pragma unroll
      for (int t = 0; t < 7; ++t) {
        mfma_v(acc[1][t], bf[k & 1][t], a[k % R3][1]);
        if (t == 0 || t == 2) {
          const int i = t >> 1;
          if (k + W3 < 20) a[(k + W3) % R3][i] = hld128(ws, lane * 16, sa + (i * 20 + k + W3) * 1024);
          else b4[22 + 2 * (k + W3 - 20) + i] = hld128(ws, lane * 16, sb4 + (22 + 2 * (k + W3 - 20) + i) * 1024);
        }
        if (t == 4 && (k & 1)) b4[12 + k / 2] = hld128(ws, lane * 16, sb4 + (12 + k / 2) * 1024);
        PIN();
      }
      if (k == 0) STAMP(28);
    }
    MFENCE();
    PIN();
    STAMP(29);
    unsigned os[2][2];
    const unsigned hs = O_H + ((8 * rg + 2 * w) * 56 + 7 * (cg >> 1)) * 1024;
#pragma unroll
    for (int i = 0; i < 2; ++i) {
      f2 v[14];
      unsigned o[14];
#pragma unroll
      for (int t = 0; t < 7; ++t) {
        v[2 * t] = f2{acc[i][t][0], acc[i][t][1]};
        v[2 * t + 1] = f2{acc[i][t][2], acc[i][t][3]};
      }
      gelu_pack<14>(v, r[i], den[i], o, [&](int m) {
        const int f = 34 + i * 8 + 2 * m;
        b4[f] = hld128(ws, lane * 16, sb4 + f * 1024);
        b4[f + 1] = hld128(ws, lane * 16, sb4 + (f + 1) * 1024);
      });
#pragma unroll
      for (int e = 0; e < 3; ++e)
        hst128(v4u{o[4 * e], o[4 * e + 1], o[4 * e + 2], o[4 * e + 3]}, ws, lane * 16 + e * 1024, hs + i * 57344 + (cg & 1) * 4096);
      os[i][0] = o[12];
      os[i][1] = o[13];
    }
    {
      const auto s0 = __builtin_amdgcn_permlane16_swap(os[0][0], os[1][0], false, false);
      const auto s1 = __builtin_amdgcn_permlane16_swap(os[0][1], os[1][1], false, false);
      hst128(v4u{s0[0], s1[0], s0[1], s1[1]}, ws, (g & 1) * 57344 + (l15 + 16 * (g >> 1)) * 16, hs + 3072 + (cg & 1) * 512);
    }
    YOUNG();
    LB4(50, 56);
    PIN();
    STAMP(31);
    WAITV(6);
    WGBAR();
    STAMP(32);
    if (w == 0) {
      hst64(tag, ws, lane == 0 ? O_F3 + rg * 128 + cg * 8 : OOB, 0);
      poll64(ws, lane < 16 ? O_F3 + (tm >> 1) * 128 + lane * 8 : OOB, tag);
    }
    WGBAR();
    STAMP(33);
  }
  {
    v4u gb[2];
    v2u gbo;
    f4 acc[5];
    LB4(56, 70);
    gemm_ks<14, 14>(ws, O_H + (4 * tm * 56 + 14 * w) * 1024, 56 * 1024, b4, L, w, lane, acc, [&](int n) {
      if (n < 2) gb[n] = hld128(ws, g * 16 + n * 64, O_MOD + (5 * D_ + 80 * tn + 16 * (tn & 1)) * 2);
      else if (n == 2) gbo = hld64(ws, g * 8, O_MOD + (5 * D_ + 80 * tn + ((tn & 1) ? 0 : 64)) * 2);
    }, [&]() {} STPASS(34));
    unsigned o[10];
#pragma unroll
    for (int i = 0; i < 10; ++i) {
      const unsigned xw = i < 8 ? xr[i >> 2][i & 3] : xro[i & 1], gw = i < 8 ? gb[i >> 2][i & 3] : gbo[i & 1];
      const f2 p = (f2{acc[i >> 1][2 * (i & 1)], acc[i >> 1][2 * (i & 1) + 1]} + up2(x1p[i])) * up2(gw) + up2(xw);
      o[i] = pk2(p.x, p.y);
    }
    const rsrc_t ro = mkrs(gout, SD2);
    const unsigned d = ((64 * tm + 16 * w + l15) * D_ + 80 * tn) * 2;
    __builtin_amdgcn_raw_buffer_store_b128(v4u{o[0], o[1], o[2], o[3]}, ro, d + 16 * g + 32 * (tn & 1), 0, 0);
    __builtin_amdgcn_raw_buffer_store_b128(v4u{o[4], o[5], o[6], o[7]}, ro, d + 64 + 16 * g + 32 * (tn & 1), 0, 0);
    __builtin_amdgcn_raw_buffer_store_b64(v2u{o[8], o[9]}, ro, d + 8 * g, (tn & 1) ? 0 : 128, 0);
  }
  asm volatile(".Lfused_end:" ::: "memory");
}

extern "C" void kernel_launch(void* const* d_in, const int* in_sizes, int n_in, void* d_out, int out_size,
                              void* d_ws, size_t ws_size, hipStream_t stream) {
  (void)hipFuncSetAttribute((const void*)fused, hipFuncAttributeMaxDynamicSharedMemorySize, LDS_BYTES);
  fused<<<256, 256, LDS_BYTES, stream>>>(d_in[0], d_in[1], d_in[2], d_in[3], d_in[4], d_in[5], d_in[7], d_in[8], d_in[9],
                                         d_in[10], d_in[11], d_in[12], d_out, d_ws);
}
```

```cpp
#include <hip/hip_runtime.h>
#include <stdint.h>

typedef unsigned v4u __attribute__((ext_vector_type(4)));
typedef unsigned v2u __attribute__((ext_vector_type(2)));
typedef float f4 __attribute__((ext_vector_type(4)));
typedef float f2 __attribute__((ext_vector_type(2)));
typedef short s8v __attribute__((ext_vector_type(8)));
typedef short s4v __attribute__((ext_vector_type(4)));
typedef __bf16 bf2 __attribute__((ext_vector_type(2)));
typedef __bf16 bf8 __attribute__((ext_vector_type(8)));
typedef __amdgpu_buffer_rsrc_t rsrc_t;
#define LAS __attribute__((address_space(3)))
#define GAS __attribute__((address_space(1)))
#define DI __device__ __forceinline__

static constexpr int S_ = 2048, D_ = 640, F_ = 1728;
static constexpr unsigned OOB = 0x80000000u;

static constexpr unsigned O_EPOCH = 0;
static constexpr unsigned O_FP = 1024;
static constexpr unsigned O_FR = O_FP + 16 * 128;
static constexpr unsigned O_F1 = O_FR + 32 * 128;
static constexpr unsigned O_F2 = O_F1 + 32 * 128;
static constexpr unsigned O_F3 = O_F2 + 16 * 128;
static constexpr unsigned O_F4 = O_F3 + 16 * 512;
static constexpr unsigned O_CNT = O_F4 + 16 * 512;
static constexpr unsigned O_FW = O_CNT + 64 * 128;
static constexpr unsigned O_FB = O_FW + 8192;
static constexpr unsigned O_MOD = 65536;
static constexpr unsigned O_STAT = O_MOD + 16384;
static constexpr unsigned O_KVT = O_STAT + 131072;
static constexpr unsigned SD2 = S_ * D_ * 2;
static constexpr unsigned KVP = 4096 + 128, ZP = 10240 + 128;
static constexpr unsigned O_ZT = O_KVT + D_ * KVP;
static constexpr unsigned O_A1 = O_ZT + 256 * ZP;
static constexpr unsigned O_HP = O_A1 + SD2;
static constexpr unsigned O_X = O_HP + SD2;
static constexpr unsigned O_H1 = O_X + SD2;
static constexpr unsigned O_X1 = O_H1 + SD2;
static constexpr unsigned O_H = O_X1 + SD2;
static constexpr unsigned O_W1 = O_H + 128 * 56 * 1024;
static constexpr unsigned O_W2 = O_W1 + 800 * 1024;
static constexpr unsigned O_W3 = O_W2 + 800 * 1024;
static constexpr unsigned O_W4 = O_W3 + 2240 * 1024;
static constexpr unsigned O_END = O_W4 + 2240 * 1024;
static constexpr unsigned O_DBG = O_END;

static constexpr int CS = 4160;
static constexpr int UOFF = 8 * CS;
static constexpr int WR = 8 * CS + 4608;
static constexpr int LDS_BYTES = 160 * 1024;
static constexpr int RED_OFF = 100 * 1024;
static constexpr int CSUM_OFF = 140 * 1024;
static constexpr unsigned SPIN_CAP = 200000;

template <int V> struct IC { static constexpr int v = V; };
template <int I, int N, class F> DI void sfor(F&& f) {
  if constexpr (I < N) {
    f(IC<I>{});
    sfor<I + 1, N>(f);
  }
}
DI float lo2f(unsigned w) { return __builtin_bit_cast(float, w << 16); }
DI float hi2f(unsigned w) { return __builtin_bit_cast(float, w & 0xffff0000u); }
DI f2 up2(unsigned w) { return f2{lo2f(w), hi2f(w)}; }
DI unsigned pk2(float a, float b) { return __builtin_bit_cast(unsigned, __builtin_convertvector(f2{a, b}, bf2)); }
DI f4 mfma(v4u a, v4u b, f4 c) {
  return __builtin_amdgcn_mfma_f32_16x16x32_bf16(__builtin_bit_cast(bf8, a), __builtin_bit_cast(bf8, b), c, 0, 0, 0);
}
DI void mfma_v(f4& c, v4u a, v4u b) { asm volatile("v_mfma_f32_16x16x32_bf16 %0, %1, %2, %0" : "+v"(c) : "v"(a), "v"(b)); }
DI void mfma_v0(f4& c, v4u a, v4u b) { asm volatile("v_mfma_f32_16x16x32_bf16 %0, %1, %2, 0" : "=&v"(c) : "v"(a), "v"(b)); }
DI void mfma_va(f4& c, v4u a, v4u b) { asm volatile("v_mfma_f32_16x16x32_bf16 %0, %1, %2, %0" : "+v"(c) : "v"(a), "a"(b)); }
DI void mfma_va0(f4& c, v4u a, v4u b) { asm volatile("v_mfma_f32_16x16x32_bf16 %0, %1, %2, 0" : "=&v"(c) : "v"(a), "a"(b)); }
DI void mfma_av(f4& c, v4u a, v4u b) { asm volatile("v_mfma_f32_16x16x32_bf16 %0, %1, %2, %0" : "+v"(c) : "a"(a), "v"(b)); }
DI void mfma_av0(f4& c, v4u a, v4u b) { asm volatile("v_mfma_f32_16x16x32_bf16 %0, %1, %2, 0" : "=&v"(c) : "a"(a), "v"(b)); }
#define KEEP(x) asm volatile("" : "+v"(x))
#define MFENCE() asm volatile("s_nop 7\n\ts_nop 1" ::: "memory")
DI float add1(float a, float b) { float r; asm("v_add_f32 %0, %1, %2" : "=v"(r) : "v"(a), "v"(b)); return r; }
DI float fma1(float a, float b, float c) { float r; asm("v_fma_f32 %0, %1, %2, %3" : "=v"(r) : "v"(a), "v"(b), "v"(c)); return r; }
DI rsrc_t mkrs(const void* p, unsigned bytes) { return __builtin_amdgcn_make_buffer_rsrc((void*)p, 0, bytes, 0x00020000); }
#define INAUX 2
DI v4u ld128(rsrc_t r, unsigned v, unsigned s) { return __builtin_amdgcn_raw_buffer_load_b128(r, v, s, INAUX); }
DI v2u ld64(rsrc_t r, unsigned v, unsigned s) { return __builtin_amdgcn_raw_buffer_load_b64(r, v, s, INAUX); }
DI unsigned ld32(rsrc_t r, unsigned v, unsigned s) { return __builtin_amdgcn_raw_buffer_load_b32(r, v, s, INAUX); }
DI v4u hld128(rsrc_t r, unsigned v, unsigned s) { return __builtin_amdgcn_raw_buffer_load_b128(r, v, s, 16); }
DI v2u hld64(rsrc_t r, unsigned v, unsigned s) { return __builtin_amdgcn_raw_buffer_load_b64(r, v, s, 16); }
DI void hst128(v4u x, rsrc_t r, unsigned v, unsigned s) { __builtin_amdgcn_raw_buffer_store_b128(x, r, v + s, 0, 16); }
DI void hst64(v2u x, rsrc_t r, unsigned v, unsigned s) { __builtin_amdgcn_raw_buffer_store_b64(x, r, v, s, 16); }
DI void hst32(unsigned x, rsrc_t r, unsigned v, unsigned s) { __builtin_amdgcn_raw_buffer_store_b32(x, r, v, s, 16); }
#define DRAIN() do { asm volatile("s_waitcnt vmcnt(0)" ::: "memory"); __builtin_amdgcn_s_waitcnt(0x0F70); } while (0)
#define WGBAR() asm volatile("s_waitcnt lgkmcnt(0)\n\ts_barrier" ::: "memory")
#define PIN() __builtin_amdgcn_sched_barrier(0)

template <int CTRL> DI float dpp(float x) {
  return __builtin_bit_cast(float, __builtin_amdgcn_mov_dpp(__builtin_bit_cast(int, x), CTRL, 0xf, 0xf, true));
}
DI float xrow_sum(float x) {
  auto s = __builtin_amdgcn_permlane16_swap(__builtin_bit_cast(unsigned, x), __builtin_bit_cast(unsigned, x), false, false);
  x = __builtin_bit_cast(float, (unsigned)s[0]) + __builtin_bit_cast(float, (unsigned)s[1]);
  auto t = __builtin_amdgcn_permlane32_swap(__builtin_bit_cast(unsigned, x), __builtin_bit_cast(unsigned, x), false, false);
  return __builtin_bit_cast(float, (unsigned)t[0]) + __builtin_bit_cast(float, (unsigned)t[1]);
}
DI float swap32_add(float a, float b) {
  auto t = __builtin_amdgcn_permlane32_swap(__builtin_bit_cast(unsigned, a), __builtin_bit_cast(unsigned, b), false, false);
  return __builtin_bit_cast(float, (unsigned)t[0]) + __builtin_bit_cast(float, (unsigned)t[1]);
}
DI float swap16_add(float a, float b) {
  auto t = __builtin_amdgcn_permlane16_swap(__builtin_bit_cast(unsigned, a), __builtin_bit_cast(unsigned, b), false, false);
  return __builtin_bit_cast(float, (unsigned)t[0]) + __builtin_bit_cast(float, (unsigned)t[1]);
}
DI float rdl(float v, int l) { return __builtin_bit_cast(float, __builtin_amdgcn_readlane(__builtin_bit_cast(int, v), l)); }
DI void wave_sum4(f2& s, f2& ss) {
  float v = swap16_add(swap32_add(s.x, s.y), swap32_add(ss.x, ss.y));
  v += dpp<0xB1>(v);
  v += dpp<0x4E>(v);
  v += dpp<0x141>(v);
  v += dpp<0x128>(v);
  s = f2{rdl(v, 0), rdl(v, 32)};
  ss = f2{rdl(v, 16), rdl(v, 48)};
}
DI void poll64(rsrc_t ws, unsigned voff, v2u want) {
  const bool idle = voff >= OOB;
  for (unsigned spin = 0; spin < SPIN_CAP; ++spin) {
    asm volatile("" ::: "memory");
    const v2u v = hld64(ws, voff, 0);
    if (__all(idle || (v.x == want.x && v.y == want.y))) break;
  }
  __builtin_amdgcn_fence(__ATOMIC_ACQUIRE, "wavefront");
}

DI bool wflags_ok(v4u a, int lane, int nl, v2u want) {
  return __all(lane >= nl || (a.x == want.x && a.y == want.y && a.z == want.x && a.w == want.y));
}
DI void pollw(rsrc_t ws, unsigned voff, int lane, int nl, v2u want) {
  for (unsigned spin = 0; spin < SPIN_CAP; ++spin) {
    asm volatile("" ::: "memory");
    if (wflags_ok(hld128(ws, voff, 0), lane, nl, want)) break;
    __builtin_amdgcn_s_sleep(4);
  }
  __builtin_amdgcn_fence(__ATOMIC_ACQUIRE, "wavefront");
}
DI float rbf(float x) { return lo2f(pk2(x, 0.f)); }
DI void ln_scal(f2 s, f2 ss, f2& r, f2& mr, f2* pmean = nullptr, f2* pden = nullptr) {
  const f2 m = s * (1.f / D_);
  const f2 var = ss * (1.f / D_) - m * m;
  const f2 v = f2{fmaxf(var.x, 0.f), fmaxf(var.y, 0.f)} + 1e-5f;
  r = f2{__builtin_amdgcn_rsqf(v.x), __builtin_amdgcn_rsqf(v.y)};
  mr = -m * r;
  if (pmean) *pmean = m;
  if (pden) *pden = v * r;
}
template <int N, class Mid> DI void gelu_pack(const f2 (&x)[N], float r, float den, unsigned* o, Mid mid) {
  const float c0 = -2.f * 0.7978845608028654f * 1.4426950408889634f * r, c1 = c0 * 0.044715f * r * r;
  f2 a[N];
#pragma unroll
  for (int i = 0; i < N; ++i) a[i] = x[i] * x[i] * c1 + c0;
  mid(0);
  PIN();
#pragma unroll
  for (int i = 0; i < N; ++i) a[i] *= x[i];
  mid(1);
  PIN();
#pragma unroll
  for (int i = 0; i < N; ++i) a[i] = f2{__builtin_amdgcn_exp2f(a[i].x), __builtin_amdgcn_exp2f(a[i].y)};
  mid(2);
  PIN();
#pragma unroll
  for (int i = 0; i < N; ++i) a[i] = a[i] * den + den;
  PIN();
#pragma unroll
  for (int i = 0; i < N; ++i) a[i] = f2{__builtin_amdgcn_rcpf(a[i].x), __builtin_amdgcn_rcpf(a[i].y)};
  mid(3);
  PIN();
#pragma unroll
  for (int i = 0; i < N; ++i) a[i] *= x[i];
  PIN();
#pragma unroll
  for (int i = 0; i < N; ++i) o[i] = pk2(a[i].x, a[i].y);
}

#define STAMP(i)
#define STARG
#define STPASS(n)

struct Job { rsrc_t rs; unsigned pitch, src, dst, wide; };
static constexpr int RT_PITCH = 912, RT_BYTES = 32 * RT_PITCH;
DI Job job_desc(int job, int w, const void* w1, const void* w2, const void* w3, const void* w4) {
  Job t;
  const bool is4 = job >= 160, is3 = !is4 && job >= 80;
  const int jj = job - (is4 ? 160 : (is3 ? 80 : (job >= 40 ? 40 : 0)));
  const int gq = is3 ? jj & 3 : jj & 1, ks = is3 ? jj >> 2 : jj >> 1;
  t.wide = is3 ? (gq == 3 ? 2 : 1) : 0;
  t.pitch = is3 ? F_ * 2 : D_ * 2;
  t.rs = mkrs(is4 ? w4 : (is3 ? w3 : (job >= 40 ? w2 : w1)), (is3 || is4) ? D_ * F_ * 2 : D_ * D_ * 2);
  t.src = (32 * ks + 8 * w) * t.pitch + gq * (is3 ? 896 : 640);
  t.dst = (is4 ? O_W4 : (is3 ? O_W3 : (job >= 40 ? O_W2 : O_W1))) + ((gq * 4 + w) * (is4 ? 56 : 20) + ks) * (is3 ? 7168 : 5120);
  return t;
}
DI void job_load(const Job& t, int lane, v4u (&r)[7]) {
  const unsigned nc = t.wide ? 56 : 40, magic = t.wide ? 1171u : 1639u, clim = t.wide == 2 ? 48 : 64;
#pragma unroll
  for (int i = 0; i < 7; ++i) {
    const unsigned c = i * 64 + lane, row = (c * magic) >> 16, col = c - row * nc;
    r[i] = ld128(t.rs, (row < 8 && col < clim) ? row * t.pitch + col * 16 + t.src : OOB, 0);
  }
}
DI void job_put(const Job& t, int w, int lane, const v4u (&r)[7], LAS char* L) {
  const unsigned nc = t.wide ? 56 : 40, magic = t.wide ? 1171u : 1639u;
#pragma unroll
  for (int i = 0; i < 7; ++i) {
    const unsigned c = i * 64 + lane, row = (c * magic) >> 16, col = c - row * nc;
    if (i < 5 || t.wide) *(LAS v4u*)(L + (8 * w + row) * RT_PITCH + col * 16) = r[i];
  }
}
DI void job_emit(const Job& t, int w, int lane, LAS char* L, rsrc_t ws) {
  const int g = lane >> 4, q = (lane & 15) >> 2, p = lane & 3;
  LAS char* lo = L + (8 * g + q) * RT_PITCH + w * (t.wide ? 224 : 160);
  auto frag = [&](unsigned cb, unsigned f) {
    const v2u a = __builtin_bit_cast(v2u, __builtin_amdgcn_ds_read_tr16_b64_v4i16((LAS s4v*)(lo + cb)));
    const v2u c = __builtin_bit_cast(v2u, __builtin_amdgcn_ds_read_tr16_b64_v4i16((LAS s4v*)(lo + cb + 4 * RT_PITCH)));
    hst128(v4u{a.x, a.y, c.x, c.y}, ws, lane * 16 + f * 1024, t.dst);
  };
  const unsigned pb = 32 * (w & 1) + 16 * p;
#pragma unroll
  for (int f = 0; f < 4; ++f) frag(pb + 64 * (f >> 1) + 8 * (f & 1), f);
  if (t.wide) {
    frag(pb + 128, 4);
    frag(pb + 136, 5);
    frag(((w & 1) ? 0 : 192) + 8 * p, 6);
  } else {
    frag(((w & 1) ? 0 : 128) + 8 * p, 4);
  }
}

template <int KS, int WIN, class Extra, class Tail, class Early>
DI void gemm_ks(rsrc_t ws, unsigned abase, unsigned mstride, const v4u* b, LAS char* L, int w, int lane, f4 (&acc)[5], Extra extra, Tail tail, Early early STARG) {
  constexpr int NA = 4 * KS, RING = WIN + 1;
  v4u a[RING];
  unsigned so[4];
#pragma unroll
  for (int i = 0; i < 4; ++i) so[i] = abase + ((w + 1 + i) & 3) * mstride;
#pragma unroll
  for (int n = 0; n < WIN; ++n) {
    a[n] = hld128(ws, lane * 16 + (n % KS) * 1024, so[n / KS]);
    early(n);
    PIN();
  }
  f4 prev[5], p[15];
  unsigned lr = RED_OFF + lane * 16;
  KEEP(lr);
  sfor<0, NA>([&](auto ic) {
    constexpr int n = decltype(ic)::v, i = n / KS, k = n % KS;
    if (n == NA - 3) {
      STAMP(sb + 1);
      WGBAR();
      STAMP(sb + 2);
    }
#pragma unroll
    for (int t = 0; t < 5; ++t) {
      acc[t] = mfma(b[k * 5 + t], a[n % RING], k == 0 ? f4{0.f, 0.f, 0.f, 0.f} : acc[t]);
      if (t == 1) {
        if constexpr (n + WIN < NA) a[(n + WIN) % RING] = hld128(ws, lane * 16 + ((n + WIN) % KS) * 1024, so[(n + WIN) / KS]);
        else extra(n + WIN - NA);
      }
      if (k == 0 && i > 0) *(LAS f4*)(L + (lr + ((w + i) & 3) * 15360) + ((i - 1) * 5 + t) * 1024) = prev[t];
      if (n == NA - 3) {
        p[t] = *(LAS f4*)(L + (lr + w * 15360) + t * 1024);
        p[5 + t] = *(LAS f4*)(L + (lr + w * 15360) + (5 + t) * 1024);
      }
      if (n == NA - 2) p[10 + t] = *(LAS f4*)(L + (lr + w * 15360) + (10 + t) * 1024);
      if (n < NA - 1) PIN();
    }
    if (n == 0) STAMP(sb);
    if (k == KS - 1) {
#pragma unroll
      for (int t = 0; t < 5; ++t) prev[t] = acc[t];
    }
  });
  tail();
#pragma unroll
  for (int t = 0; t < 5; ++t) acc[t] += (p[t] + p[5 + t]) + p[10 + t];
}

#define YOUNG() do { asm volatile("; YOUNG" ::: "memory"); PIN(); } while (0)
#define WAITV(n) asm volatile("s_waitcnt vmcnt(" #n ") ; WAITV" ::: "memory")
__global__ __launch_bounds__(256, 1) void fused(const void*, const void*, const void*, const void*, const void*, const void*,
                                                const void*, const void*, const void*, const void*, const void*, const void*,
                                                void*, void*) {
  extern __shared__ __attribute__((aligned(16))) char smem[];
  LAS char* L = (LAS char*)smem;
  STAMP(0);
  unsigned pf = 0;
  if (blockIdx.x >= 160) {
    unsigned long long pc;
    unsigned off = (((blockIdx.x >> 3) & 1) * 256 + threadIdx.x) * 128;
    asm volatile("s_getpc_b64 %1\n.Lfused_pc:\n\tv_min_u32 %2, .Lfused_end-.Lfused_pc, %2\n\tglobal_load_dword %0, %2, %1"
                 : "=&v"(pf), "=&s"(pc), "+v"(off));
  }
  PIN();
  typedef void* const __attribute__((address_space(4))) * karg_t;
  const karg_t ka = (karg_t)__builtin_amdgcn_kernarg_segment_ptr();
  void *gq = ka[0], *gk = ka[1], *gv = ka[2], *gsk = ka[3], *gssm = ka[4], *gD = ka[5], *gw1 = ka[6], *gw2 = ka[7], *gw3 = ka[8],
       *gw4 = ka[9], *gc = ka[10], *gwm = ka[11], *gout = ka[12], *gws = ka[13];
  asm volatile("" ::"s"(gq), "s"(gk), "s"(gv), "s"(gsk), "s"(gssm), "s"(gD), "s"(gw1), "s"(gw2), "s"(gw3), "s"(gw4),
               "s"(gc), "s"(gwm), "s"(gout), "s"(gws));
  const int tid = threadIdx.x, lane = tid & 63;
  const int w = __builtin_amdgcn_readfirstlane(tid >> 6);
  const int b = blockIdx.x, x = b & 7, j = b >> 3;
  const int tm = 4 * x + (j >> 3), tn = j & 7;
  const int rg = 2 * x + (j >> 4), cg = j & 15;
  const bool conv_wg = j < 20;
  const rsrc_t ws = mkrs(gws, O_END);
  const int g = lane >> 4, l15 = lane & 15;

  const int rt0 = 64 * tm + 8 * tn + 2 * w;
  const int rcm = 8 * lane, rco = 512 + 2 * lane;
  v4u qm[2];
  unsigned qo[2];
  auto load_q = [&]() {
    const rsrc_t rq = mkrs(gq, SD2);
#pragma unroll
    for (int t = 0; t < 2; ++t) {
      qm[t] = ld128(rq, rcm * 2, (rt0 + t) * D_ * 2);
      qo[t] = ld32(rq, rco * 2, (rt0 + t) * D_ * 2);
    }
  };
  v2u tag;
  auto make_tag = [&](v2u ep) {
    tag.x = __builtin_amdgcn_readfirstlane(ep.x) * 0x9E3779B1u + 0x7F4A7C15u;
    tag.y = (__builtin_amdgcn_readfirstlane(ep.y) ^ tag.x) * 0x85EBCA77u + 0x2545F491u;
    tag.x &= 0xffffff00u;
  };
  v4u b1[25], b2[25];
  const unsigned sb1 = O_W1 + (tn * 100 + w * 25) * 1024, sb2 = O_W2 + (tn * 100 + w * 25) * 1024;
  auto zero_h1 = [&](int i) {
    hst128(v4u{0u, 0u, 0u, 0u}, ws, (i < 2 || tid < 128) ? tid * 16 : OOB, O_H1 + b * 10240 + i * 4096);
  };
  v4u mm[5];
  unsigned mo[5];
  auto load_mod = [&](bool ok) {
    unsigned o1 = (ok ? 0u : OOB) | (lane * 16), o2 = (ok ? 0u : OOB) | (lane * 4 + 1024);
    asm volatile("" : "+v"(o1), "+v"(o2));
#pragma unroll
    for (int i = 0; i < 5; ++i) {
      mm[i] = hld128(ws, o1, O_MOD + i * D_ * 2);
      mo[i] = __builtin_amdgcn_raw_buffer_load_b32(ws, o2, O_MOD + i * D_ * 2, 16);
    }
  };
  f2 qv[10], n1[10];
  auto ln1 = [&]() {
#pragma unroll
    for (int i = 0; i < 4; ++i) {
      qv[2 * i] = f2{lo2f(qm[0][i]), lo2f(qm[1][i])};
      qv[2 * i + 1] = f2{hi2f(qm[0][i]), hi2f(qm[1][i])};
    }
    qv[8] = f2{lo2f(qo[0]), lo2f(qo[1])};
    qv[9] = f2{hi2f(qo[0]), hi2f(qo[1])};
    f2 s = qv[0], ss = qv[0] * qv[0];
#pragma unroll
    for (int i = 1; i < 10; ++i) { s += qv[i]; ss += qv[i] * qv[i]; }
    wave_sum4(s, ss);
    f2 r, mr;
    ln_scal(s, ss, r, mr);
#pragma unroll
    for (int i = 0; i < 10; ++i) n1[i] = qv[i] * r + mr;
  };
  bool wok = false, bok = false;
  v4u bfl;
  if (conv_wg) {
    const int cgp = j < 16 ? x : 8 + (x >> 2), ci = j < 16 ? j : (x & 3) * 4 + (j - 16);
    const int pT0 = 128 * ci + 32 * w + 8 * ((lane >> 3) & 3) + 4 * (lane >> 5), pC0 = 64 * cgp + 8 * (lane & 7);
    const int ch = 64 * cgp + 4 * ci + w;
    v4u kw[7], vw[4], skw[4], kr[4];
    {
      const rsrc_t rk = mkrs(gk, SD2), rv = mkrs(gv, SD2), rs = mkrs(gsk, D_ * 8);
      const unsigned o = (unsigned)(pT0 * D_ + pC0) * 2;
#pragma unroll
      for (int i = 0; i < 7; ++i) kw[i] = ld128(rk, o + (i - 3) * D_ * 2, 0);
#pragma unroll
      for (int i = 0; i < 4; ++i) vw[i] = ld128(rv, o + i * D_ * 2, 0);
#pragma unroll
      for (int i = 0; i < 4; ++i) skw[i] = __builtin_amdgcn_raw_buffer_load_b128(rs, pC0 * 8 + i * 16, 0, 0);
      PIN();
      asm volatile("s_waitcnt vmcnt(4)" ::: "memory");
      const rsrc_t rK = mkrs((const char*)gssm + (size_t)ch * 4096, 4096);
#pragma unroll
      for (int i = 0; i < 4; ++i) kr[i] = ld128(rK, lane * 16 + i * 1024, 0);
    }
    const v2u ep = hld64(ws, lane == 0 ? O_EPOCH : O_FP + x * 256 + (j >= 16) * 128, 0);
    const unsigned dvb = __builtin_amdgcn_raw_buffer_load_b16(mkrs(gD, D_ * 2), ch * 2, 0, 0);
    PIN();
    {
      unsigned pe[8][2];
#pragma unroll
      for (int pr = 0; pr < 4; ++pr) {
        const f2 w0 = {lo2f(skw[pr].x), lo2f(skw[pr].z)}, w1 = {hi2f(skw[pr].x), hi2f(skw[pr].z)};
        const f2 w2 = {lo2f(skw[pr].y), lo2f(skw[pr].w)}, w3 = {hi2f(skw[pr].y), hi2f(skw[pr].w)};
        f2 kf[7], o[4];
#pragma unroll
        for (int i = 0; i < 7; ++i) kf[i] = up2(kw[i][pr]);
#pragma unroll
        for (int i = 0; i < 4; ++i) o[i] = (kf[i + 3] * w0 + kf[i + 2] * w1 + kf[i + 1] * w2 + kf[i] * w3) * up2(vw[i][pr]);
        pe[2 * pr][0] = pk2(o[3].x, o[2].x);
        pe[2 * pr][1] = pk2(o[1].x, o[0].x);
        pe[2 * pr + 1][0] = pk2(o[3].y, o[2].y);
        pe[2 * pr + 1][1] = pk2(o[1].y, o[0].y);
      }
      const unsigned so = (pC0 + 4 * (lane >> 5)) * KVP + (pT0 & ~7) * 2;
#pragma unroll
      for (int e = 0; e < 4; ++e) {
        const auto s0 = __builtin_amdgcn_permlane32_swap(pe[e][0], pe[e + 4][0], false, false);
        const auto s1 = __builtin_amdgcn_permlane32_swap(pe[e][1], pe[e + 4][1], false, false);
        hst128(v4u{s0[1], s1[1], s0[0], s1[0]}, ws, so, O_KVT + e * KVP);
      }
    }
    STAMP(1);
    PIN();
    LAS char* T = L + w * WR;
    if (lane < 8) *(LAS v4u*)(T + lane * CS) = v4u{0u, 0u, 0u, 0u};
#pragma unroll
    for (int i = 0; i < 4; ++i) *(LAS v4u*)(T + 7 * CS + (i * 64 + lane + 1) * 16) = kr[i];
    asm volatile("s_waitcnt lgkmcnt(0)" ::: "memory");
    v4u kl[4];
#pragma unroll
    for (int i = 0; i < 4; ++i) kl[i] = *(LAS v4u*)(T + 7 * CS + (i * 64 + lane) * 16);
    auto tbuild = [&](int i) {
      const unsigned d[8] = {kl[i].x, kl[i].y, kl[i].z, kl[i].w, kr[i].x, kr[i].y, kr[i].z, kr[i].w};
      unsigned a[7];
#pragma unroll
      for (int e = 0; e < 7; ++e) a[e] = __builtin_amdgcn_alignbit(d[e + 1], d[e], 16);
#pragma unroll
      for (int m = 0; m < 7; ++m) {
        const int h = (m + 1) >> 1;
        const v4u o = (m & 1) ? v4u{d[h], d[h + 1], d[h + 2], d[h + 3]} : v4u{a[h], a[h + 1], a[h + 2], a[h + 3]};
        *(LAS v4u*)(T + m * CS + (i * 64 + lane + 1) * 16) = o;
      }
    };
    tbuild(0);
    make_tag(ep);
    DRAIN();
    asm volatile("s_barrier" ::: "memory");
    if (w == 0) {
      unsigned o = OOB;
      if (j < 16 && lane == 0) o = O_FP + x * 256 + ci * 8;
      if (j >= 16 && lane < 4) o = O_FP + ((x & 4) + lane) * 256 + 128 + ci * 8;
      hst64(tag, ws, o, 0);
    }
    STAMP(2);
    tbuild(1);
    tbuild(2);
    tbuild(3);
    if (lane < 32) *(LAS v4u*)(T + UOFF + lane * 16) = v4u{0u, 0u, 0u, 0u};
    STAMP(3);
    if (w == 0) poll64(ws, lane < 16 ? O_FP + x * 256 + (j >= 16) * 128 + lane * 8 : OOB, tag);
    WGBAR();
    STAMP(4);
    const v4u wf = hld128(ws, O_FW + x * 1024 + lane * 16, 0);
    v4u ur[4];
#pragma unroll
    for (int i = 0; i < 4; ++i) ur[i] = hld128(ws, lane * 16 + i * 1024, O_KVT + ch * KVP);
    const LAS char* pa = T + (l15 & 7) * CS + (3 - g + (l15 >> 3)) * 16;
    const LAS char* pv = T + UOFF + 512 + (l15 - 1) * 32 + g * 16;
    v4u fa[2][8], fv[2][8];
#pragma unroll
    for (int a = 0; a < 8; ++a) fa[0][a] = *(const LAS v4u*)(pa + 8 * a * 64);
    f4 acc[8];
    v4u ad;
    {
      const int jd = 8 * g - 9 - l15;
#pragma unroll
      for (int e = 0; e < 4; ++e) ad[e] = jd == 2 * e ? dvb : (jd == 2 * e + 1 ? dvb << 16 : 0u);
      asm volatile("" : "+v"(ad));
    }
    PIN();
#pragma unroll
    for (int i = 0; i < 4; ++i) *(LAS v4u*)(T + UOFF + 512 + i * 1024 + lane * 16) = ur[i];
    asm volatile("s_waitcnt lgkmcnt(0)" ::: "memory");
#pragma unroll
    for (int a = 0; a < 8; ++a) fv[0][a] = *(const LAS v4u*)(pv + a * 512);
    wok = wflags_ok(wf, lane, 40, tag);
    __builtin_amdgcn_fence(__ATOMIC_ACQUIRE, "wavefront");
    const unsigned boff = wok ? lane * 16 : OOB;
    STAMP(5);
#pragma unroll
    for (int q = 0; q < 8; ++q) mfma_v0(acc[q], ad, fv[0][q]);
    PIN();
#pragma unroll
    for (int g0 = 0; g0 < 8; ++g0) {
      int n = 0;
      if (g0 == 0) load_q();
#pragma unroll
      for (int a = 0; a < 8; ++a)
#pragma unroll
        for (int q = a; q < 8; ++q) {
          mfma_v(acc[q], fa[g0 & 1][a], fv[g0 & 1][q - a]);
          ++n;
          if ((n & 1) == 0) {
            const int e = n / 2 - 1;
            if (g0 < 7 && e < 16) {
              if (e < 8) fv[(g0 + 1) & 1][e] = *(const LAS v4u*)(pv + e * 512 - (g0 + 1) * 64);
              else fa[(g0 + 1) & 1][e - 8] = *(const LAS v4u*)(pa + (g0 + 1 + 8 * (e - 8)) * 64);
            }
            PIN();
          }
          if (n % 5 == 0) {
            const int i = g0 * 7 + n / 5 - 13;
            if (i < 0) {
              if (i < -9) zero_h1(i + 12);
            } else if (i < 25) b1[i] = hld128(ws, boff, sb1 + i * 1024);
            else if (i < 50) b2[i - 25] = hld128(ws, boff, sb2 + (i - 25) * 1024);
            PIN();
          }
        }
    }
    MFENCE();
    PIN();
    STAMP(6);
#pragma unroll
    for (int q = 0; q < 8; q += 2) {
      const auto s0 = __builtin_amdgcn_permlane16_swap(pk2(acc[q][0], acc[q][1]), pk2(acc[q + 1][0], acc[q + 1][1]), false, false);
      const auto s1 = __builtin_amdgcn_permlane16_swap(pk2(acc[q][2], acc[q][3]), pk2(acc[q + 1][2], acc[q + 1][3]), false, false);
      *(LAS v4u*)(T + 64 * w + (32 * (q + (g & 1)) + 2 * l15 + (g >> 1)) * 16) = v4u{s0[0], s1[0], s0[1], s1[1]};
    }
    WGBAR();
    {
      bfl = hld128(ws, O_FB + x * 1024 + lane * 16, 0);
      v4u zo[4];
#pragma unroll
      for (int i = 0; i < 4; ++i) zo[i] = *(LAS v4u*)(L + (lane & 3) * (WR + 64) + (64 * w + 16 * i + (lane >> 2)) * 16);
#pragma unroll
      for (int i = 0; i < 4; ++i) hst128(zo[i], ws, (lane >> 2) * ZP + (lane & 3) * 16, O_ZT + (64 * w + 16 * i) * ZP + (ch - w) * 16);
    }
    PIN();
    ln1();
    PIN();
    DRAIN();
  } else {
    const int hb = (j - 20) * 8 + x;
    const Job j0 = job_desc(hb, w, gw1, gw2, gw3, gw4);
    v4u r0[7];
    job_load(j0, lane, r0);
    const v2u ep = hld64(ws, O_EPOCH, 0);
    auto first_job = [&]() {
      job_put(j0, w, lane, r0, L);
      make_tag(ep);
      WGBAR();
      job_emit(j0, w, lane, L, ws);
      if (j == 20 && w == 0) hst128(v4u{tag.x, tag.y, 0u, 0u}, ws, (lane >> 3) * 1024 + (lane & 7) * 16, O_CNT + x * 128);
      asm volatile("" ::"v"(pf) : "memory");
      STAMP(1);
    };
    auto first_pub = [&]() {
      WGBAR();
      if (w == 0) hst64(tag, ws, lane < 8 ? O_FW + lane * 1024 + hb * 8 : OOB, 0);
      STAMP(2);
    };
    const v2u cw = __builtin_amdgcn_raw_buffer_load_b64(mkrs(gc, D_ * 2), tid * 8, 0, 0);
    PIN();
    Job jb[4];
#pragma unroll
    for (int i = 0; i < 4; ++i) jb[i] = job_desc(hb < 60 ? 240 + (hb & 31) : 96 + (hb - 60) + 36 * i, w, gw1, gw2, gw3, gw4);
    LAS float* sc = (LAS float*)(L + 4 * RT_BYTES);
    {
      const float c4[4] = {lo2f(cw.x), hi2f(cw.x), lo2f(cw.y), hi2f(cw.y)};
      f4 sv;
#pragma unroll
      for (int e = 0; e < 4; ++e) sv[e] = rbf(c4[e] * __builtin_amdgcn_rcpf(1.f + __builtin_amdgcn_exp2f(-1.4426950408889634f * c4[e])));
      *(LAS f4*)(sc + tid * 4) = sv;
    }
    first_job();
    if (hb < 60) {
      const rsrc_t rm = mkrs(gwm, D_ * 6 * D_ * 2);
      v4u wm[20], r1[7];
      YOUNG();
#pragma unroll
      for (int i = 0; i < 8; ++i) wm[i] = ld128(rm, (tid >> 3) * (6 * D_ * 2) + (tid & 7) * 16, hb * 128 + i * 32 * (6 * D_ * 2));
      PIN();
      WAITV(8);
      first_pub();
#pragma unroll
      for (int i = 8; i < 20; ++i) wm[i] = ld128(rm, (tid >> 3) * (6 * D_ * 2) + (tid & 7) * 16, hb * 128 + i * 32 * (6 * D_ * 2));
      const Job& j1 = jb[0];
      if (hb < 32) job_load(j1, lane, r1);
      load_q();
      float sl[20];
#pragma unroll
      for (int i = 0; i < 20; ++i) sl[i] = sc[(tid >> 3) + 32 * i];
      PIN();
      f2 a[4] = {f2{0.f, 0.f}, f2{0.f, 0.f}, f2{0.f, 0.f}, f2{0.f, 0.f}};
#pragma unroll
      for (int i = 0; i < 20; ++i)
#pragma unroll
        for (int e = 0; e < 4; ++e) a[e] += up2(wm[i][e]) * sl[i];
      LAS char* mred = L + 4 * RT_BYTES + 4096;
      *(LAS f4*)(mred + (tid >> 3) * 272 + (tid & 7) * 32) = f4{a[0].x, a[0].y, a[1].x, a[1].y};
      *(LAS f4*)(mred + (tid >> 3) * 272 + (tid & 7) * 32 + 16) = f4{a[2].x, a[2].y, a[3].x, a[3].y};
      if (hb < 32) job_put(j1, w, lane, r1, L);
      WGBAR();
      STAMP(3);
      {
        const int col = tid >> 2, part = tid & 3;
        float v[8];
#pragma unroll
        for (int r = 0; r < 8; ++r) v[r] = *(LAS float*)(mred + (part * 8 + r) * 272 + col * 4);
        float s = ((v[0] + v[1]) + (v[2] + v[3])) + ((v[4] + v[5]) + (v[6] + v[7]));
        s += dpp<0xB1>(s);
        s += dpp<0x4E>(s);
        s = rbf(s);
        if ((hb >= 10 && hb < 20) || (hb >= 40 && hb < 50)) s = rbf(1.f + s);
        if (part == 0) *((LAS unsigned short*)sc + 8 + col) = (unsigned short)(__builtin_bit_cast(unsigned, s) >> 16);
      }
      WGBAR();
      if (w == 0) {
        hst128(*(LAS v4u*)((LAS char*)sc + 16 + (lane & 7) * 16), ws, lane < 8 ? lane * 16 : OOB, O_MOD + hb * 128);
        DRAIN();
        hst64(tag, ws, lane < 8 ? O_FB + lane * 1024 + hb * 8 : OOB, 0);
      }
      if (hb < 32) job_emit(j1, w, lane, L, ws);
    } else {
      v4u rr[4][7];
      DRAIN();
      first_pub();
#pragma unroll
      for (int i = 0; i < 3; ++i) job_load(jb[i], lane, rr[i]);
      PIN();
#pragma unroll
      for (int i = 0; i < 2; ++i) job_put(jb[i], w, lane, rr[i], L + i * RT_BYTES);
      WGBAR();
#pragma unroll
      for (int i = 0; i < 2; ++i) job_emit(jb[i], w, lane, L + i * RT_BYTES, ws);
      YOUNG();
      job_load(jb[3], lane, rr[3]);
      load_q();
      PIN();
      job_put(jb[2], w, lane, rr[2], L + 2 * RT_BYTES);
      WAITV(9);
      WGBAR();
      if (w == 0) hst64(tag, ws, lane < 8 ? O_FB + lane * 1024 + hb * 8 : OOB, 0);
      STAMP(3);
      job_emit(jb[2], w, lane, L + 2 * RT_BYTES, ws);
      job_put(jb[3], w, lane, rr[3], L);
      WGBAR();
      job_emit(jb[3], w, lane, L, ws);
    }
    zero_h1(0);
    zero_h1(1);
    zero_h1(2);
    if (hb >= 34 && hb < 60) hst128(v4u{0u, 0u, 0u, 0u}, ws, w == 0 ? lane * 16 : OOB, O_FR + (hb - 34) * 1024);
    STAMP(4);
    ln1();
    DRAIN();
  }
  if (!wok) pollw(ws, O_FW + x * 1024 + lane * 16, lane, 40, tag);
  WGBAR();
  STAMP(7);
  if (w == 0 && lane < 8)
    __hip_atomic_fetch_add((GAS unsigned*)((GAS char*)gws + O_CNT + (lane * 8 + x) * 128), 1u, __ATOMIC_RELAXED, __HIP_MEMORY_SCOPE_AGENT);
  if (conv_wg) {
    bok = wflags_ok(bfl, lane, 48, tag);
    __builtin_amdgcn_fence(__ATOMIC_ACQUIRE, "wavefront");
    load_mod(bok);
  }
  if (!wok) {
#pragma unroll
    for (int i = 0; i < 25; ++i) b1[i] = hld128(ws, lane * 16, sb1 + i * 1024);
#pragma unroll
    for (int i = 0; i < 19; ++i) b2[i] = hld128(ws, lane * 16, sb2 + i * 1024);
  }
#pragma unroll
  for (int i = 19; i < 25; ++i) b2[i] = hld128(ws, lane * 16, sb2 + i * 1024);
  v4u b3[35], b4[70];
  const unsigned sb3 = O_W3 + (cg * 140 + w * 35) * 1024, sb4 = O_W4 + (tn * 280 + w * 70) * 1024;
#define LB3(lo, hi) _Pragma("unroll") for (int i_ = lo; i_ < hi; ++i_) b3[i_] = hld128(ws, lane * 16 + (i_ & 3) * 1024, sb3 + (i_ >> 2) * 4096)
  auto ks4 = [](int s) { return s < 8 ? 7 * (s >> 2) + (s & 1) + 4 * ((s >> 1) & 1) : 7 * ((s - 8) / 3) + ((s - 8) % 3 == 2 ? 6 : 2 + (s - 8) % 3); };
  auto lb4 = [&](int i) {
    const int f = ks4(i / 5) * 5 + i % 5;
    b4[i] = hld128(ws, lane * 16 + (f & 3) * 1024, sb4 + (f >> 2) * 4096);
  };
#define LB4(lo, hi) _Pragma("unroll") for (int i_ = lo; i_ < hi; ++i_) lb4(i_)
  if (!conv_wg) {
    pollw(ws, O_FB + x * 1024 + lane * 16, lane, 48, tag);
    bok = true;
    load_mod(true);
  }
  constexpr int NB3C = 10;
  f2 gh[10];
  float cb1[10], sbv[10];
  auto md = [&](int v, int i) { const unsigned u = i < 8 ? mm[v][(i >> 1) & 3] : mo[v]; return (i & 1) ? hi2f(u) : lo2f(u); };
  auto use_mod = [&]() {
    LB3(0, NB3C);
#pragma unroll
    for (int i = 0; i < 10; ++i) {
      gh[i] = (n1[i] * md(1, i) + md(0, i)) * md(2, i);
      cb1[i] = md(4, i);
      sbv[i] = md(3, i);
      asm volatile("" : "+v"(gh[i]), "+v"(cb1[i]), "+v"(sbv[i]));
    }
  };
  if (bok) use_mod();
  unsigned z3off = tid < 128 ? tid * 16 + 8192 : OOB;
  unsigned im0 = RED_OFF + 16384 + lane * 128 + (((2 * w) ^ (lane & 7)) << 4);
  unsigned io0 = RED_OFF + 16384 + (64 + (lane >> 2)) * 128 + (((2 * w) ^ ((lane >> 2) & 7)) << 4) + 4 * (lane & 3);
  unsigned roff = O_A1 + ((4 * tm + (tn >> 1)) * 20 + (tid >> 5)) * 1024 + (((tid ^ (tid >> 3)) & 7) + 8 * (tn & 1) + 16 * ((tid >> 3) & 3)) * 16;
  unsigned roff3 = tid < 128 ? roff : OOB;
  const unsigned own_m = ((4 * tm + w) * 20 + 5 * (tn >> 1) + 3 * (tn & 1)) * 1024;
  const unsigned own_s = ((4 * tm + w) * 20 + 5 * (tn >> 1) + 2) * 1024 + (tn & 1) * 512;
  unsigned own_r = (l15 + 16 * (g >> 1)) * 16 + 8 * (g & 1), own_w = (g & 1) ? OOB : (l15 + 16 * (g >> 1)) * 16;
  const int sh = (tn >> 1) == 0 ? 1 : ((tn >> 1) == 1 ? -1 : ((tn >> 1) == 2 ? 2 : -2));
  unsigned h1m, h1s, f1pub = OOB, f1wait;
  {
    const int t2 = 64 * tm + 16 * w + l15 + sh;
    const bool ok = t2 >= 0 && t2 < S_;
    const unsigned d = ((t2 >> 4) * 20 + 5 * (tn >> 1)) * 1024 + (t2 & 15) * 16;
    h1m = ok ? d + 3072 * (tn & 1) + 256 * g : OOB;
    h1s = (ok && !(g & 1)) ? d + 2048 + 512 * (tn & 1) + 256 * (g >> 1) : OOB;
    const int nb = tm + (sh > 0 ? 1 : -1);
    if (lane == 0) f1pub = O_F1 + tm * 128 + tn * 8;
    if (lane == 1 && nb >= 0 && nb < 32) f1pub = O_F1 + nb * 128 + 64 + tn * 8;
    const bool up = ((lane >> 1) & 1) == 0;
    f1wait = (lane < 8 || (lane < 16 && (up ? tm > 0 : tm < 31))) ? O_F1 + tm * 128 + lane * 8 : OOB;
  }
  unsigned frpub = lane == 0 ? O_FR + tm * 128 + tn * 8 : OOB, frwait = lane < 8 ? O_FR + tm * 128 + lane * 8 : OOB;
  unsigned f2pub = lane == 0 ? O_F2 + (tm >> 1) * 128 + ((tm & 1) * 8 + tn) * 8 : OOB, f2wait = lane < 16 ? O_F2 + rg * 128 + lane * 8 : OOB;
  asm volatile("" : "+v"(frpub), "+v"(frwait), "+v"(f2pub), "+v"(f2wait));
  asm volatile("" : "+v"(z3off), "+v"(im0), "+v"(io0), "+v"(roff), "+v"(roff3), "+v"(own_r), "+v"(own_w), "+v"(h1m), "+v"(h1s), "+v"(f1pub), "+v"(f1wait));
  PIN();
  if (w == 0) {
    poll64(ws, lane < 8 ? O_CNT + (x * 8 + lane) * 128 : OOB, v2u{tag.x + 32, tag.y});
    if (b == 0) hst64(tag, ws, lane == 0 ? O_EPOCH : OOB, 0);
  }
  WGBAR();
  STAMP(8);

  v4u hx[6];
  f4 csp[7];
  auto img3 = [&](int lo, int hi, int hc) {
    const v4u ones = {0x3f803f80u, 0x3f803f80u, 0x3f803f80u, 0x3f803f80u};
    unsigned la = w * 25600 + lane * 16, lb = (75 + w * 10) * 1024 + lane * 16;
    asm volatile("" : "+v"(la), "+v"(lb));
#pragma unroll
    for (int i = lo; i < 35; ++i) {
      if (i < hi) *(LAS v4u*)(L + (i < 25 ? la : lb) + i * 1024) = b3[i];
      if (i < hc) csp[i % 7] = mfma(b3[i], ones, i < 7 ? f4{0.f, 0.f, 0.f, 0.f} : csp[i % 7]);
      PIN();
    }
  };
  {
    v4u zc[3];
#pragma unroll
    for (int i = 0; i < 3; ++i) zc[i] = hld128(ws, i < 2 ? tid * 16 + i * 4096 : z3off, O_ZT + (8 * tm + tn) * ZP);
    if (!bok) {
      load_mod(true);
      use_mod();
    }
    LB3(10, 17);
    PIN();
#pragma unroll
    for (int i = 0; i < 3; ++i)
#pragma unroll
      for (int e = 0; e < 4; ++e) *(LAS unsigned*)(L + RED_OFF + e * 3072 + (tid + 256 * i) * 4) = zc[i][e];
    STAMP(9);
    WGBAR();
    STAMP(10);
    const v4u z0 = *(LAS v4u*)(L + RED_OFF + w * 3072 + rcm * 4), z1 = *(LAS v4u*)(L + RED_OFF + w * 3072 + rcm * 4 + 16);
    const v2u z2 = *(LAS v2u*)(L + RED_OFF + w * 3072 + rco * 4);
    LB3(17, 23);
    f2 xv[10], hv[10], av[10], s, ss, r, mr;
#pragma unroll
    for (int i = 0; i < 10; ++i) {
      xv[i] = gh[i] * up2(i < 4 ? z0[i & 3] : (i < 8 ? z1[i & 3] : z2[i & 1])) + qv[i];
      s = i ? s + xv[i] : xv[i];
      ss = i ? ss + xv[i] * xv[i] : xv[i] * xv[i];
    }
    wave_sum4(s, ss);
    ln_scal(s, ss, r, mr);
#pragma unroll
    for (int i = 0; i < 10; ++i) {
      hv[i] = (xv[i] * r + mr) * cb1[i] + sbv[i];
      s = i ? s + hv[i] : hv[i];
      ss = i ? ss + hv[i] * hv[i] : hv[i] * hv[i];
    }
    wave_sum4(s, ss);
    ln_scal(s, ss, r, mr);
#pragma unroll
    for (int i = 0; i < 10; ++i) av[i] = hv[i] * r + mr;
    auto put = [&](const f2(&v)[10], int img) {
      *(LAS v4u*)(L + im0 + img * 10240) = v4u{pk2(v[0].x, v[1].x), pk2(v[2].x, v[3].x), pk2(v[4].x, v[5].x), pk2(v[6].x, v[7].x)};
      *(LAS v4u*)(L + (im0 ^ 16) + img * 10240) = v4u{pk2(v[0].y, v[1].y), pk2(v[2].y, v[3].y), pk2(v[4].y, v[5].y), pk2(v[6].y, v[7].y)};
      *(LAS unsigned*)(L + io0 + img * 10240) = pk2(v[8].x, v[9].x);
      *(LAS unsigned*)(L + (io0 ^ 16) + img * 10240) = pk2(v[8].y, v[9].y);
    };
    put(av, 0);
    STAMP(11);
    WGBAR();
    {
      v4u oc[3];
#pragma unroll
      for (int i = 0; i < 3; ++i) oc[i] = *(LAS v4u*)(L + RED_OFF + 16384 + i * 4096 + tid * 16);
#pragma unroll
      for (int i = 0; i < 3; ++i) hst128(oc[i], ws, i < 2 ? roff : roff3, i * 8192);
    }
    PIN();
    put(hv, 1);
    put(xv, 2);
    STAMP(12);
    DRAIN();
    WGBAR();
    STAMP(13);
    if (w == 0) hst64(tag, ws, frpub, 0);
#pragma unroll
    for (int i = 0; i < 6; ++i) hx[i] = *(LAS v4u*)(L + RED_OFF + 26624 + (i / 3) * 10240 + (i % 3) * 4096 + tid * 16);
    img3(0, 16, 16);
    if (w == 0) poll64(ws, frwait, tag);
    WGBAR();
    STAMP(14);
  }

  {
    f4 acc[5];
    gemm_ks<5, 12>(ws, O_A1 + (4 * tm * 20 + 5 * w) * 1024, 20 * 1024, b1, L, w, lane, acc,
                   [&](int n) {
                     if (n < 6) hst128(hx[n], ws, (n % 3) < 2 ? roff : roff3, (1 + n / 3) * SD2 + (n % 3) * 8192);
                     b3[23 + n] = hld128(ws, lane * 16, sb3 + (23 + n) * 1024);
                   }, [&]() { LB4(0, 4); }, [](int) {} STPASS(15));
    STAMP(18);
    f2 v[10];
    unsigned o[10];
#pragma unroll
    for (int t = 0; t < 5; ++t) {
      v[2 * t] = f2{acc[t][0], acc[t][1]};
      v[2 * t + 1] = f2{acc[t][2], acc[t][3]};
    }
    gelu_pack<10>(v, 1.f, 1.f, o, [](int) {});
    const auto s0 = __builtin_amdgcn_permlane16_swap(o[8], o[8], false, false);
    const auto s1 = __builtin_amdgcn_permlane16_swap(o[9], o[9], false, false);
    hst128(v4u{o[0], o[1], o[2], o[3]}, ws, h1m, O_H1);
    hst128(v4u{o[4], o[5], o[6], o[7]}, ws, h1m + 1024, O_H1);
    hst128(v4u{s0[0], s1[0], s0[1], s1[1]}, ws, h1s, O_H1);
    STAMP(19);
    DRAIN();
    WGBAR();
    STAMP(20);
    if (w == 0) hst64(tag, ws, f1pub, 0);
    img3(16, 24, 35);
    if (l15 == 0) {
#pragma unroll
      for (int t = 0; t < 7; ++t) *(LAS f4*)(L + (w * 25 + 24) * 1024 + (t * 4 + g) * 16) = csp[t];
    }
    if (w == 0) poll64(ws, f1wait, tag);
    WGBAR();
    STAMP(21);
  }
  v4u xr[2];
  v2u xro;
  unsigned x1p[10];
  {
    v4u hr[2];
    v2u hro;
    f4 acc[5];
    gemm_ks<5, 12>(ws, O_H1 + (4 * tm * 20 + 5 * w) * 1024, 20 * 1024, b2, L, w, lane, acc, [&](int n) {
      if (n == 0) hr[0] = hld128(ws, lane * 16, O_HP + own_m);
      else if (n == 1) hr[1] = hld128(ws, lane * 16 + 1024, O_HP + own_m);
      else if (n == 2) hro = hld64(ws, own_r, O_HP + own_s);
      else {
        lb4(2 * n - 2);
        lb4(2 * n - 1);
      }
    }, [&]() { lb4(22); }, [](int) {} STPASS(22));
#pragma unroll
    for (int t = 0; t < 5; ++t) {
      const unsigned h0 = t < 4 ? hr[t >> 1][2 * (t & 1)] : hro.x, h1 = t < 4 ? hr[t >> 1][2 * (t & 1) + 1] : hro.y;
      const f2 p = f2{acc[t][0], acc[t][1]} + up2(h0), q = f2{acc[t][2], acc[t][3]} + up2(h1);
      x1p[2 * t] = pk2(p.x, p.y);
      x1p[2 * t + 1] = pk2(q.x, q.y);
    }
    const auto s0 = __builtin_amdgcn_permlane16_swap(x1p[8], x1p[8], false, false);
    const auto s1 = __builtin_amdgcn_permlane16_swap(x1p[9], x1p[9], false, false);
    hst128(v4u{x1p[0], x1p[1], x1p[2], x1p[3]}, ws, lane * 16, O_X1 + own_m);
    hst128(v4u{x1p[4], x1p[5], x1p[6], x1p[7]}, ws, lane * 16 + 1024, O_X1 + own_m);
    hst128(v4u{s0[0], s1[0], s0[1], s1[1]}, ws, own_w, O_X1 + own_s);
    float t1 = 0.f, t2 = 0.f;
#pragma unroll
    for (int i = 0; i < 10; ++i) {
      t1 = __builtin_amdgcn_fdot2_f32_bf16(__builtin_bit_cast(bf2, x1p[i]), __builtin_bit_cast(bf2, 0x3f803f80u), t1, false);
      t2 = __builtin_amdgcn_fdot2_f32_bf16(__builtin_bit_cast(bf2, x1p[i]), __builtin_bit_cast(bf2, x1p[i]), t2, false);
    }
    t1 = xrow_sum(t1);
    t2 = xrow_sum(t2);
    hst64(v2u{__builtin_bit_cast(unsigned, t1), __builtin_bit_cast(unsigned, t2)}, ws, lane < 16 ? lane * 8 : OOB,
          O_STAT + ((4 * tm + w) * 8 + tn) * 128);
    STAMP(25);
    {
      f4 cp[28];
#pragma unroll
      for (int t = 0; t < 28; ++t) cp[t] = *(LAS f4*)(L + ((t / 7) * 25 + 24) * 1024 + ((t % 7) * 4 + g) * 16);
      PIN();
#pragma unroll
      for (int t = 0; t < 7; ++t) {
        csp[t] = (cp[t] + cp[7 + t]) + (cp[14 + t] + cp[21 + t]);
        KEEP(csp[t]);
      }
    }
    PIN();
    DRAIN();
    WGBAR();
    STAMP(26);
    if (w == 0) hst64(tag, ws, f2pub, 0);
    img3(24, 35, 0);
    if (w == 1 && l15 == 0) {
#pragma unroll
      for (int t = 0; t < 7; ++t) *(LAS f4*)(L + CSUM_OFF + (t * 4 + g) * 16) = csp[t];
    }
    if (w == 0) poll64(ws, f2wait, tag);
    WGBAR();
    STAMP(27);
  }
#define PUBK 10
#define PUBN 9
#define SMPN 23
#define CHKN 28
#define WIN4_ 10
  constexpr int WIN4 = WIN4_, RING4 = WIN4 + 1;
  unsigned f3pub = lane == 0 ? O_F3 + rg * 512 + (w >> 1) * 256 + cg * 16 + (w & 1) * 8 : OOB;
  unsigned f3wait = lane < 8 ? O_F3 + rg * 512 + (tm & 1) * 256 + w * 64 + lane * 8 : OOB;
  asm volatile("" : "+v"(f3pub), "+v"(f3wait));
  v4u a4[RING4];
  auto lda4 = [&](int n) {
    const int i = n < 32 ? n >> 3 : (n - 32) / 6, k = ks4(n < 32 ? n & 7 : 8 + (n - 32) % 6);
    a4[n % RING4] = hld128(ws, lane * 16 + (k & 3) * 1024, O_H + ((4 * tm + ((w + 1 + i) & 3)) * 56 + 14 * w + (k & ~3)) * 1024);
  };
  {
    constexpr int W3 = 6;
    v4u a[20][2];
    const unsigned sa = O_X1 + (8 * rg + 2 * w) * 20 * 1024;
    auto lda = [&](int k, int i) { a[k][i] = hld128(ws, lane * 16 + (k & 3) * 1024, sa + i * 20480 + (k >> 2) * 4096); };
#pragma unroll
    for (int k = 0; k < W3; ++k)
#pragma unroll
      for (int i = 0; i < 2; ++i) {
        lda(k, i);
        PIN();
      }
    f4 acc[2][7];
    v2u sv[2][2];
    f2 r, den, mean;
    auto bslot = [](int k, int t) { const int v = k / 5, i = (k % 5) * 7 + t; return (i < 25 ? v * 25 + i : 75 + v * 10 + i) * 1024; };
    const LAS char* Lb = L + lane * 16;
    const LAS char* Lb2 = L + lane * 16 + 65536;
    const LAS char* Lb3 = L + lane * 16 + 131072;
    auto rdb = [&](int k, int t) { const int o = bslot(k, t); return *(const LAS v4u*)(o < 65536 ? Lb + o : (o < 131072 ? Lb2 + (o - 65536) : Lb3 + (o - 131072))); };
    v4u bf[3][4];
#pragma unroll
    for (int t = 0; t < 8; ++t) {
      bf[t >> 2][t & 3] = rdb(t >> 2, t & 3);
      PIN();
    }
    sfor<0, 20>([&](auto ic) {
      constexpr int k = decltype(ic)::v;
      __builtin_amdgcn_s_waitcnt(k < 19 ? 0xC47F : 0xC37F);
#pragma unroll
      for (int t = 0; t < 4; ++t) {
        if (k == 0) mfma_va0(acc[0][t], bf[0][t], a[0][0]);
        else mfma_va(acc[0][t], bf[k % 3][t], a[k][0]);
        if (k < 18) bf[(k + 2) % 3][t] = rdb(k + 2, t);
        else if (t < 3) bf[(k + 2) % 3][t] = rdb(k - 18, 4 + t);
        PIN();
      }
#pragma unroll
      for (int t = 0; t < 4; ++t) {
        if (k == 0) mfma_va0(acc[1][t], bf[0][t], a[0][1]);
        else mfma_va(acc[1][t], bf[k % 3][t], a[k][1]);
        if (t < 2) {
          if constexpr (k + W3 < 20) lda(k + W3, t);
          else if constexpr (k + W3 < 22) sv[k + W3 - 20][t] = hld64(ws, l15 * 8 + g * 256 + t * 128, O_STAT + (8 * rg + 2 * w + k + W3 - 20) * 1024);
          else lb4(23 + 2 * (k + W3 - 22) + t);
        }
        PIN();
      }
      if (k == 0) STAMP(28);
    });
    STAMP(29);
    const unsigned hs = O_H + ((8 * rg + 2 * w) * 56 + 7 * (cg >> 1) + 4 * (cg & 1)) * 1024 + lane * 16;
    unsigned lcs = CSUM_OFF + g * 16;
    KEEP(lcs);
#pragma unroll
    for (int t = 0; t < 4; ++t) csp[t] = *(LAS f4*)(L + lcs + t * 64);
    MFENCE();
    PIN();
    {
      asm volatile("" : "+v"(sv[0][0]), "+v"(sv[0][1]), "+v"(sv[1][0]), "+v"(sv[1][1]));
      const f2 p0 = __builtin_bit_cast(f2, sv[0][0]) + __builtin_bit_cast(f2, sv[0][1]);
      const f2 p1 = __builtin_bit_cast(f2, sv[1][0]) + __builtin_bit_cast(f2, sv[1][1]);
      f2 mr;
      ln_scal(f2{xrow_sum(p0.x), xrow_sum(p1.x)}, f2{xrow_sum(p0.y), xrow_sum(p1.y)}, r, mr, &mean, &den);
    }
#pragma unroll
    for (int i = 0; i < 2; ++i) {
      f2 v[8];
      unsigned o[8];
#pragma unroll
      for (int e = 0; e < 8; ++e) {
        const int t = e >> 1, h = 2 * (e & 1);
        v[e] = f2{acc[i][t][h], acc[i][t][h + 1]} - f2{csp[t][h], csp[t][h + 1]} * mean[i];
      }
      gelu_pack<8>(v, r[i], den[i], o, [&](int m) {
        if (i == 0) { lb4(31 + 2 * m); lb4(32 + 2 * m); }
        else if (m == 0) lb4(39);
      });
      hst128(v4u{o[0], o[1], o[2], o[3]}, ws, hs, i * 57344);
      hst128(v4u{o[4], o[5], o[6], o[7]}, ws, hs, i * 57344 + 1024);
    }
    YOUNG();
    STAMP(30);
    sfor<0, 20>([&](auto ic) {
      constexpr int k = decltype(ic)::v;
      __builtin_amdgcn_s_waitcnt(k < 19 ? 0xC37F : 0xC07F);
      if (k == PUBK) {
        WAITV(12);
        hst64(tag, ws, f3pub, 0);
      }
#pragma unroll
      for (int e = 0; e < 6; ++e) {
        const int i = e / 3, t = e % 3;
        if (k == 0) mfma_va0(acc[i][4 + t], bf[2][t], a[0][i]);
        else mfma_va(acc[i][4 + t], bf[(k + 2) % 3][t], a[k][i]);
        if (i == 0 && k < 18) bf[(k + 1) % 3][t] = rdb(k + 2, 4 + t);
        if (k >= 1 && k < 6 && e == 5) lb4(39 + k);
        if (k >= 1 && k < 19 && e == 3) lb4(44 + k);
        PIN();
      }
    });
    v2u fl;
#pragma unroll
    for (int t = 4; t < 7; ++t) csp[t] = *(LAS f4*)(L + lcs + t * 64);
    MFENCE();
    PIN();
    STAMP(37);
    unsigned os[2][2];
#pragma unroll
    for (int i = 0; i < 2; ++i) {
      f2 v[6];
      unsigned o[6];
#pragma unroll
      for (int e = 0; e < 6; ++e) {
        const int t = 4 + (e >> 1), h = 2 * (e & 1);
        v[e] = f2{acc[i][t][h], acc[i][t][h + 1]} - f2{csp[t][h], csp[t][h + 1]} * mean[i];
      }
      gelu_pack<6>(v, r[i], den[i], o, [&](int m) {
        if (i == 0 && m == 1) fl = hld64(ws, f3wait, 0);
        if (i == 1 && m == 0) { LB4(63, 70); }
      });
      hst128(v4u{o[0], o[1], o[2], o[3]}, ws, hs, i * 57344 + 2048);
      os[i][0] = o[4];
      os[i][1] = o[5];
      if (i == 0) {
        PIN();
        KEEP(fl);
        if (!__all(f3wait >= OOB || (fl.x == tag.x && fl.y == tag.y))) poll64(ws, f3wait, tag);
        __builtin_amdgcn_fence(__ATOMIC_ACQUIRE, "wavefront");
        STAMP(32);
#pragma unroll
        for (int n = 0; n < WIN4; ++n) {
          lda4(n);
          PIN();
        }
      }
    }
    {
      const auto s0 = __builtin_amdgcn_permlane16_swap(os[0][0], os[1][0], false, false);
      const auto s1 = __builtin_amdgcn_permlane16_swap(os[0][1], os[1][1], false, false);
      hst128(v4u{s0[0], s1[0], s0[1], s1[1]}, ws, (g & 1) * 57344 + (l15 + 16 * (g >> 1)) * 16,
             O_H + ((8 * rg + 2 * w) * 56 + 7 * (cg >> 1) + 3) * 1024 + (cg & 1) * 512);
    }
    YOUNG();
    STAMP(31);
  }
  {
    v4u gb[2];
    v2u gbo, fl;
    f4 ac[4][5], p[15];
    f2 gf[10], cf[10];
    unsigned lq = lane;
    KEEP(lq);
    const unsigned lr = RED_OFF + lq * 16, gq4 = lq >> 4;
    unsigned dm = ((64 * tm + 16 * w + (lq & 15)) * D_ + 80 * tn) * 2, ds = dm + 8 * gq4 + ((tn & 1) ? 0 : 128);
    dm += 16 * gq4 + 32 * (tn & 1);
    sfor<0, 56>([&](auto ic) {
      constexpr int n = decltype(ic)::v, i = n < 32 ? n >> 3 : (n - 32) / 6, sl = n < 32 ? n & 7 : 8 + (n - 32) % 6;
      if (n == 52) {
        STAMP(35);
        WGBAR();
        STAMP(36);
      }
      if (n == PUBN) {
        WAITV(8);
        hst64(tag, ws, f3pub + (O_F4 - O_F3), 0);
        STAMP(33);
      }
      if (n == 40) asm volatile("" : "+v"(dm), "+v"(ds));
      if (n == 37) asm volatile("s_barrier" ::: "memory");
      if (n == CHKN) {
        KEEP(fl);
        if (!__all(f3wait >= OOB || (fl.x == tag.x && fl.y == tag.y))) poll64(ws, f3wait + (O_F4 - O_F3), tag);
        __builtin_amdgcn_fence(__ATOMIC_ACQUIRE, "wavefront");
#pragma unroll
        for (int m = 32; m < CHKN + WIN4; ++m) lda4(m);
        PIN();
      }
#pragma unroll
      for (int t = 0; t < 5; ++t) {
        const int f = sl * 5 + t;
        if (sl == 0) mfma_av0(ac[i][t], b4[f], a4[n % RING4]);
        else if (f >= 23 && f < 40) mfma_v(ac[i][t], b4[f], a4[n % RING4]);
        else mfma_av(ac[i][t], b4[f], a4[n % RING4]);
        if (t == 1) {
          if constexpr (n + WIN4 < 56) {
            if (n < 32 - WIN4 || n >= CHKN) lda4(n + WIN4);
          }
        }
        if (t == 3) {
          if (n == 28 || n == 29) gb[n & 1] = hld128(ws, gq4 * 16 + (n & 1) * 64, O_MOD + (5 * D_ + 80 * tn + 16 * (tn & 1)) * 2);
          if (n == 30) gbo = hld64(ws, gq4 * 8, O_MOD + (5 * D_ + 80 * tn + ((tn & 1) ? 0 : 64)) * 2);
          if (n == 31 || n == 32) xr[n & 1] = hld128(ws, lq * 16 + (n & 1) * 1024, O_X + own_m);
          if (n == 33) xro = hld64(ws, ((lq & 15) + 16 * (gq4 >> 1)) * 16 + 8 * (gq4 & 1), O_X + own_s);
        }
        if (n >= 45 && n < 50 && t < 4) {
          const int e = (n - 45) * 4 + t, q = e >> 1;
          unsigned xw = q < 8 ? xr[q >> 2][q & 3] : xro[q & 1], gw = q < 8 ? gb[q >> 2][q & 3] : gbo[q & 1];
          asm volatile("" : "+v"(xw), "+v"(gw));
          gf[q][e & 1] = (e & 1) ? hi2f(gw) : lo2f(gw);
          cf[q][e & 1] = fma1((e & 1) ? hi2f(x1p[q]) : lo2f(x1p[q]), gf[q][e & 1], (e & 1) ? hi2f(xw) : lo2f(xw));
          if (e & 1) asm volatile("" : "+v"(gf[q]), "+v"(cf[q]));
        }
        if (n == SMPN && t == 0) fl = hld64(ws, f3wait + (O_F4 - O_F3), 0);
        if (n >= 38 && sl == 8) *(LAS f4*)(L + (lr + ((w + i) & 3) * 15360) + ((i - 1) * 5 + t) * 1024) = ac[i > 0 ? i - 1 : 0][t];
        if (n == 52) {
          p[t] = *(LAS f4*)(L + (lr + w * 15360) + t * 1024);
          p[5 + t] = *(LAS f4*)(L + (lr + w * 15360) + (5 + t) * 1024);
        }
        if (n == 53) p[10 + t] = *(LAS f4*)(L + (lr + w * 15360) + (10 + t) * 1024);
        if (n >= 54) {
          const int h = 2 * (n - 54);
          p[t][h] = add1(p[t][h], p[5 + t][h]);
          p[t][h + 1] = add1(p[t][h + 1], p[5 + t][h + 1]);
          KEEP(p[t]);
        }
        PIN();
      }
      if (n == 0) STAMP(34);
    });
    MFENCE();
    PIN();
    unsigned o[10];
#pragma unroll
    for (int t = 0; t < 5; ++t) {
      const f4 y = (ac[3][t] + p[t]) + p[10 + t];
      const f2 u = f2{y[0], y[1]} * gf[2 * t] + cf[2 * t], v = f2{y[2], y[3]} * gf[2 * t + 1] + cf[2 * t + 1];
      o[2 * t] = pk2(u.x, u.y);
      o[2 * t + 1] = pk2(v.x, v.y);
    }
    const rsrc_t ro = mkrs(gout, SD2);
    __builtin_amdgcn_raw_buffer_store_b128(v4u{o[0], o[1], o[2], o[3]}, ro, dm, 0, 0);
    __builtin_amdgcn_raw_buffer_store_b128(v4u{o[4], o[5], o[6], o[7]}, ro, dm + 64, 0, 0);
    __builtin_amdgcn_raw_buffer_store_b64(v2u{o[8], o[9]}, ro, ds, 0, 0);
  }
  asm volatile(".Lfused_end:" ::: "memory");
}

extern "C" void kernel_launch(void* const* d_in, const int* in_sizes, int n_in, void* d_out, int out_size,
                              void* d_ws, size_t ws_size, hipStream_t stream) {
  (void)hipFuncSetAttribute((const void*)fused, hipFuncAttributeMaxDynamicSharedMemorySize, LDS_BYTES);
  fused<<<256, 256, LDS_BYTES, stream>>>(d_in[0], d_in[1], d_in[2], d_in[3], d_in[4], d_in[5], d_in[7], d_in[8], d_in[9],
                                         d_in[10], d_in[11], d_in[12], d_out, d_ws);
}
```

```cpp
#include <hip/hip_runtime.h>
#include <stdint.h>

typedef unsigned v4u __attribute__((ext_vector_type(4)));
typedef unsigned v2u __attribute__((ext_vector_type(2)));
typedef float f4 __attribute__((ext_vector_type(4)));
typedef float f2 __attribute__((ext_vector_type(2)));
typedef short s8v __attribute__((ext_vector_type(8)));
typedef short s4v __attribute__((ext_vector_type(4)));
typedef __bf16 bf2 __attribute__((ext_vector_type(2)));
typedef __bf16 bf8 __attribute__((ext_vector_type(8)));
typedef __amdgpu_buffer_rsrc_t rsrc_t;
#define LAS __attribute__((address_space(3)))
#define GAS __attribute__((address_space(1)))
#define DI __device__ __forceinline__

static constexpr int S_ = 2048, D_ = 640, F_ = 1728;
static constexpr unsigned OOB = 0x80000000u;

static constexpr unsigned O_EPOCH = 0;
static constexpr unsigned O_FP = 1024;
static constexpr unsigned O_FR = O_FP + 16 * 128;
static constexpr unsigned O_F1 = O_FR + 32 * 128;
static constexpr unsigned O_F2 = O_F1 + 32 * 128;
static constexpr unsigned O_F3 = O_F2 + 16 * 128;
static constexpr unsigned O_F4 = O_F3 + 16 * 512;
static constexpr unsigned O_CNT = O_F4 + 16 * 512;
static constexpr unsigned O_FW = O_CNT + 64 * 128;
static constexpr unsigned O_FB = O_FW + 8192;
static constexpr unsigned O_MOD = 65536;
static constexpr unsigned O_STAT = O_MOD + 16384;
static constexpr unsigned O_KVT = O_STAT + 131072;
static constexpr unsigned SD2 = S_ * D_ * 2;
static constexpr unsigned KVP = 4096 + 128, ZP = 10240 + 128;
static constexpr unsigned O_ZT = O_KVT + D_ * KVP;
static constexpr unsigned O_A1 = O_ZT + 256 * ZP;
static constexpr unsigned O_HP = O_A1 + SD2;
static constexpr unsigned O_X = O_HP + SD2;
static constexpr unsigned O_H1 = O_X + SD2;
static constexpr unsigned O_X1 = O_H1 + SD2;
static constexpr unsigned O_H = O_X1 + SD2;
static constexpr unsigned O_W1 = O_H + 128 * 56 * 1024;
static constexpr unsigned O_W2 = O_W1 + 800 * 1024;
static constexpr unsigned O_W3 = O_W2 + 800 * 1024;
static constexpr unsigned O_W4 = O_W3 + 2240 * 1024;
static constexpr unsigned O_END = O_W4 + 2240 * 1024;
static constexpr unsigned O_DBG = O_END;

static constexpr int CS = 4160;
static constexpr int UOFF = 8 * CS;
static constexpr int WR = 8 * CS + 4608;
static constexpr int LDS_BYTES = 160 * 1024;
static constexpr int RED_OFF = 100 * 1024;
static constexpr int CSUM_OFF = 140 * 1024;
static constexpr unsigned SPIN_CAP = 200000;

template <int V> struct IC { static constexpr int v = V; };
template <int I, int N, class F> DI void sfor(F&& f) {
  if constexpr (I < N) {
    f(IC<I>{});
    sfor<I + 1, N>(f);
  }
}
DI float lo2f(unsigned w) { return __builtin_bit_cast(float, w << 16); }
DI float hi2f(unsigned w) { return __builtin_bit_cast(float, w & 0xffff0000u); }
DI f2 up2(unsigned w) { return f2{lo2f(w), hi2f(w)}; }
DI unsigned pk2(float a, float b) { return __builtin_bit_cast(unsigned, __builtin_convertvector(f2{a, b}, bf2)); }
DI f4 mfma(v4u a, v4u b, f4 c) {
  return __builtin_amdgcn_mfma_f32_16x16x32_bf16(__builtin_bit_cast(bf8, a), __builtin_bit_cast(bf8, b), c, 0, 0, 0);
}
DI void mfma_v(f4& c, v4u a, v4u b) { asm volatile("v_mfma_f32_16x16x32_bf16 %0, %1, %2, %0" : "+v"(c) : "v"(a), "v"(b)); }
DI void mfma_v0(f4& c, v4u a, v4u b) { asm volatile("v_mfma_f32_16x16x32_bf16 %0, %1, %2, 0" : "=&v"(c) : "v"(a), "v"(b)); }
DI void mfma_va(f4& c, v4u a, v4u b) { asm volatile("v_mfma_f32_16x16x32_bf16 %0, %1, %2, %0" : "+v"(c) : "v"(a), "a"(b)); }
DI void mfma_va0(f4& c, v4u a, v4u b) { asm volatile("v_mfma_f32_16x16x32_bf16 %0, %1, %2, 0" : "=&v"(c) : "v"(a), "a"(b)); }
DI void mfma_av(f4& c, v4u a, v4u b) { asm volatile("v_mfma_f32_16x16x32_bf16 %0, %1, %2, %0" : "+v"(c) : "a"(a), "v"(b)); }
DI void mfma_av0(f4& c, v4u a, v4u b) { asm volatile("v_mfma_f32_16x16x32_bf16 %0, %1, %2, 0" : "=&v"(c) : "a"(a), "v"(b)); }
#define KEEP(x) asm volatile("" : "+v"(x))
#define MFENCE() asm volatile("s_nop 7\n\ts_nop 1" ::: "memory")
DI float add1(float a, float b) { float r; asm("v_add_f32 %0, %1, %2" : "=v"(r) : "v"(a), "v"(b)); return r; }
DI float fma1(float a, float b, float c) { float r; asm("v_fma_f32 %0, %1, %2, %3" : "=v"(r) : "v"(a), "v"(b), "v"(c)); return r; }
DI rsrc_t mkrs(const void* p, unsigned bytes) { return __builtin_amdgcn_make_buffer_rsrc((void*)p, 0, bytes, 0x00020000); }
#define INAUX 2
DI v4u ld128(rsrc_t r, unsigned v, unsigned s) { return __builtin_amdgcn_raw_buffer_load_b128(r, v, s, INAUX); }
DI v2u ld64(rsrc_t r, unsigned v, unsigned s) { return __builtin_amdgcn_raw_buffer_load_b64(r, v, s, INAUX); }
DI unsigned ld32(rsrc_t r, unsigned v, unsigned s) { return __builtin_amdgcn_raw_buffer_load_b32(r, v, s, INAUX); }
DI v4u hld128(rsrc_t r, unsigned v, unsigned s) { return __builtin_amdgcn_raw_buffer_load_b128(r, v, s, 16); }
DI v2u hld64(rsrc_t r, unsigned v, unsigned s) { return __builtin_amdgcn_raw_buffer_load_b64(r, v, s, 16); }
DI void hst128(v4u x, rsrc_t r, unsigned v, unsigned s) { __builtin_amdgcn_raw_buffer_store_b128(x, r, v + s, 0, 16); }
DI void hst64(v2u x, rsrc_t r, unsigned v, unsigned s) { __builtin_amdgcn_raw_buffer_store_b64(x, r, v, s, 16); }
DI void hst32(unsigned x, rsrc_t r, unsigned v, unsigned s) { __builtin_amdgcn_raw_buffer_store_b32(x, r, v, s, 16); }
#define DRAIN() do { asm volatile("s_waitcnt vmcnt(0)" ::: "memory"); __builtin_amdgcn_s_waitcnt(0x0F70); } while (0)
#define WGBAR() asm volatile("s_waitcnt lgkmcnt(0)\n\ts_barrier" ::: "memory")
#define PIN() __builtin_amdgcn_sched_barrier(0)

template <int CTRL> DI float dpp(float x) {
  return __builtin_bit_cast(float, __builtin_amdgcn_mov_dpp(__builtin_bit_cast(int, x), CTRL, 0xf, 0xf, true));
}
DI float xrow_sum(float x) {
  auto s = __builtin_amdgcn_permlane16_swap(__builtin_bit_cast(unsigned, x), __builtin_bit_cast(unsigned, x), false, false);
  x = __builtin_bit_cast(float, (unsigned)s[0]) + __builtin_bit_cast(float, (unsigned)s[1]);
  auto t = __builtin_amdgcn_permlane32_swap(__builtin_bit_cast(unsigned, x), __builtin_bit_cast(unsigned, x), false, false);
  return __builtin_bit_cast(float, (unsigned)t[0]) + __builtin_bit_cast(float, (unsigned)t[1]);
}
DI float swap32_add(float a, float b) {
  auto t = __builtin_amdgcn_permlane32_swap(__builtin_bit_cast(unsigned, a), __builtin_bit_cast(unsigned, b), false, false);
  return __builtin_bit_cast(float, (unsigned)t[0]) + __builtin_bit_cast(float, (unsigned)t[1]);
}
DI float swap16_add(float a, float b) {
  auto t = __builtin_amdgcn_permlane16_swap(__builtin_bit_cast(unsigned, a), __builtin_bit_cast(unsigned, b), false, false);
  return __builtin_bit_cast(float, (unsigned)t[0]) + __builtin_bit_cast(float, (unsigned)t[1]);
}
DI float rdl(float v, int l) { return __builtin_bit_cast(float, __builtin_amdgcn_readlane(__builtin_bit_cast(int, v), l)); }
DI void wave_sum4(f2& s, f2& ss) {
  float v = swap16_add(swap32_add(s.x, s.y), swap32_add(ss.x, ss.y));
  v += dpp<0xB1>(v);
  v += dpp<0x4E>(v);
  v += dpp<0x141>(v);
  v += dpp<0x128>(v);
  s = f2{rdl(v, 0), rdl(v, 32)};
  ss = f2{rdl(v, 16), rdl(v, 48)};
}
DI void poll64(rsrc_t ws, unsigned voff, v2u want) {
  const bool idle = voff >= OOB;
  for (unsigned spin = 0; spin < SPIN_CAP; ++spin) {
    asm volatile("" ::: "memory");
    const v2u v = hld64(ws, voff, 0);
    if (__all(idle || (v.x == want.x && v.y == want.y))) break;
  }
  __builtin_amdgcn_fence(__ATOMIC_ACQUIRE, "wavefront");
}

DI bool wflags_ok(v4u a, int lane, int nl, v2u want) {
  return __all(lane >= nl || (a.x == want.x && a.y == want.y && a.z == want.x && a.w == want.y));
}
DI void pollw(rsrc_t ws, unsigned voff, int lane, int nl, v2u want) {
  for (unsigned spin = 0; spin < SPIN_CAP; ++spin) {
    asm volatile("" ::: "memory");
    if (wflags_ok(hld128(ws, voff, 0), lane, nl, want)) break;
    __builtin_amdgcn_s_sleep(4);
  }
  __builtin_amdgcn_fence(__ATOMIC_ACQUIRE, "wavefront");
}
DI float rbf(float x) { return lo2f(pk2(x, 0.f)); }
DI void ln_scal(f2 s, f2 ss, f2& r, f2& mr, f2* pmean = nullptr, f2* pden = nullptr) {
  const f2 m = s * (1.f / D_);
  const f2 var = ss * (1.f / D_) - m * m;
  const f2 v = f2{fmaxf(var.x, 0.f), fmaxf(var.y, 0.f)} + 1e-5f;
  r = f2{__builtin_amdgcn_rsqf(v.x), __builtin_amdgcn_rsqf(v.y)};
  mr = -m * r;
  if (pmean) *pmean = m;
  if (pden) *pden = v * r;
}
template <int N, class Mid> DI void gelu_pack(const f2 (&x)[N], float r, float den, unsigned* o, Mid mid) {
  const float c0 = -2.f * 0.7978845608028654f * 1.4426950408889634f * r, c1 = c0 * 0.044715f * r * r;
  f2 a[N];
#pragma unroll
  for (int i = 0; i < N; ++i) a[i] = x[i] * x[i] * c1 + c0;
  mid(0);
  PIN();
#pragma unroll
  for (int i = 0; i < N; ++i) a[i] *= x[i];
  mid(1);
  PIN();
#pragma unroll
  for (int i = 0; i < N; ++i) a[i] = f2{__builtin_amdgcn_exp2f(a[i].x), __builtin_amdgcn_exp2f(a[i].y)};
  mid(2);
  PIN();
#pragma unroll
  for (int i = 0; i < N; ++i) a[i] = a[i] * den + den;
  PIN();
#pragma unroll
  for (int i = 0; i < N; ++i) a[i] = f2{__builtin_amdgcn_rcpf(a[i].x), __builtin_amdgcn_rcpf(a[i].y)};
  mid(3);
  PIN();
#pragma unroll
  for (int i = 0; i < N; ++i) a[i] *= x[i];
  PIN();
#pragma unroll
  for (int i = 0; i < N; ++i) o[i] = pk2(a[i].x, a[i].y);
}

#define STAMP(i)
#define STARG
#define STPASS(n)

static constexpr int RT_BYTES = 32 * 896;
template <bool WIDE> struct Job {
  static constexpr int NL = WIDE ? 7 : 5, RB = WIDE ? 896 : 640, PITCH = WIDE ? F_ * 2 : D_ * 2;
  v4u r[NL];
  unsigned dst;
  DI void load(int job, int w, const unsigned (&vo)[NL], const void* w1, const void* w2, const void* wx) {
    const bool big = WIDE || job >= 80;
    const int jj = WIDE ? job : (job >= 80 ? job - 80 : (job >= 40 ? job - 40 : job));
    const int gq = WIDE ? jj & 3 : jj & 1, ks = WIDE ? jj >> 2 : jj >> 1;
    const rsrc_t rs = mkrs(big ? wx : (job >= 40 ? w2 : w1), big ? D_ * F_ * 2 : D_ * D_ * 2);
    const unsigned src = (32 * ks + 8 * w) * PITCH + gq * RB;
    dst = (WIDE ? O_W3 : (job >= 80 ? O_W4 : (job >= 40 ? O_W2 : O_W1))) + ((gq * 4 + w) * (!WIDE && job >= 80 ? 56 : 20) + ks) * (NL * 1024);
#pragma unroll
    for (int i = 0; i < NL; ++i)
      r[i] = ld128(rs, (WIDE && gq == 3 && (vo[i] & 1)) ? OOB : (vo[i] & ~1u) + src, 0);
  }
  DI void put(int w, int lane, LAS char* L) {
#pragma unroll
    for (int i = 0; i < NL; ++i) *(LAS v4u*)(L + w * (8 * RB) + lane * 16 + i * 1024) = r[i];
  }
  DI void emit(int w, int lane, LAS char* L, rsrc_t ws) {
    const int g = lane >> 4, q = (lane & 15) >> 2, p = lane & 3;
    LAS char* row = L + (8 * g + q) * RB + w * (RB / 4);
    LAS char* pr = row + 32 * (w & 1) + 16 * p;
    LAS char* sg = row + ((w & 1) ? 0 : RB / 4 - 32) + 8 * p;
    v2u lo[NL], hi[NL];
#pragma unroll
    for (int f = 0; f < NL; ++f) {
      LAS char* c = f < NL - 1 ? pr + 64 * (f >> 1) + 8 * (f & 1) : sg;
      lo[f] = __builtin_bit_cast(v2u, __builtin_amdgcn_ds_read_tr16_b64_v4i16((LAS s4v*)c));
      hi[f] = __builtin_bit_cast(v2u, __builtin_amdgcn_ds_read_tr16_b64_v4i16((LAS s4v*)(c + 4 * RB)));
    }
#pragma unroll
    for (int f = 0; f < NL; ++f) hst128(v4u{lo[f].x, lo[f].y, hi[f].x, hi[f].y}, ws, lane * 16 + dst, f * 1024);
  }
};

template <int KS, int WIN, class Extra, class Tail, class Early>
DI void gemm_ks(rsrc_t ws, unsigned abase, unsigned mstride, const v4u* b, LAS char* L, int w, int lane, f4 (&acc)[5], Extra extra, Tail tail, Early early STARG) {
  constexpr int NA = 4 * KS, RING = WIN + 1;
  v4u a[RING];
  unsigned so[4];
#pragma unroll
  for (int i = 0; i < 4; ++i) so[i] = abase + ((w + 1 + i) & 3) * mstride;
#pragma unroll
  for (int n = 0; n < WIN; ++n) {
    a[n] = hld128(ws, lane * 16 + (n % KS) * 1024, so[n / KS]);
    early(n);
    PIN();
  }
  f4 prev[5], p[15];
  unsigned lr = RED_OFF + lane * 16;
  KEEP(lr);
  sfor<0, NA>([&](auto ic) {
    constexpr int n = decltype(ic)::v, i = n / KS, k = n % KS;
    if (n == NA - 3) {
      STAMP(sb + 1);
      WGBAR();
      STAMP(sb + 2);
    }
#pragma unroll
    for (int t = 0; t < 5; ++t) {
      acc[t] = mfma(b[k * 5 + t], a[n % RING], k == 0 ? f4{0.f, 0.f, 0.f, 0.f} : acc[t]);
      if (t == 1) {
        if constexpr (n + WIN < NA) a[(n + WIN) % RING] = hld128(ws, lane * 16 + ((n + WIN) % KS) * 1024, so[(n + WIN) / KS]);
        else extra(n + WIN - NA);
      }
      if (k == 0 && i > 0) *(LAS f4*)(L + (lr + ((w + i) & 3) * 15360) + ((i - 1) * 5 + t) * 1024) = prev[t];
      if (n == NA - 3) {
        p[t] = *(LAS f4*)(L + (lr + w * 15360) + t * 1024);
        p[5 + t] = *(LAS f4*)(L + (lr + w * 15360) + (5 + t) * 1024);
      }
      if (n == NA - 2) p[10 + t] = *(LAS f4*)(L + (lr + w * 15360) + (10 + t) * 1024);
      if (n < NA - 1) PIN();
    }
    if (n == 0) STAMP(sb);
    if (k == KS - 1) {
#pragma unroll
      for (int t = 0; t < 5; ++t) prev[t] = acc[t];
    }
  });
  tail();
#pragma unroll
  for (int t = 0; t < 5; ++t) acc[t] += (p[t] + p[5 + t]) + p[10 + t];
}

#define YOUNG() do { asm volatile("; YOUNG" ::: "memory"); PIN(); } while (0)
#define WAITV(n) asm volatile("s_waitcnt vmcnt(" #n ") ; WAITV" ::: "memory")
__global__ __launch_bounds__(256, 1) void fused(const void*, const void*, const void*, const void*, const void*, const void*,
                                                const void*, const void*, const void*, const void*, const void*, const void*,
                                                void*, void*) {
  extern __shared__ __attribute__((aligned(16))) char smem[];
  LAS char* L = (LAS char*)smem;
  STAMP(0);
  unsigned pf = 0;
  if (blockIdx.x >= 160) {
    unsigned long long pc;
    unsigned off = (((blockIdx.x >> 3) & 1) * 256 + threadIdx.x) * 128;
    asm volatile("s_getpc_b64 %1\n.Lfused_pc:\n\tv_min_u32 %2, .Lfused_end-.Lfused_pc, %2\n\tglobal_load_dword %0, %2, %1"
                 : "=&v"(pf), "=&s"(pc), "+v"(off));
  }
  PIN();
  typedef void* const __attribute__((address_space(4))) * karg_t;
  const karg_t ka = (karg_t)__builtin_amdgcn_kernarg_segment_ptr();
  void *gq = ka[0], *gk = ka[1], *gv = ka[2], *gsk = ka[3], *gssm = ka[4], *gD = ka[5], *gw1 = ka[6], *gw2 = ka[7], *gw3 = ka[8],
       *gw4 = ka[9], *gc = ka[10], *gwm = ka[11], *gout = ka[12], *gws = ka[13];
  asm volatile("" ::"s"(gq), "s"(gk), "s"(gv), "s"(gsk), "s"(gssm), "s"(gD), "s"(gw1), "s"(gw2), "s"(gw3), "s"(gw4),
               "s"(gc), "s"(gwm), "s"(gout), "s"(gws));
  const int tid = threadIdx.x, lane = tid & 63;
  const int w = __builtin_amdgcn_readfirstlane(tid >> 6);
  const int b = blockIdx.x, x = b & 7, j = b >> 3;
  const int tm = 4 * x + (j >> 3), tn = j & 7;
  const int rg = 2 * x + (j >> 4), cg = j & 15;
  const bool conv_wg = j < 20;
  const rsrc_t ws = mkrs(gws, O_END);
  const int g = lane >> 4, l15 = lane & 15;

  const int rt0 = 64 * tm + 8 * tn + 2 * w;
  const int rcm = 8 * lane, rco = 512 + 2 * lane;
  v4u qm[2];
  unsigned qo[2];
  auto load_q = [&]() {
    const rsrc_t rq = mkrs(gq, SD2);
#pragma unroll
    for (int t = 0; t < 2; ++t) {
      qm[t] = ld128(rq, rcm * 2, (rt0 + t) * D_ * 2);
      qo[t] = ld32(rq, rco * 2, (rt0 + t) * D_ * 2);
    }
  };
  v2u tag;
  auto make_tag = [&](v2u ep) {
    tag.x = __builtin_amdgcn_readfirstlane(ep.x) * 0x9E3779B1u + 0x7F4A7C15u;
    tag.y = (__builtin_amdgcn_readfirstlane(ep.y) ^ tag.x) * 0x85EBCA77u + 0x2545F491u;
    tag.x &= 0xffffff00u;
  };
  v4u b1[25], b2[25];
  const unsigned sb1 = O_W1 + (tn * 100 + w * 25) * 1024, sb2 = O_W2 + (tn * 100 + w * 25) * 1024;
  auto zero_h1 = [&](int i) {
    hst128(v4u{0u, 0u, 0u, 0u}, ws, (i < 2 || tid < 128) ? tid * 16 : OOB, O_H1 + b * 10240 + i * 4096);
  };
  v4u mm[5];
  unsigned mo[5];
  auto load_mod = [&](bool ok) {
    unsigned o1 = (ok ? 0u : OOB) | (lane * 16), o2 = (ok ? 0u : OOB) | (lane * 4 + 1024);
    asm volatile("" : "+v"(o1), "+v"(o2));
#pragma unroll
    for (int i = 0; i < 5; ++i) {
      mm[i] = hld128(ws, o1, O_MOD + i * D_ * 2);
      mo[i] = __builtin_amdgcn_raw_buffer_load_b32(ws, o2, O_MOD + i * D_ * 2, 16);
    }
  };
  f2 qv[10], n1[10];
  auto ln1 = [&]() {
#pragma unroll
    for (int i = 0; i < 4; ++i) {
      qv[2 * i] = f2{lo2f(qm[0][i]), lo2f(qm[1][i])};
      qv[2 * i + 1] = f2{hi2f(qm[0][i]), hi2f(qm[1][i])};
    }
    qv[8] = f2{lo2f(qo[0]), lo2f(qo[1])};
    qv[9] = f2{hi2f(qo[0]), hi2f(qo[1])};
    f2 s = qv[0], ss = qv[0] * qv[0];
#pragma unroll
    for (int i = 1; i < 10; ++i) { s += qv[i]; ss += qv[i] * qv[i]; }
    wave_sum4(s, ss);
    f2 r, mr;
    ln_scal(s, ss, r, mr);
#pragma unroll
    for (int i = 0; i < 10; ++i) n1[i] = qv[i] * r + mr;
  };
  unsigned z3off, im0, io0, roff, roff3, own_r, own_w, h1m, h1s, f1pub, f1wait, frpub, frwait, f2pub, f2wait;
  const unsigned own_m = ((4 * tm + w) * 20 + 5 * (tn >> 1) + 3 * (tn & 1)) * 1024;
  const unsigned own_s = ((4 * tm + w) * 20 + 5 * (tn >> 1) + 2) * 1024 + (tn & 1) * 512;
  auto offsets = [&]() {
    z3off = tid < 128 ? tid * 16 + 8192 : OOB;
    im0 = RED_OFF + 16384 + lane * 128 + (((2 * w) ^ (lane & 7)) << 4);
    io0 = RED_OFF + 16384 + (64 + (lane >> 2)) * 128 + (((2 * w) ^ ((lane >> 2) & 7)) << 4) + 4 * (lane & 3);
    roff = O_A1 + ((4 * tm + (tn >> 1)) * 20 + (tid >> 5)) * 1024 + (((tid ^ (tid >> 3)) & 7) + 8 * (tn & 1) + 16 * ((tid >> 3) & 3)) * 16;
    roff3 = tid < 128 ? roff : OOB;
    own_r = (l15 + 16 * (g >> 1)) * 16 + 8 * (g & 1);
    own_w = (g & 1) ? OOB : (l15 + 16 * (g >> 1)) * 16;
    const int sh = (tn >> 1) == 0 ? 1 : ((tn >> 1) == 1 ? -1 : ((tn >> 1) == 2 ? 2 : -2));
    const int t2 = 64 * tm + 16 * w + l15 + sh;
    const bool ok = t2 >= 0 && t2 < S_;
    const unsigned d = O_H1 + ((t2 >> 4) * 20 + 5 * (tn >> 1)) * 1024 + (t2 & 15) * 16;
    h1m = ok ? d + 3072 * (tn & 1) + 256 * g : OOB;
    h1s = (ok && !(g & 1)) ? d + 2048 + 512 * (tn & 1) + 256 * (g >> 1) : OOB;
    const int nb = tm + (sh > 0 ? 1 : -1);
    f1pub = OOB;
    if (lane == 0) f1pub = O_F1 + tm * 128 + tn * 8;
    if (lane == 1 && nb >= 0 && nb < 32) f1pub = O_F1 + nb * 128 + 64 + tn * 8;
    const bool up = ((lane >> 1) & 1) == 0;
    f1wait = (lane < 8 || (lane < 16 && (up ? tm > 0 : tm < 31))) ? O_F1 + tm * 128 + lane * 8 : OOB;
    frpub = lane == 0 ? O_FR + tm * 128 + tn * 8 : OOB;
    frwait = lane < 8 ? O_FR + tm * 128 + lane * 8 : OOB;
    f2pub = lane == 0 ? O_F2 + (tm >> 1) * 128 + ((tm & 1) * 8 + tn) * 8 : OOB;
    f2wait = lane < 16 ? O_F2 + rg * 128 + lane * 8 : OOB;
    asm volatile("" : "+v"(frpub), "+v"(frwait), "+v"(f2pub), "+v"(f2wait));
    asm volatile("" : "+v"(z3off), "+v"(im0), "+v"(io0), "+v"(roff), "+v"(roff3), "+v"(own_r), "+v"(own_w), "+v"(h1m), "+v"(h1s), "+v"(f1pub), "+v"(f1wait));
  };
  bool wok = false, bok = false;
  if (conv_wg) {
    const int cgp = j < 16 ? x : 8 + (x >> 2), ci = j < 16 ? j : (x & 3) * 4 + (j - 16);
    const int pT0 = 128 * ci + 32 * w + 8 * ((lane >> 3) & 3) + 4 * (lane >> 5), pC0 = 64 * cgp + 8 * (lane & 7);
    const int ch = 64 * cgp + 4 * ci + w;
    v4u kw[7], vw[4], skw[4], kr[4];
    {
      const rsrc_t rk = mkrs(gk, SD2), rv = mkrs(gv, SD2), rs = mkrs(gsk, D_ * 8);
      const unsigned o = (unsigned)(pT0 * D_ + pC0) * 2;
#pragma unroll
      for (int i = 0; i < 7; ++i) kw[i] = ld128(rk, o + (i - 3) * D_ * 2, 0);
#pragma unroll
      for (int i = 0; i < 4; ++i) vw[i] = ld128(rv, o + i * D_ * 2, 0);
#pragma unroll
      for (int i = 0; i < 4; ++i) skw[i] = __builtin_amdgcn_raw_buffer_load_b128(rs, pC0 * 8 + i * 16, 0, 0);
      PIN();
      offsets();
      PIN();
      asm volatile("s_waitcnt vmcnt(4)" ::: "memory");
      const rsrc_t rK = mkrs((const char*)gssm + (size_t)ch * 4096, 4096);
#pragma unroll
      for (int i = 0; i < 4; ++i) kr[i] = ld128(rK, lane * 16 + i * 1024, 0);
    }
    const v2u ep = hld64(ws, lane == 0 ? O_EPOCH : O_FP + x * 256 + (j >= 16) * 128, 0);
    const unsigned dvb = __builtin_amdgcn_raw_buffer_load_b16(mkrs(gD, D_ * 2), ch * 2, 0, 0);
    PIN();
    {
      unsigned pe[8][2];
#pragma unroll
      for (int pr = 0; pr < 4; ++pr) {
        const f2 w0 = {lo2f(skw[pr].x), lo2f(skw[pr].z)}, w1 = {hi2f(skw[pr].x), hi2f(skw[pr].z)};
        const f2 w2 = {lo2f(skw[pr].y), lo2f(skw[pr].w)}, w3 = {hi2f(skw[pr].y), hi2f(skw[pr].w)};
        f2 kf[7], o[4];
#pragma unroll
        for (int i = 0; i < 7; ++i) kf[i] = up2(kw[i][pr]);
#pragma unroll
        for (int i = 0; i < 4; ++i) o[i] = (kf[i + 3] * w0 + kf[i + 2] * w1 + kf[i + 1] * w2 + kf[i] * w3) * up2(vw[i][pr]);
        pe[2 * pr][0] = pk2(o[3].x, o[2].x);
        pe[2 * pr][1] = pk2(o[1].x, o[0].x);
        pe[2 * pr + 1][0] = pk2(o[3].y, o[2].y);
        pe[2 * pr + 1][1] = pk2(o[1].y, o[0].y);
      }
      const unsigned so = (pC0 + 4 * (lane >> 5)) * KVP + (pT0 & ~7) * 2;
#pragma unroll
      for (int e = 0; e < 4; ++e) {
        const auto s0 = __builtin_amdgcn_permlane32_swap(pe[e][0], pe[e + 4][0], false, false);
        const auto s1 = __builtin_amdgcn_permlane32_swap(pe[e][1], pe[e + 4][1], false, false);
        hst128(v4u{s0[1], s1[1], s0[0], s1[0]}, ws, so, O_KVT + e * KVP);
      }
    }
    STAMP(1);
    PIN();
    LAS char* T = L + w * WR;
    if (lane < 8) *(LAS v4u*)(T + lane * CS) = v4u{0u, 0u, 0u, 0u};
#pragma unroll
    for (int i = 0; i < 4; ++i) *(LAS v4u*)(T + 7 * CS + (i * 64 + lane + 1) * 16) = kr[i];
    asm volatile("s_waitcnt lgkmcnt(0)" ::: "memory");
    v4u kl[4];
#pragma unroll
    for (int i = 0; i < 4; ++i) kl[i] = *(LAS v4u*)(T + 7 * CS + (i * 64 + lane) * 16);
    auto tbuild = [&](int i) {
      const unsigned d[8] = {kl[i].x, kl[i].y, kl[i].z, kl[i].w, kr[i].x, kr[i].y, kr[i].z, kr[i].w};
      unsigned a[7];
#pragma unroll
      for (int e = 0; e < 7; ++e) a[e] = __builtin_amdgcn_alignbit(d[e + 1], d[e], 16);
#pragma unroll
      for (int m = 0; m < 7; ++m) {
        const int h = (m + 1) >> 1;
        const v4u o = (m & 1) ? v4u{d[h], d[h + 1], d[h + 2], d[h + 3]} : v4u{a[h], a[h + 1], a[h + 2], a[h + 3]};
        *(LAS v4u*)(T + m * CS + (i * 64 + lane + 1) * 16) = o;
      }
    };
    tbuild(0);
    make_tag(ep);
    DRAIN();
    asm volatile("s_barrier" ::: "memory");
    if (w == 0) {
      unsigned o = OOB;
      if (j < 16 && lane == 0) o = O_FP + x * 256 + ci * 8;
      if (j >= 16 && lane < 4) o = O_FP + ((x & 4) + lane) * 256 + 128 + ci * 8;
      hst64(tag, ws, o, 0);
    }
    STAMP(2);
    tbuild(1);
    tbuild(2);
    tbuild(3);
    if (lane < 32) *(LAS v4u*)(T + UOFF + lane * 16) = v4u{0u, 0u, 0u, 0u};
    STAMP(3);
    if (w == 0) poll64(ws, lane < 16 ? O_FP + x * 256 + (j >= 16) * 128 + lane * 8 : OOB, tag);
    WGBAR();
    STAMP(4);
    const v4u wf = hld128(ws, O_FW + x * 1024 + lane * 16, 0);
    v4u ur[4];
#pragma unroll
    for (int i = 0; i < 4; ++i) ur[i] = hld128(ws, lane * 16 + i * 1024, O_KVT + ch * KVP);
    const LAS char* pa = T + (l15 & 7) * CS + (3 - g + (l15 >> 3)) * 16;
    const LAS char* pv = T + UOFF + 512 + (l15 - 1) * 32 + g * 16;
    v4u fa[2][8], fv[2][8];
#pragma unroll
    for (int a = 0; a < 8; ++a) fa[0][a] = *(const LAS v4u*)(pa + 8 * a * 64);
    f4 acc[8];
    v4u ad;
    {
      const int jd = 8 * g - 9 - l15;
#pragma unroll
      for (int e = 0; e < 4; ++e) ad[e] = jd == 2 * e ? dvb : (jd == 2 * e + 1 ? dvb << 16 : 0u);
      asm volatile("" : "+v"(ad));
    }
    PIN();
#pragma unroll
    for (int i = 0; i < 4; ++i) *(LAS v4u*)(T + UOFF + 512 + i * 1024 + lane * 16) = ur[i];
    asm volatile("s_waitcnt lgkmcnt(0)" ::: "memory");
#pragma unroll
    for (int a = 0; a < 8; ++a) fv[0][a] = *(const LAS v4u*)(pv + a * 512);
    wok = wflags_ok(wf, lane, 40, tag);
    __builtin_amdgcn_fence(__ATOMIC_ACQUIRE, "wavefront");
    const unsigned boff = wok ? lane * 16 : OOB;
    STAMP(5);
#pragma unroll
    for (int q = 0; q < 8; ++q) mfma_v0(acc[q], ad, fv[0][q]);
    PIN();
#pragma unroll
    for (int g0 = 0; g0 < 8; ++g0) {
      int n = 0;
      if (g0 == 0) load_q();
#pragma unroll
      for (int a = 0; a < 8; ++a)
#pragma unroll
        for (int q = a; q < 8; ++q) {
          mfma_v(acc[q], fa[g0 & 1][a], fv[g0 & 1][q - a]);
          ++n;
          if ((n & 1) == 0) {
            const int e = n / 2 - 1;
            if (g0 < 7 && e < 16) {
              if (e < 8) fv[(g0 + 1) & 1][e] = *(const LAS v4u*)(pv + e * 512 - (g0 + 1) * 64);
              else fa[(g0 + 1) & 1][e - 8] = *(const LAS v4u*)(pa + (g0 + 1 + 8 * (e - 8)) * 64);
            }
            PIN();
          }
          if (n % 5 == 0) {
            const int i = g0 * 7 + n / 5 - 13;
            if (i < 0) {
              if (i < -9) zero_h1(i + 12);
            } else if (i < 25) b1[i] = hld128(ws, boff, sb1 + i * 1024);
            else if (i < 50) b2[i - 25] = hld128(ws, boff, sb2 + (i - 25) * 1024);
            PIN();
          }
        }
    }
    const v4u bfl = hld128(ws, O_FB + x * 1024 + lane * 16, 0);
    MFENCE();
    PIN();
    STAMP(6);
#pragma unroll
    for (int q = 0; q < 8; q += 2) {
      const auto s0 = __builtin_amdgcn_permlane16_swap(pk2(acc[q][0], acc[q][1]), pk2(acc[q + 1][0], acc[q + 1][1]), false, false);
      const auto s1 = __builtin_amdgcn_permlane16_swap(pk2(acc[q][2], acc[q][3]), pk2(acc[q + 1][2], acc[q + 1][3]), false, false);
      *(LAS v4u*)(T + 64 * w + (32 * (q + (g & 1)) + 2 * l15 + (g >> 1)) * 16) = v4u{s0[0], s1[0], s0[1], s1[1]};
    }
    WGBAR();
    {
      v4u zo[4];
#pragma unroll
      for (int i = 0; i < 4; ++i) zo[i] = *(LAS v4u*)(L + (lane & 3) * (WR + 64) + (64 * w + 16 * i + (lane >> 2)) * 16);
#pragma unroll
      for (int i = 0; i < 4; ++i) hst128(zo[i], ws, (lane >> 2) * ZP + (lane & 3) * 16, O_ZT + (64 * w + 16 * i) * ZP + (ch - w) * 16);
    }
    YOUNG();
    bok = wflags_ok(bfl, lane, 48, tag);
    __builtin_amdgcn_fence(__ATOMIC_ACQUIRE, "wavefront");
    load_mod(bok);
    PIN();
    ln1();
    PIN();
    WAITV(10);
  } else {
    const int hb = (j - 20) * 8 + x;
    unsigned vn[5];
#pragma unroll
    for (int i = 0; i < 5; ++i) vn[i] = (i * 64 + lane) * 16 + (((i * 64 + lane) * 1639u) >> 16) * 640;
    Job<false> j0, j1;
    j0.load(hb, w, vn, gw1, gw2, gw4);
    const v2u ep = hld64(ws, O_EPOCH, 0);
    const v2u cw = __builtin_amdgcn_raw_buffer_load_b64(mkrs(gc, D_ * 2), tid * 8, 0, 0);
    PIN();
    unsigned vw[7];
#pragma unroll
    for (int i = 0; i < 7; ++i) {
      const unsigned c = i * 64 + lane, row = (c * 1171u) >> 16;
      vw[i] = c * 16 + row * 2560 + (c - row * 56 >= 48);
      KEEP(vw[i]);
    }
    offsets();
    LAS float* sc = (LAS float*)(L + 4 * RT_BYTES);
    {
      const float c4[4] = {lo2f(cw.x), hi2f(cw.x), lo2f(cw.y), hi2f(cw.y)};
      f4 sv;
#pragma unroll
      for (int e = 0; e < 4; ++e) sv[e] = rbf(c4[e] * __builtin_amdgcn_rcpf(1.f + __builtin_amdgcn_exp2f(-1.4426950408889634f * c4[e])));
      *(LAS f4*)(sc + tid * 4) = sv;
    }
    j0.put(w, lane, L);
    make_tag(ep);
    WGBAR();
    j0.emit(w, lane, L, ws);
    if (j == 20 && w == 0) hst128(v4u{tag.x, tag.y, 0u, 0u}, ws, (lane >> 3) * 1024 + (lane & 7) * 16, O_CNT + x * 128);
    asm volatile("" ::"v"(pf) : "memory");
    STAMP(1);
    auto first_pub = [&]() {
      WGBAR();
      if (w == 0) hst64(tag, ws, lane < 8 ? O_FW + lane * 1024 + hb * 8 : OOB, 0);
      STAMP(2);
    };
    if (hb < 60) {
      const rsrc_t rm = mkrs(gwm, D_ * 6 * D_ * 2);
      v4u wm[20];
      YOUNG();
#pragma unroll
      for (int i = 0; i < 8; ++i) wm[i] = ld128(rm, (tid >> 3) * (6 * D_ * 2) + (tid & 7) * 16, hb * 128 + i * 32 * (6 * D_ * 2));
      PIN();
      WAITV(8);
      first_pub();
#pragma unroll
      for (int i = 8; i < 20; ++i) wm[i] = ld128(rm, (tid >> 3) * (6 * D_ * 2) + (tid & 7) * 16, hb * 128 + i * 32 * (6 * D_ * 2));
      j1.load(132 + hb, w, vn, gw1, gw2, gw4);
      load_q();
      float sl[20];
#pragma unroll
      for (int i = 0; i < 20; ++i) sl[i] = sc[(tid >> 3) + 32 * i];
      PIN();
      f2 a[4] = {f2{0.f, 0.f}, f2{0.f, 0.f}, f2{0.f, 0.f}, f2{0.f, 0.f}};
#pragma unroll
      for (int i = 0; i < 20; ++i)
#pragma unroll
        for (int e = 0; e < 4; ++e) a[e] += up2(wm[i][e]) * sl[i];
      LAS char* mred = L + 4 * RT_BYTES + 4096;
      *(LAS f4*)(mred + (tid >> 3) * 272 + (tid & 7) * 32) = f4{a[0].x, a[0].y, a[1].x, a[1].y};
      *(LAS f4*)(mred + (tid >> 3) * 272 + (tid & 7) * 32 + 16) = f4{a[2].x, a[2].y, a[3].x, a[3].y};
      WGBAR();
      {
        const int col = tid >> 2, part = tid & 3;
        float v[8];
#pragma unroll
        for (int r = 0; r < 8; ++r) v[r] = *(LAS float*)(mred + (part * 8 + r) * 272 + col * 4);
        float s = ((v[0] + v[1]) + (v[2] + v[3])) + ((v[4] + v[5]) + (v[6] + v[7]));
        s += dpp<0xB1>(s);
        s += dpp<0x4E>(s);
        s = rbf(s);
        if ((hb >= 10 && hb < 20) || (hb >= 40 && hb < 50)) s = rbf(1.f + s);
        if (part == 0) *((LAS unsigned short*)sc + 8 + col) = (unsigned short)(__builtin_bit_cast(unsigned, s) >> 16);
      }
      j1.put(w, lane, L);
      WGBAR();
      if (w == 0) {
        hst128(*(LAS v4u*)((LAS char*)sc + 16 + (lane & 7) * 16), ws, lane < 8 ? lane * 16 : OOB, O_MOD + hb * 128);
        DRAIN();
        hst64(tag, ws, lane < 8 ? O_FB + lane * 1024 + hb * 8 : OOB, 0);
      }
      STAMP(3);
    } else {
      Job<true> ja, jb, jc;
      YOUNG();
      ja.load(hb - 60, w, vw, gw1, gw2, gw3);
      PIN();
      WAITV(7);
      first_pub();
      jb.load(hb - 24, w, vw, gw1, gw2, gw3);
      if (hb < 68) jc.load(hb + 12, w, vw, gw1, gw2, gw3);
      j1.load(36 + hb, w, vn, gw1, gw2, gw4);
      load_q();
      PIN();
      ja.put(w, lane, L + RT_BYTES);
      jb.put(w, lane, L + 2 * RT_BYTES);
      if (hb < 68) jc.put(w, lane, L + 3 * RT_BYTES);
      WGBAR();
      ja.emit(w, lane, L + RT_BYTES, ws);
      jb.emit(w, lane, L + 2 * RT_BYTES, ws);
      if (hb < 68) jc.emit(w, lane, L + 3 * RT_BYTES, ws);
      j1.put(w, lane, L);
      DRAIN();
      WGBAR();
      if (w == 0) hst64(tag, ws, lane < 8 ? O_FB + lane * 1024 + hb * 8 : OOB, 0);
      STAMP(3);
    }
    pollw(ws, O_FW + x * 1024 + lane * 16, lane, 40, tag);
    j1.emit(w, lane, L, ws);
    zero_h1(0);
    zero_h1(1);
    zero_h1(2);
    if (hb >= 34 && hb < 60) hst128(v4u{0u, 0u, 0u, 0u}, ws, w == 0 ? lane * 16 : OOB, O_FR + (hb - 34) * 1024);
    STAMP(4);
    YOUNG();
#pragma unroll
    for (int i = 0; i < 10; ++i) b1[i] = hld128(ws, lane * 16, sb1 + i * 1024);
    PIN();
    ln1();
    PIN();
    WAITV(10);
  }
  if (conv_wg && !wok) pollw(ws, O_FW + x * 1024 + lane * 16, lane, 40, tag);
  WGBAR();
  STAMP(7);
  if (w == 0 && lane < 8)
    __hip_atomic_fetch_add((GAS unsigned*)((GAS char*)gws + O_CNT + (lane * 8 + x) * 128), 1u, __ATOMIC_RELAXED, __HIP_MEMORY_SCOPE_AGENT);
  if (!wok) {
    if (conv_wg) {
#pragma unroll
      for (int i = 0; i < 10; ++i) b1[i] = hld128(ws, lane * 16, sb1 + i * 1024);
    }
#pragma unroll
    for (int i = 10; i < 25; ++i) b1[i] = hld128(ws, lane * 16, sb1 + i * 1024);
#pragma unroll
    for (int i = 0; i < 19; ++i) b2[i] = hld128(ws, lane * 16, sb2 + i * 1024);
  }
#pragma unroll
  for (int i = 19; i < 25; ++i) b2[i] = hld128(ws, lane * 16, sb2 + i * 1024);
  v4u b3[35], b4[70];
  const unsigned sb3 = O_W3 + (cg * 140 + w * 35) * 1024, sb4 = O_W4 + (tn * 280 + w * 70) * 1024;
#define LB3(lo, hi) _Pragma("unroll") for (int i_ = lo; i_ < hi; ++i_) b3[i_] = hld128(ws, lane * 16 + (i_ & 3) * 1024, sb3 + (i_ >> 2) * 4096)
  auto ks4 = [](int s) { return s < 8 ? 7 * (s >> 2) + (s & 1) + 4 * ((s >> 1) & 1) : 7 * ((s - 8) / 3) + ((s - 8) % 3 == 2 ? 6 : 2 + (s - 8) % 3); };
  auto lb4 = [&](int i) {
    const int f = ks4(i / 5) * 5 + i % 5;
    b4[i] = hld128(ws, lane * 16 + (f & 3) * 1024, sb4 + (f >> 2) * 4096);
  };
#define LB4(lo, hi) _Pragma("unroll") for (int i_ = lo; i_ < hi; ++i_) lb4(i_)
  if (!bok) {
    pollw(ws, O_FB + x * 1024 + lane * 16, lane, 48, tag);
    load_mod(true);
  }
  constexpr int NB3C = 10;
  f2 gh[10];
  float cb1[10], sbv[10];
  auto md = [&](int v, int i) { const unsigned u = i < 8 ? mm[v][(i >> 1) & 3] : mo[v]; return (i & 1) ? hi2f(u) : lo2f(u); };
  auto use_mod = [&]() {
    LB3(0, NB3C);
#pragma unroll
    for (int i = 0; i < 10; ++i) {
      gh[i] = (n1[i] * md(1, i) + md(0, i)) * md(2, i);
      cb1[i] = md(4, i);
      sbv[i] = md(3, i);
      asm volatile("" : "+v"(gh[i]), "+v"(cb1[i]), "+v"(sbv[i]));
    }
  };
  use_mod();
  PIN();
  if (w == 0) {
    poll64(ws, lane < 8 ? O_CNT + (x * 8 + lane) * 128 : OOB, v2u{tag.x + 32, tag.y});
    if (b == 0) hst64(tag, ws, lane == 0 ? O_EPOCH : OOB, 0);
  }
  WGBAR();
  STAMP(8);

  v4u hx[6];
  f4 csp[7];
  auto img3 = [&](int lo, int hi, int hc) {
    const v4u ones = {0x3f803f80u, 0x3f803f80u, 0x3f803f80u, 0x3f803f80u};
    unsigned la = w * 25600 + lane * 16, lb = (75 + w * 10) * 1024 + lane * 16;
    asm volatile("" : "+v"(la), "+v"(lb));
#pragma unroll
    for (int i = lo; i < 35; ++i) {
      if (i < hi) *(LAS v4u*)(L + (i < 25 ? la : lb) + i * 1024) = b3[i];
      if (i < hc) csp[i % 7] = mfma(b3[i], ones, i < 7 ? f4{0.f, 0.f, 0.f, 0.f} : csp[i % 7]);
      PIN();
    }
  };
  {
    v4u zc[3];
#pragma unroll
    for (int i = 0; i < 3; ++i) zc[i] = hld128(ws, i < 2 ? tid * 16 + i * 4096 : z3off, O_ZT + (8 * tm + tn) * ZP);
    LB3(10, 16);
    PIN();
#pragma unroll
    for (int i = 0; i < 3; ++i)
#pragma unroll
      for (int e = 0; e < 4; ++e) *(LAS unsigned*)(L + RED_OFF + e * 3072 + (tid + 256 * i) * 4) = zc[i][e];
    STAMP(9);
    WGBAR();
    STAMP(10);
    const v4u z0 = *(LAS v4u*)(L + RED_OFF + w * 3072 + rcm * 4), z1 = *(LAS v4u*)(L + RED_OFF + w * 3072 + rcm * 4 + 16);
    const v2u z2 = *(LAS v2u*)(L + RED_OFF + w * 3072 + rco * 4);
    LB3(16, 23);
    f2 xv[10], hv[10], av[10], s, ss, r, mr;
#pragma unroll
    for (int i = 0; i < 10; ++i) {
      xv[i] = gh[i] * up2(i < 4 ? z0[i & 3] : (i < 8 ? z1[i & 3] : z2[i & 1])) + qv[i];
      s = i ? s + xv[i] : xv[i];
      ss = i ? ss + xv[i] * xv[i] : xv[i] * xv[i];
    }
    wave_sum4(s, ss);
    ln_scal(s, ss, r, mr);
#pragma unroll
    for (int i = 0; i < 10; ++i) {
      hv[i] = (xv[i] * r + mr) * cb1[i] + sbv[i];
      s = i ? s + hv[i] : hv[i];
      ss = i ? ss + hv[i] * hv[i] : hv[i] * hv[i];
    }
    wave_sum4(s, ss);
    ln_scal(s, ss, r, mr);
#pragma unroll
    for (int i = 0; i < 10; ++i) av[i] = hv[i] * r + mr;
    auto put = [&](const f2(&v)[10], int img) {
      *(LAS v4u*)(L + im0 + img * 10240) = v4u{pk2(v[0].x, v[1].x), pk2(v[2].x, v[3].x), pk2(v[4].x, v[5].x), pk2(v[6].x, v[7].x)};
      *(LAS v4u*)(L + (im0 ^ 16) + img * 10240) = v4u{pk2(v[0].y, v[1].y), pk2(v[2].y, v[3].y), pk2(v[4].y, v[5].y), pk2(v[6].y, v[7].y)};
      *(LAS unsigned*)(L + io0 + img * 10240) = pk2(v[8].x, v[9].x);
      *(LAS unsigned*)(L + (io0 ^ 16) + img * 10240) = pk2(v[8].y, v[9].y);
    };
    put(av, 0);
    STAMP(11);
    WGBAR();
    {
      v4u oc[3];
#pragma unroll
      for (int i = 0; i < 3; ++i) oc[i] = *(LAS v4u*)(L + RED_OFF + 16384 + i * 4096 + tid * 16);
#pragma unroll
      for (int i = 0; i < 3; ++i) hst128(oc[i], ws, i < 2 ? roff : roff3, i * 8192);
    }
    PIN();
    put(hv, 1);
    put(xv, 2);
    STAMP(12);
    DRAIN();
    WGBAR();
    STAMP(13);
    if (w == 0) hst64(tag, ws, frpub, 0);
#pragma unroll
    for (int i = 0; i < 6; ++i) hx[i] = *(LAS v4u*)(L + RED_OFF + 26624 + (i / 3) * 10240 + (i % 3) * 4096 + tid * 16);
    img3(0, 16, 16);
    if (w == 0) poll64(ws, frwait, tag);
    WGBAR();
    STAMP(14);
  }

  {
    f4 acc[5];
    gemm_ks<5, 12>(ws, O_A1 + (4 * tm * 20 + 5 * w) * 1024, 20 * 1024, b1, L, w, lane, acc,
                   [&](int n) {
                     if (n < 6) hst128(hx[n], ws, (n % 3) < 2 ? roff : roff3, (1 + n / 3) * SD2 + (n % 3) * 8192);
                     b3[23 + n] = hld128(ws, lane * 16, sb3 + (23 + n) * 1024);
                   }, [&]() { LB4(0, 4); }, [](int) {} STPASS(15));
    STAMP(18);
    f2 v[10];
    unsigned o[10];
#pragma unroll
    for (int t = 0; t < 5; ++t) {
      v[2 * t] = f2{acc[t][0], acc[t][1]};
      v[2 * t + 1] = f2{acc[t][2], acc[t][3]};
    }
    gelu_pack<10>(v, 1.f, 1.f, o, [](int) {});
    const auto s0 = __builtin_amdgcn_permlane16_swap(o[8], o[8], false, false);
    const auto s1 = __builtin_amdgcn_permlane16_swap(o[9], o[9], false, false);
    hst128(v4u{o[0], o[1], o[2], o[3]}, ws, h1m, 0);
    hst128(v4u{o[4], o[5], o[6], o[7]}, ws, h1m + 1024, 0);
    hst128(v4u{s0[0], s1[0], s0[1], s1[1]}, ws, h1s, 0);
    STAMP(19);
    DRAIN();
    WGBAR();
    STAMP(20);
    if (w == 0) hst64(tag, ws, f1pub, 0);
    img3(16, 24, 35);
    if (l15 == 0) {
#pragma unroll
      for (int t = 0; t < 7; ++t) *(LAS f4*)(L + (w * 25 + 24) * 1024 + (t * 4 + g) * 16) = csp[t];
    }
    if (w == 0) poll64(ws, f1wait, tag);
    WGBAR();
    STAMP(21);
  }
  v4u xr[2];
  v2u xro;
  unsigned x1p[10];
  {
    v4u hr[2];
    v2u hro;
    f4 acc[5];
    gemm_ks<5, 12>(ws, O_H1 + (4 * tm * 20 + 5 * w) * 1024, 20 * 1024, b2, L, w, lane, acc, [&](int n) {
      if (n == 0) hr[0] = hld128(ws, lane * 16, O_HP + own_m);
      else if (n == 1) hr[1] = hld128(ws, lane * 16 + 1024, O_HP + own_m);
      else if (n == 2) hro = hld64(ws, own_r, O_HP + own_s);
      else {
        lb4(2 * n - 2);
        lb4(2 * n - 1);
      }
    }, [&]() { lb4(22); }, [](int) {} STPASS(22));
#pragma unroll
    for (int t = 0; t < 5; ++t) {
      const unsigned h0 = t < 4 ? hr[t >> 1][2 * (t & 1)] : hro.x, h1 = t < 4 ? hr[t >> 1][2 * (t & 1) + 1] : hro.y;
      const f2 p = f2{acc[t][0], acc[t][1]} + up2(h0), q = f2{acc[t][2], acc[t][3]} + up2(h1);
      x1p[2 * t] = pk2(p.x, p.y);
      x1p[2 * t + 1] = pk2(q.x, q.y);
    }
    const auto s0 = __builtin_amdgcn_permlane16_swap(x1p[8], x1p[8], false, false);
    const auto s1 = __builtin_amdgcn_permlane16_swap(x1p[9], x1p[9], false, false);
    hst128(v4u{x1p[0], x1p[1], x1p[2], x1p[3]}, ws, lane * 16, O_X1 + own_m);
    hst128(v4u{x1p[4], x1p[5], x1p[6], x1p[7]}, ws, lane * 16 + 1024, O_X1 + own_m);
    hst128(v4u{s0[0], s1[0], s0[1], s1[1]}, ws, own_w, O_X1 + own_s);
    float t1 = 0.f, t2 = 0.f;
#pragma unroll
    for (int i = 0; i < 10; ++i) {
      t1 = __builtin_amdgcn_fdot2_f32_bf16(__builtin_bit_cast(bf2, x1p[i]), __builtin_bit_cast(bf2, 0x3f803f80u), t1, false);
      t2 = __builtin_amdgcn_fdot2_f32_bf16(__builtin_bit_cast(bf2, x1p[i]), __builtin_bit_cast(bf2, x1p[i]), t2, false);
    }
    t1 = xrow_sum(t1);
    t2 = xrow_sum(t2);
    hst64(v2u{__builtin_bit_cast(unsigned, t1), __builtin_bit_cast(unsigned, t2)}, ws, lane < 16 ? lane * 8 : OOB,
          O_STAT + ((4 * tm + w) * 8 + tn) * 128);
    STAMP(25);
    {
      f4 cp[28];
#pragma unroll
      for (int t = 0; t < 28; ++t) cp[t] = *(LAS f4*)(L + ((t / 7) * 25 + 24) * 1024 + ((t % 7) * 4 + g) * 16);
      PIN();
#pragma unroll
      for (int t = 0; t < 7; ++t) {
        csp[t] = (cp[t] + cp[7 + t]) + (cp[14 + t] + cp[21 + t]);
        KEEP(csp[t]);
      }
    }
    PIN();
    DRAIN();
    WGBAR();
    STAMP(26);
    if (w == 0) hst64(tag, ws, f2pub, 0);
    img3(24, 35, 0);
    if (w == 1 && l15 == 0) {
#pragma unroll
      for (int t = 0; t < 7; ++t) *(LAS f4*)(L + CSUM_OFF + (t * 4 + g) * 16) = csp[t];
    }
    if (w == 0) poll64(ws, f2wait, tag);
    WGBAR();
    STAMP(27);
  }
#define PUBK 10
#define PUBN 9
#define SMPN 23
#define CHKN 28
#define WIN4_ 10
  constexpr int WIN4 = WIN4_, RING4 = WIN4 + 1;
  unsigned f3pub = lane == 0 ? O_F3 + rg * 512 + (w >> 1) * 256 + cg * 16 + (w & 1) * 8 : OOB;
  unsigned f3wait = lane < 8 ? O_F3 + rg * 512 + (tm & 1) * 256 + w * 64 + lane * 8 : OOB;
  asm volatile("" : "+v"(f3pub), "+v"(f3wait));
  v4u a4[RING4];
  auto lda4 = [&](int n) {
    const int i = n < 32 ? n >> 3 : (n - 32) / 6, k = ks4(n < 32 ? n & 7 : 8 + (n - 32) % 6);
    a4[n % RING4] = hld128(ws, lane * 16 + (k & 3) * 1024, O_H + ((4 * tm + ((w + 1 + i) & 3)) * 56 + 14 * w + (k & ~3)) * 1024);
  };
  {
    constexpr int W3 = 6;
    v4u a[20][2];
    const unsigned sa = O_X1 + (8 * rg + 2 * w) * 20 * 1024;
    auto lda = [&](int k, int i) { a[k][i] = hld128(ws, lane * 16 + (k & 3) * 1024, sa + i * 20480 + (k >> 2) * 4096); };
#pragma unroll
    for (int k = 0; k < W3; ++k)
#pragma unroll
      for (int i = 0; i < 2; ++i) {
        lda(k, i);
        PIN();
      }
    f4 acc[2][7];
    v2u sv[2][2];
    f2 r, den, mean;
    auto bslot = [](int k, int t) { const int v = k / 5, i = (k % 5) * 7 + t; return (i < 25 ? v * 25 + i : 75 + v * 10 + i) * 1024; };
    const LAS char* Lb = L + lane * 16;
    const LAS char* Lb2 = L + lane * 16 + 65536;
    const LAS char* Lb3 = L + lane * 16 + 131072;
    auto rdb = [&](int k, int t) { const int o = bslot(k, t); return *(const LAS v4u*)(o < 65536 ? Lb + o : (o < 131072 ? Lb2 + (o - 65536) : Lb3 + (o - 131072))); };
    v4u bf[3][4];
#pragma unroll
    for (int t = 0; t < 8; ++t) {
      bf[t >> 2][t & 3] = rdb(t >> 2, t & 3);
      PIN();
    }
    sfor<0, 20>([&](auto ic) {
      constexpr int k = decltype(ic)::v;
      __builtin_amdgcn_s_waitcnt(k < 19 ? 0xC47F : 0xC37F);
#pragma unroll
      for (int t = 0; t < 4; ++t) {
        if (k == 0) mfma_va0(acc[0][t], bf[0][t], a[0][0]);
        else mfma_va(acc[0][t], bf[k % 3][t], a[k][0]);
        if (k < 18) bf[(k + 2) % 3][t] = rdb(k + 2, t);
        else if (t < 3) bf[(k + 2) % 3][t] = rdb(k - 18, 4 + t);
        PIN();
      }
#pragma unroll
      for (int t = 0; t < 4; ++t) {
        if (k == 0) mfma_va0(acc[1][t], bf[0][t], a[0][1]);
        else mfma_va(acc[1][t], bf[k % 3][t], a[k][1]);
        if (t < 2) {
          if constexpr (k + W3 < 20) lda(k + W3, t);
          else if constexpr (k + W3 < 22) sv[k + W3 - 20][t] = hld64(ws, l15 * 8 + g * 256 + t * 128, O_STAT + (8 * rg + 2 * w + k + W3 - 20) * 1024);
          else lb4(23 + 2 * (k + W3 - 22) + t);
        }
        PIN();
      }
      if (k == 0) STAMP(28);
    });
    STAMP(29);
    const unsigned hs = O_H + ((8 * rg + 2 * w) * 56 + 7 * (cg >> 1) + 4 * (cg & 1)) * 1024 + lane * 16;
    unsigned lcs = CSUM_OFF + g * 16;
    KEEP(lcs);
#pragma unroll
    for (int t = 0; t < 4; ++t) csp[t] = *(LAS f4*)(L + lcs + t * 64);
    MFENCE();
    PIN();
    {
      asm volatile("" : "+v"(sv[0][0]), "+v"(sv[0][1]), "+v"(sv[1][0]), "+v"(sv[1][1]));
      const f2 p0 = __builtin_bit_cast(f2, sv[0][0]) + __builtin_bit_cast(f2, sv[0][1]);
      const f2 p1 = __builtin_bit_cast(f2, sv[1][0]) + __builtin_bit_cast(f2, sv[1][1]);
      f2 mr;
      ln_scal(f2{xrow_sum(p0.x), xrow_sum(p1.x)}, f2{xrow_sum(p0.y), xrow_sum(p1.y)}, r, mr, &mean, &den);
    }
#pragma unroll
    for (int i = 0; i < 2; ++i) {
      f2 v[8];
      unsigned o[8];
#pragma unroll
      for (int e = 0; e < 8; ++e) {
        const int t = e >> 1, h = 2 * (e & 1);
        v[e] = f2{acc[i][t][h], acc[i][t][h + 1]} - f2{csp[t][h], csp[t][h + 1]} * mean[i];
      }
      gelu_pack<8>(v, r[i], den[i], o, [&](int m) {
        if (i == 0) { lb4(31 + 2 * m); lb4(32 + 2 * m); }
        else if (m == 0) lb4(39);
      });
      hst128(v4u{o[0], o[1], o[2], o[3]}, ws, hs, i * 57344);
      hst128(v4u{o[4], o[5], o[6], o[7]}, ws, hs, i * 57344 + 1024);
    }
    YOUNG();
    STAMP(30);
    sfor<0, 20>([&](auto ic) {
      constexpr int k = decltype(ic)::v;
      __builtin_amdgcn_s_waitcnt(k < 19 ? 0xC37F : 0xC07F);
      if (k == PUBK) {
        WAITV(12);
        hst64(tag, ws, f3pub, 0);
      }
#pragma unroll
      for (int e = 0; e < 6; ++e) {
        const int i = e / 3, t = e % 3;
        if (k == 0) mfma_va0(acc[i][4 + t], bf[2][t], a[0][i]);
        else mfma_va(acc[i][4 + t], bf[(k + 2) % 3][t], a[k][i]);
        if (i == 0 && k < 18) bf[(k + 1) % 3][t] = rdb(k + 2, 4 + t);
        if (k >= 1 && k < 6 && e == 5) lb4(39 + k);
        if (k >= 1 && k < 19 && e == 3) lb4(44 + k);
        PIN();
      }
    });
    v2u fl;
#pragma unroll
    for (int t = 4; t < 7; ++t) csp[t] = *(LAS f4*)(L + lcs + t * 64);
    MFENCE();
    PIN();
    STAMP(37);
    unsigned os[2][2];
#pragma unroll
    for (int i = 0; i < 2; ++i) {
      f2 v[6];
      unsigned o[6];
#pragma unroll
      for (int e = 0; e < 6; ++e) {
        const int t = 4 + (e >> 1), h = 2 * (e & 1);
        v[e] = f2{acc[i][t][h], acc[i][t][h + 1]} - f2{csp[t][h], csp[t][h + 1]} * mean[i];
      }
      gelu_pack<6>(v, r[i], den[i], o, [&](int m) {
        if (i == 0 && m == 1) fl = hld64(ws, f3wait, 0);
        if (i == 1 && m == 0) { LB4(63, 70); }
      });
      hst128(v4u{o[0], o[1], o[2], o[3]}, ws, hs, i * 57344 + 2048);
      os[i][0] = o[4];
      os[i][1] = o[5];
      if (i == 0) {
        PIN();
        KEEP(fl);
        if (!__all(f3wait >= OOB || (fl.x == tag.x && fl.y == tag.y))) poll64(ws, f3wait, tag);
        __builtin_amdgcn_fence(__ATOMIC_ACQUIRE, "wavefront");
        STAMP(32);
#pragma unroll
        for (int n = 0; n < WIN4; ++n) {
          lda4(n);
          PIN();
        }
      }
    }
    {
      const auto s0 = __builtin_amdgcn_permlane16_swap(os[0][0], os[1][0], false, false);
      const auto s1 = __builtin_amdgcn_permlane16_swap(os[0][1], os[1][1], false, false);
      hst128(v4u{s0[0], s1[0], s0[1], s1[1]}, ws, (g & 1) * 57344 + (l15 + 16 * (g >> 1)) * 16,
             O_H + ((8 * rg + 2 * w) * 56 + 7 * (cg >> 1) + 3) * 1024 + (cg & 1) * 512);
    }
    YOUNG();
    STAMP(31);
  }
  {
    v4u gb[2];
    v2u gbo, fl;
    f4 ac[4][5], p[15];
    f2 gf[10], cf[10];
    unsigned lq = lane;
    KEEP(lq);
    const unsigned lr = RED_OFF + lq * 16, gq4 = lq >> 4;
    unsigned dm = ((64 * tm + 16 * w + (lq & 15)) * D_ + 80 * tn) * 2, ds = dm + 8 * gq4 + ((tn & 1) ? 0 : 128);
    dm += 16 * gq4 + 32 * (tn & 1);
    sfor<0, 56>([&](auto ic) {
      constexpr int n = decltype(ic)::v, i = n < 32 ? n >> 3 : (n - 32) / 6, sl = n < 32 ? n & 7 : 8 + (n - 32) % 6;
      if (n == 52) {
        STAMP(35);
        WGBAR();
        STAMP(36);
      }
      if (n == PUBN) {
        WAITV(8);
        hst64(tag, ws, f3pub + (O_F4 - O_F3), 0);
        STAMP(33);
      }
      if (n == 40) asm volatile("" : "+v"(dm), "+v"(ds));
      if (n == 37) asm volatile("s_barrier" ::: "memory");
      if (n == CHKN) {
        KEEP(fl);
        if (!__all(f3wait >= OOB || (fl.x == tag.x && fl.y == tag.y))) poll64(ws, f3wait + (O_F4 - O_F3), tag);
        __builtin_amdgcn_fence(__ATOMIC_ACQUIRE, "wavefront");
#pragma unroll
        for (int m = 32; m < CHKN + WIN4; ++m) lda4(m);
        PIN();
      }
#pragma unroll
      for (int t = 0; t < 5; ++t) {
        const int f = sl * 5 + t;
        if (sl == 0) mfma_av0(ac[i][t], b4[f], a4[n % RING4]);
        else if (f >= 23 && f < 40) mfma_v(ac[i][t], b4[f], a4[n % RING4]);
        else mfma_av(ac[i][t], b4[f], a4[n % RING4]);
        if (t == 1) {
          if constexpr (n + WIN4 < 56) {
            if (n < 32 - WIN4 || n >= CHKN) lda4(n + WIN4);
          }
        }
        if (t == 3) {
          if (n == 28 || n == 29) gb[n & 1] = hld128(ws, gq4 * 16 + (n & 1) * 64, O_MOD + (5 * D_ + 80 * tn + 16 * (tn & 1)) * 2);
          if (n == 30) gbo = hld64(ws, gq4 * 8, O_MOD + (5 * D_ + 80 * tn + ((tn & 1) ? 0 : 64)) * 2);
          if (n == 31 || n == 32) xr[n & 1] = hld128(ws, lq * 16 + (n & 1) * 1024, O_X + own_m);
          if (n == 33) xro = hld64(ws, ((lq & 15) + 16 * (gq4 >> 1)) * 16 + 8 * (gq4 & 1), O_X + own_s);
        }
        if (n >= 45 && n < 50 && t < 4) {
          const int e = (n - 45) * 4 + t, q = e >> 1;
          unsigned xw = q < 8 ? xr[q >> 2][q & 3] : xro[q & 1], gw = q < 8 ? gb[q >> 2][q & 3] : gbo[q & 1];
          asm volatile("" : "+v"(xw), "+v"(gw));
          gf[q][e & 1] = (e & 1) ? hi2f(gw) : lo2f(gw);
          cf[q][e & 1] = fma1((e & 1) ? hi2f(x1p[q]) : lo2f(x1p[q]), gf[q][e & 1], (e & 1) ? hi2f(xw) : lo2f(xw));
          if (e & 1) asm volatile("" : "+v"(gf[q]), "+v"(cf[q]));
        }
        if (n == SMPN && t == 0) fl = hld64(ws, f3wait + (O_F4 - O_F3), 0);
        if (n >= 38 && sl == 8) *(LAS f4*)(L + (lr + ((w + i) & 3) * 15360) + ((i - 1) * 5 + t) * 1024) = ac[i > 0 ? i - 1 : 0][t];
        if (n == 52) {
          p[t] = *(LAS f4*)(L + (lr + w * 15360) + t * 1024);
          p[5 + t] = *(LAS f4*)(L + (lr + w * 15360) + (5 + t) * 1024);
        }
        if (n == 53) p[10 + t] = *(LAS f4*)(L + (lr + w * 15360) + (10 + t) * 1024);
        if (n >= 54) {
          const int h = 2 * (n - 54);
          p[t][h] = add1(p[t][h], p[5 + t][h]);
          p[t][h + 1] = add1(p[t][h + 1], p[5 + t][h + 1]);
          KEEP(p[t]);
        }
        PIN();
      }
      if (n == 0) STAMP(34);
    });
    MFENCE();
    PIN();
    unsigned o[10];
#pragma unroll
    for (int t = 0; t < 5; ++t) {
      const f4 y = (ac[3][t] + p[t]) + p[10 + t];
      const f2 u = f2{y[0], y[1]} * gf[2 * t] + cf[2 * t], v = f2{y[2], y[3]} * gf[2 * t + 1] + cf[2 * t + 1];
      o[2 * t] = pk2(u.x, u.y);
      o[2 * t + 1] = pk2(v.x, v.y);
    }
    const rsrc_t ro = mkrs(gout, SD2);
    __builtin_amdgcn_raw_buffer_store_b128(v4u{o[0], o[1], o[2], o[3]}, ro, dm, 0, 0);
    __builtin_amdgcn_raw_buffer_store_b128(v4u{o[4], o[5], o[6], o[7]}, ro, dm + 64, 0, 0);
    __builtin_amdgcn_raw_buffer_store_b64(v2u{o[8], o[9]}, ro, ds, 0, 0);
  }
  asm volatile(".Lfused_end:" ::: "memory");
}

extern "C" void kernel_launch(void* const* d_in, const int* in_sizes, int n_in, void* d_out, int out_size,
                              void* d_ws, size_t ws_size, hipStream_t stream) {
  (void)hipFuncSetAttribute((const void*)fused, hipFuncAttributeMaxDynamicSharedMemorySize, LDS_BYTES);
  fused<<<256, 256, LDS_BYTES, stream>>>(d_in[0], d_in[1], d_in[2], d_in[3], d_in[4], d_in[5], d_in[7], d_in[8], d_in[9],
                                         d_in[10], d_in[11], d_in[12], d_out, d_ws);
}
```

```cpp
#include <hip/hip_runtime.h>
#include <stdint.h>

typedef unsigned v4u __attribute__((ext_vector_type(4)));
typedef unsigned v2u __attribute__((ext_vector_type(2)));
typedef float f4 __attribute__((ext_vector_type(4)));
typedef float f2 __attribute__((ext_vector_type(2)));
typedef short s8v __attribute__((ext_vector_type(8)));
typedef short s4v __attribute__((ext_vector_type(4)));
typedef __bf16 bf2 __attribute__((ext_vector_type(2)));
typedef __bf16 bf8 __attribute__((ext_vector_type(8)));
typedef __amdgpu_buffer_rsrc_t rsrc_t;
#define LAS __attribute__((address_space(3)))
#define GAS __attribute__((address_space(1)))
#define DI __device__ __forceinline__

static constexpr int S_ = 2048, D_ = 640, F_ = 1728;
static constexpr unsigned OOB = 0x80000000u;

static constexpr unsigned O_EPOCH = 0;
static constexpr unsigned O_FP = 1024;
static constexpr unsigned O_FR = O_FP + 16 * 128;
static constexpr unsigned O_F1 = O_FR + 32 * 128;
static constexpr unsigned O_F2 = O_F1 + 32 * 128;
static constexpr unsigned O_F3 = O_F2 + 16 * 128;
static constexpr unsigned O_F4 = O_F3 + 16 * 512;
static constexpr unsigned O_CNT = O_F4 + 16 * 512;
static constexpr unsigned O_FW = O_CNT + 64 * 128;
static constexpr unsigned O_FB = O_FW + 8192;
static constexpr unsigned O_MOD = 65536;
static constexpr unsigned O_STAT = O_MOD + 16384;
static constexpr unsigned O_KVT = O_STAT + 131072;
static constexpr unsigned SD2 = S_ * D_ * 2;
static constexpr unsigned KVP = 4096 + 128, ZP = 10240 + 128;
static constexpr unsigned O_ZT = O_KVT + D_ * KVP;
static constexpr unsigned O_A1 = O_ZT + 256 * ZP;
static constexpr unsigned O_HP = O_A1 + SD2;
static constexpr unsigned O_X = O_HP + SD2;
static constexpr unsigned O_H1 = O_X + SD2;
static constexpr unsigned O_X1 = O_H1 + SD2;
static constexpr unsigned O_H = O_X1 + SD2;
static constexpr unsigned O_W1 = O_H + 128 * 56 * 1024;
static constexpr unsigned O_W2 = O_W1 + 800 * 1024;
static constexpr unsigned O_W3 = O_W2 + 800 * 1024;
static constexpr unsigned O_W4 = O_W3 + 2240 * 1024;
static constexpr unsigned O_END = O_W4 + 2240 * 1024;
static constexpr unsigned O_DBG = O_END;

static constexpr int CS = 4160;
static constexpr int UOFF = 8 * CS;
static constexpr int WR = 8 * CS + 4608;
static constexpr int LDS_BYTES = 160 * 1024;
static constexpr int RED_OFF = 100 * 1024;
static constexpr int CSUM_OFF = 140 * 1024;
static constexpr unsigned SPIN_CAP = 200000;

template <int V> struct IC { static constexpr int v = V; };
template <int I, int N, class F> DI void sfor(F&& f) {
  if constexpr (I < N) {
    f(IC<I>{});
    sfor<I + 1, N>(f);
  }
}
DI float lo2f(unsigned w) { return __builtin_bit_cast(float, w << 16); }
DI float hi2f(unsigned w) { return __builtin_bit_cast(float, w & 0xffff0000u); }
DI f2 up2(unsigned w) { return f2{lo2f(w), hi2f(w)}; }
DI unsigned pk2(float a, float b) { return __builtin_bit_cast(unsigned, __builtin_convertvector(f2{a, b}, bf2)); }
DI f4 mfma(v4u a, v4u b, f4 c) {
  return __builtin_amdgcn_mfma_f32_16x16x32_bf16(__builtin_bit_cast(bf8, a), __builtin_bit_cast(bf8, b), c, 0, 0, 0);
}
DI void mfma_v(f4& c, v4u a, v4u b) { asm volatile("v_mfma_f32_16x16x32_bf16 %0, %1, %2, %0" : "+v"(c) : "v"(a), "v"(b)); }
DI void mfma_v0(f4& c, v4u a, v4u b) { asm volatile("v_mfma_f32_16x16x32_bf16 %0, %1, %2, 0" : "=&v"(c) : "v"(a), "v"(b)); }
DI void mfma_va(f4& c, v4u a, v4u b) { asm volatile("v_mfma_f32_16x16x32_bf16 %0, %1, %2, %0" : "+v"(c) : "v"(a), "a"(b)); }
DI void mfma_va0(f4& c, v4u a, v4u b) { asm volatile("v_mfma_f32_16x16x32_bf16 %0, %1, %2, 0" : "=&v"(c) : "v"(a), "a"(b)); }
DI void mfma_av(f4& c, v4u a, v4u b) { asm volatile("v_mfma_f32_16x16x32_bf16 %0, %1, %2, %0" : "+v"(c) : "a"(a), "v"(b)); }
DI void mfma_av0(f4& c, v4u a, v4u b) { asm volatile("v_mfma_f32_16x16x32_bf16 %0, %1, %2, 0" : "=&v"(c) : "a"(a), "v"(b)); }
DI void mfma_aa(f4& c, v4u a, v4u b) { asm volatile("v_mfma_f32_16x16x32_bf16 %0, %1, %2, %0" : "+v"(c) : "a"(a), "a"(b)); }
DI void mfma_aa0(f4& c, v4u a, v4u b) { asm volatile("v_mfma_f32_16x16x32_bf16 %0, %1, %2, 0" : "=&v"(c) : "a"(a), "a"(b)); }
#define KEEP(x) asm volatile("" : "+v"(x))
#define MFENCE() asm volatile("s_nop 7\n\ts_nop 1" ::: "memory")
DI float add1(float a, float b) { float r; asm("v_add_f32 %0, %1, %2" : "=v"(r) : "v"(a), "v"(b)); return r; }
DI float fma1(float a, float b, float c) { float r; asm("v_fma_f32 %0, %1, %2, %3" : "=v"(r) : "v"(a), "v"(b), "v"(c)); return r; }
DI rsrc_t mkrs(const void* p, unsigned bytes) { return __builtin_amdgcn_make_buffer_rsrc((void*)p, 0, bytes, 0x00020000); }
#define INAUX 2
DI v4u ld128(rsrc_t r, unsigned v, unsigned s) { return __builtin_amdgcn_raw_buffer_load_b128(r, v, s, INAUX); }
DI v2u ld64(rsrc_t r, unsigned v, unsigned s) { return __builtin_amdgcn_raw_buffer_load_b64(r, v, s, INAUX); }
DI unsigned ld32(rsrc_t r, unsigned v, unsigned s) { return __builtin_amdgcn_raw_buffer_load_b32(r, v, s, INAUX); }
DI v4u hld128(rsrc_t r, unsigned v, unsigned s) { return __builtin_amdgcn_raw_buffer_load_b128(r, v, s, 16); }
DI v2u hld64(rsrc_t r, unsigned v, unsigned s) { return __builtin_amdgcn_raw_buffer_load_b64(r, v, s, 16); }
DI void hst128(v4u x, rsrc_t r, unsigned v, unsigned s) { __builtin_amdgcn_raw_buffer_store_b128(x, r, v + s, 0, 16); }
DI void hst64(v2u x, rsrc_t r, unsigned v, unsigned s) { __builtin_amdgcn_raw_buffer_store_b64(x, r, v, s, 16); }
DI void hst32(unsigned x, rsrc_t r, unsigned v, unsigned s) { __builtin_amdgcn_raw_buffer_store_b32(x, r, v, s, 16); }
#define DRAIN() do { asm volatile("s_waitcnt vmcnt(0)" ::: "memory"); __builtin_amdgcn_s_waitcnt(0x0F70); } while (0)
#define WGBAR() asm volatile("s_waitcnt lgkmcnt(0)\n\ts_barrier" ::: "memory")
#define PIN() __builtin_amdgcn_sched_barrier(0)

template <int CTRL> DI float dpp(float x) {
  return __builtin_bit_cast(float, __builtin_amdgcn_mov_dpp(__builtin_bit_cast(int, x), CTRL, 0xf, 0xf, true));
}
DI float xrow_sum(float x) {
  auto s = __builtin_amdgcn_permlane16_swap(__builtin_bit_cast(unsigned, x), __builtin_bit_cast(unsigned, x), false, false);
  x = __builtin_bit_cast(float, (unsigned)s[0]) + __builtin_bit_cast(float, (unsigned)s[1]);
  auto t = __builtin_amdgcn_permlane32_swap(__builtin_bit_cast(unsigned, x), __builtin_bit_cast(unsigned, x), false, false);
  return __builtin_bit_cast(float, (unsigned)t[0]) + __builtin_bit_cast(float, (unsigned)t[1]);
}
DI float swap32_add(float a, float b) {
  auto t = __builtin_amdgcn_permlane32_swap(__builtin_bit_cast(unsigned, a), __builtin_bit_cast(unsigned, b), false, false);
  return __builtin_bit_cast(float, (unsigned)t[0]) + __builtin_bit_cast(float, (unsigned)t[1]);
}
DI float swap16_add(float a, float b) {
  auto t = __builtin_amdgcn_permlane16_swap(__builtin_bit_cast(unsigned, a), __builtin_bit_cast(unsigned, b), false, false);
  return __builtin_bit_cast(float, (unsigned)t[0]) + __builtin_bit_cast(float, (unsigned)t[1]);
}
DI float rdl(float v, int l) { return __builtin_bit_cast(float, __builtin_amdgcn_readlane(__builtin_bit_cast(int, v), l)); }
DI void wave_sum4(f2& s, f2& ss) {
  float v = swap16_add(swap32_add(s.x, s.y), swap32_add(ss.x, ss.y));
  v += dpp<0xB1>(v);
  v += dpp<0x4E>(v);
  v += dpp<0x141>(v);
  v += dpp<0x128>(v);
  s = f2{rdl(v, 0), rdl(v, 32)};
  ss = f2{rdl(v, 16), rdl(v, 48)};
}
DI void poll64(rsrc_t ws, unsigned voff, v2u want) {
  const bool idle = voff >= OOB;
  for (unsigned spin = 0; spin < SPIN_CAP; ++spin) {
    asm volatile("" ::: "memory");
    const v2u v = hld64(ws, voff, 0);
    if (__all(idle || (v.x == want.x && v.y == want.y))) break;
  }
  __builtin_amdgcn_fence(__ATOMIC_ACQUIRE, "wavefront");
}

DI bool wflags_ok(v4u a, int lane, int nl, v2u want) {
  return __all(lane >= nl || (a.x == want.x && a.y == want.y && a.z == want.x && a.w == want.y));
}
DI void pollw(rsrc_t ws, unsigned voff, int lane, int nl, v2u want) {
  for (unsigned spin = 0; spin < SPIN_CAP; ++spin) {
    asm volatile("" ::: "memory");
    if (wflags_ok(hld128(ws, voff, 0), lane, nl, want)) break;
    __builtin_amdgcn_s_sleep(4);
  }
  __builtin_amdgcn_fence(__ATOMIC_ACQUIRE, "wavefront");
}
DI float rbf(float x) { return lo2f(pk2(x, 0.f)); }
DI void ln_scal(f2 s, f2 ss, f2& r, f2& mr, f2* pmean = nullptr, f2* pden = nullptr) {
  const f2 m = s * (1.f / D_);
  const f2 var = ss * (1.f / D_) - m * m;
  const f2 v = f2{fmaxf(var.x, 0.f), fmaxf(var.y, 0.f)} + 1e-5f;
  r = f2{__builtin_amdgcn_rsqf(v.x), __builtin_amdgcn_rsqf(v.y)};
  mr = -m * r;
  if (pmean) *pmean = m;
  if (pden) *pden = v * r;
}
template <int N, class Mid> DI void gelu_pack(const f2 (&x)[N], float r, float den, unsigned* o, Mid mid) {
  const float c0 = -2.f * 0.7978845608028654f * 1.4426950408889634f * r, c1 = c0 * 0.044715f * r * r;
  f2 a[N];
#pragma unroll
  for (int i = 0; i < N; ++i) a[i] = x[i] * x[i] * c1 + c0;
  mid(0);
  PIN();
#pragma unroll
  for (int i = 0; i < N; ++i) a[i] *= x[i];
  mid(1);
  PIN();
#pragma unroll
  for (int i = 0; i < N; ++i) a[i] = f2{__builtin_amdgcn_exp2f(a[i].x), __builtin_amdgcn_exp2f(a[i].y)};
  mid(2);
  PIN();
#pragma unroll
  for (int i = 0; i < N; ++i) a[i] = a[i] * den + den;
  PIN();
#pragma unroll
  for (int i = 0; i < N; ++i) a[i] = f2{__builtin_amdgcn_rcpf(a[i].x), __builtin_amdgcn_rcpf(a[i].y)};
  mid(3);
  PIN();
#pragma unroll
  for (int i = 0; i < N; ++i) a[i] *= x[i];
  PIN();
#pragma unroll
  for (int i = 0; i < N; ++i) o[i] = pk2(a[i].x, a[i].y);
}

#define STAMP(i)
#define STARG
#define STPASS(n)

static constexpr int RT_BYTES = 32 * 896;
template <bool WIDE> struct Job {
  static constexpr int NL = WIDE ? 7 : 5, RB = WIDE ? 896 : 640, PITCH = WIDE ? F_ * 2 : D_ * 2;
  v4u r[NL];
  unsigned dst;
  DI void load(int job, int w, const unsigned (&vo)[NL], const void* w1, const void* w2, const void* wx) {
    const bool big = WIDE || job >= 80;
    const int jj = WIDE ? job : (job >= 80 ? job - 80 : (job >= 40 ? job - 40 : job));
    const int gq = WIDE ? jj & 3 : jj & 1, ks = WIDE ? jj >> 2 : jj >> 1;
    const rsrc_t rs = mkrs(big ? wx : (job >= 40 ? w2 : w1), big ? D_ * F_ * 2 : D_ * D_ * 2);
    const unsigned src = (32 * ks + 8 * w) * PITCH + gq * RB;
    dst = (WIDE ? O_W3 : (job >= 80 ? O_W4 : (job >= 40 ? O_W2 : O_W1))) + ((gq * 4 + w) * (!WIDE && job >= 80 ? 56 : 20) + ks) * (NL * 1024);
#pragma unroll
    for (int i = 0; i < NL; ++i)
      r[i] = ld128(rs, (WIDE && gq == 3 && (vo[i] & 1)) ? OOB : (vo[i] & ~1u) + src, 0);
  }
  DI void put(int w, int lane, LAS char* L) {
#pragma unroll
    for (int i = 0; i < NL; ++i) *(LAS v4u*)(L + w * (8 * RB) + lane * 16 + i * 1024) = r[i];
  }
  DI void emit(int w, int lane, LAS char* L, rsrc_t ws) {
    const int g = lane >> 4, q = (lane & 15) >> 2, p = lane & 3;
    LAS char* row = L + (8 * g + q) * RB + w * (RB / 4);
    LAS char* pr = row + 32 * (w & 1) + 16 * p;
    LAS char* sg = row + ((w & 1) ? 0 : RB / 4 - 32) + 8 * p;
    v2u lo[NL], hi[NL];
#pragma unroll
    for (int f = 0; f < NL; ++f) {
      LAS char* c = f < NL - 1 ? pr + 64 * (f >> 1) + 8 * (f & 1) : sg;
      lo[f] = __builtin_bit_cast(v2u, __builtin_amdgcn_ds_read_tr16_b64_v4i16((LAS s4v*)c));
      hi[f] = __builtin_bit_cast(v2u, __builtin_amdgcn_ds_read_tr16_b64_v4i16((LAS s4v*)(c + 4 * RB)));
    }
#pragma unroll
    for (int f = 0; f < NL; ++f) hst128(v4u{lo[f].x, lo[f].y, hi[f].x, hi[f].y}, ws, lane * 16 + dst, f * 1024);
  }
};

template <class Extra, class Gap>
DI void gemm_ks(rsrc_t ws, unsigned abase, const unsigned (&vo)[4], const v4u* b, LAS char* L, int w, int lane, f4 (&acc)[5],
                Extra extra, Gap gap STARG) {
#define WIN12 12
  constexpr int KS = 5, WIN = WIN12, NA = 4 * KS, RING = WIN + 1;
  v4u a[RING];
  unsigned so[4];
#pragma unroll
  for (int i = 0; i < 4; ++i) so[i] = abase + ((w + 1 + i) & 3) * 20480;
  auto lda = [&](int n) { a[n % RING] = hld128(ws, vo[n / KS] + ((n % KS) & 3) * 1024, so[n / KS] + ((n % KS) >> 2) * 4096); };
#pragma unroll
  for (int n = 0; n < WIN; ++n) {
    lda(n);
    PIN();
  }
  f4 ac[2][5], p[15];
  unsigned lr = RED_OFF + lane * 16;
  KEEP(lr);
  sfor<0, NA>([&](auto ic) {
    constexpr int n = decltype(ic)::v, i = n / KS, k = n % KS;
    if (n == NA - 3) {
      STAMP(sb + 1);
      WGBAR();
      STAMP(sb + 2);
    }
#pragma unroll
    for (int t = 0; t < 5; ++t) {
      if (k == 0) mfma_av0(ac[i & 1][t], b[t], a[n % RING]);
      else mfma_av(ac[i & 1][t], b[k * 5 + t], a[n % RING]);
      if (t == 1) {
        if constexpr (n + WIN < NA) lda(n + WIN);
      }
      if (t == (n + WIN < NA ? 4 : 1) && n >= NA - 12) extra(n - (NA - 12));
      if (k == 0 && i > 0 && t > 0) *(LAS f4*)(L + (lr + ((w + i) & 3) * 15360) + ((i - 1) * 5 + t - 1) * 1024) = ac[(i - 1) & 1][t - 1];
      if (k == 1 && i > 0 && t == 0) *(LAS f4*)(L + (lr + ((w + i) & 3) * 15360) + ((i - 1) * 5 + 4) * 1024) = ac[(i - 1) & 1][4];
      if (n == NA - 3) {
        p[t] = *(LAS f4*)(L + (lr + w * 15360) + t * 1024);
        p[5 + t] = *(LAS f4*)(L + (lr + w * 15360) + (5 + t) * 1024);
      }
      if (n == NA - 2) p[10 + t] = *(LAS f4*)(L + (lr + w * 15360) + (10 + t) * 1024);
      if (n == NA - 1) {
        p[t][0] = add1(p[t][0], p[5 + t][0]);
        p[t][1] = add1(p[t][1], p[5 + t][1]);
        KEEP(p[t]);
      }
      gap(n, t);
      PIN();
    }
    if (n == 0) STAMP(sb);
  });
#pragma unroll
  for (int t = 0; t < 5; ++t) {
    p[t][2] += p[5 + t][2];
    p[t][3] += p[5 + t][3];
    p[t] += p[10 + t];
    KEEP(p[t]);
  }
  PIN();
#pragma unroll
  for (int t = 0; t < 5; ++t) acc[t] = ac[1][t] + p[t];
}

#define YOUNG() do { asm volatile("; YOUNG" ::: "memory"); PIN(); } while (0)
#define WAITV(n) asm volatile("s_waitcnt vmcnt(" #n ") ; WAITV" ::: "memory")
__global__ __launch_bounds__(256, 1) void fused(const void*, const void*, const void*, const void*, const void*, const void*,
                                                const void*, const void*, const void*, const void*, const void*, const void*,
                                                void*, void*) {
  extern __shared__ __attribute__((aligned(16))) char smem[];
  LAS char* L = (LAS char*)smem;
  STAMP(0);
  unsigned pf = 0;
  if (blockIdx.x >= 160) {
    unsigned long long pc;
    unsigned off = (((blockIdx.x >> 3) & 1) * 256 + threadIdx.x) * 128;
    asm volatile("s_getpc_b64 %1\n.Lfused_pc:\n\tv_min_u32 %2, .Lfused_end-.Lfused_pc, %2\n\tglobal_load_dword %0, %2, %1"
                 : "=&v"(pf), "=&s"(pc), "+v"(off));
  }
  PIN();
  typedef void* const __attribute__((address_space(4))) * karg_t;
  const karg_t ka = (karg_t)__builtin_amdgcn_kernarg_segment_ptr();
  void *gq = ka[0], *gk = ka[1], *gv = ka[2], *gsk = ka[3], *gssm = ka[4], *gD = ka[5], *gw1 = ka[6], *gw2 = ka[7], *gw3 = ka[8],
       *gw4 = ka[9], *gc = ka[10], *gwm = ka[11], *gout = ka[12], *gws = ka[13];
  asm volatile("" ::"s"(gq), "s"(gk), "s"(gv), "s"(gsk), "s"(gssm), "s"(gD), "s"(gw1), "s"(gw2), "s"(gw3), "s"(gw4),
               "s"(gc), "s"(gwm), "s"(gout), "s"(gws));
  const int tid = threadIdx.x, lane = tid & 63;
  const int w = __builtin_amdgcn_readfirstlane(tid >> 6);
  const int b = blockIdx.x, x = b & 7, j = b >> 3;
  const int tm = 4 * x + (j >> 3), tn = j & 7;
  const int rg = 2 * x + (j >> 4), cg = j & 15;
  const bool conv_wg = j < 20;
  const rsrc_t ws = mkrs(gws, O_END);
  const int g = lane >> 4, l15 = lane & 15;

  const int rt0 = 64 * tm + 8 * tn + 2 * w;
  const int rcm = 8 * lane, rco = 512 + 2 * lane;
  v4u qm[2];
  unsigned qo[2];
  auto load_q = [&]() {
    const rsrc_t rq = mkrs(gq, SD2);
#pragma unroll
    for (int t = 0; t < 2; ++t) {
      qm[t] = ld128(rq, rcm * 2, (rt0 + t) * D_ * 2);
      qo[t] = ld32(rq, rco * 2, (rt0 + t) * D_ * 2);
    }
  };
  v2u tag;
  auto make_tag = [&](v2u ep) {
    tag.x = __builtin_amdgcn_readfirstlane(ep.x) * 0x9E3779B1u + 0x7F4A7C15u;
    tag.y = (__builtin_amdgcn_readfirstlane(ep.y) ^ tag.x) * 0x85EBCA77u + 0x2545F491u;
    tag.x &= 0xffffff00u;
  };
  v4u b1[25], b2[25];
  const unsigned sb1 = O_W1 + (tn * 100 + w * 25) * 1024, sb2 = O_W2 + (tn * 100 + w * 25) * 1024;
  auto zero_h1 = [&](int i) {
    hst128(v4u{0u, 0u, 0u, 0u}, ws, (i < 2 || tid < 128) ? tid * 16 : OOB, O_H1 + b * 10240 + i * 4096);
  };
  v4u mm[5];
  unsigned mo[5];
  auto load_mod = [&](bool ok) {
    unsigned o1 = (ok ? 0u : OOB) | (lane * 16), o2 = (ok ? 0u : OOB) | (lane * 4 + 1024);
    asm volatile("" : "+v"(o1), "+v"(o2));
#pragma unroll
    for (int i = 0; i < 5; ++i) {
      mm[i] = hld128(ws, o1, O_MOD + i * D_ * 2);
      mo[i] = __builtin_amdgcn_raw_buffer_load_b32(ws, o2, O_MOD + i * D_ * 2, 16);
    }
  };
  f2 qv[10], n1[10];
  auto ln1 = [&]() {
#pragma unroll
    for (int i = 0; i < 4; ++i) {
      qv[2 * i] = f2{lo2f(qm[0][i]), lo2f(qm[1][i])};
      qv[2 * i + 1] = f2{hi2f(qm[0][i]), hi2f(qm[1][i])};
    }
    qv[8] = f2{lo2f(qo[0]), lo2f(qo[1])};
    qv[9] = f2{hi2f(qo[0]), hi2f(qo[1])};
    f2 s = qv[0], ss = qv[0] * qv[0];
#pragma unroll
    for (int i = 1; i < 10; ++i) { s += qv[i]; ss += qv[i] * qv[i]; }
    wave_sum4(s, ss);
    f2 r, mr;
    ln_scal(s, ss, r, mr);
#pragma unroll
    for (int i = 0; i < 10; ++i) n1[i] = qv[i] * r + mr;
  };
  unsigned z3off, im0, io0, roff, roff3, own_r, x1m, x1s, stoff, h1m, h1s, f1pub, f1wait, frpub, frwait, f2pub, f2wait, f3pub, f3wait;
  unsigned v2o[4];
  const unsigned own_m = ((4 * tm + w) * 20 + 5 * (tn >> 1) + 3 * (tn & 1)) * 1024;
  const unsigned own_s = ((4 * tm + w) * 20 + 5 * (tn >> 1) + 2) * 1024 + (tn & 1) * 512;
  auto offsets = [&]() {
    z3off = tid < 128 ? tid * 16 + 8192 : OOB;
    im0 = RED_OFF + 16384 + lane * 128 + (((2 * w) ^ (lane & 7)) << 4);
    io0 = RED_OFF + 16384 + (64 + (lane >> 2)) * 128 + (((2 * w) ^ ((lane >> 2) & 7)) << 4) + 4 * (lane & 3);
    roff = O_A1 + ((4 * tm + (tn >> 1)) * 20 + (tid >> 5)) * 1024 + (((tid ^ (tid >> 3)) & 7) + 8 * (tn & 1) + 16 * ((tid >> 3) & 3)) * 16;
    roff3 = tid < 128 ? roff : OOB;
    own_r = (l15 + 16 * (g >> 1)) * 16 + 8 * (g & 1);
    x1m = O_X1 + own_m + lane * 16;
    x1s = (g & 1) ? OOB + SD2 : O_X1 + own_s + (l15 + 16 * (g >> 1)) * 16;
    stoff = (g & 1) ? OOB : O_STAT + ((4 * tm + w) * 8 + tn) * 128 + l15 * 8 + (g >> 1) * 4;
    f3pub = lane == 0 ? O_F3 + rg * 512 + (w >> 1) * 256 + cg * 16 + (w & 1) * 8 : OOB;
    f3wait = lane < 8 ? O_F3 + rg * 512 + (tm & 1) * 256 + w * 64 + lane * 8 : OOB;
    const int sh = (tn >> 1) == 0 ? 1 : ((tn >> 1) == 1 ? -1 : ((tn >> 1) == 2 ? 2 : -2));
    const int t2 = 64 * tm + 16 * w + l15 + sh;
    const bool ok = t2 >= 0 && t2 < S_;
    const unsigned d = O_H1 + ((t2 >> 4) * 20 + 5 * (tn >> 1)) * 1024 + (t2 & 15) * 16;
    h1m = ok ? d + 3072 * (tn & 1) + 256 * g : OOB;
    h1s = (ok && !(g & 1)) ? d + 2048 + 512 * (tn & 1) + 256 * (g >> 1) : OOB;
    const int nb = tm + (sh > 0 ? 1 : -1);
    f1pub = OOB;
    if (lane == 0) f1pub = O_F1 + tm * 128 + tn * 8;
    if (lane == 1 && nb >= 0 && nb < 32) f1pub = O_F1 + nb * 128 + 64 + tn * 8;
    const bool up = ((lane >> 1) & 1) == 0;
    f1wait = (lane < 8 || (lane < 16 && (up ? tm > 0 : tm < 31))) ? O_F1 + tm * 128 + lane * 8 : OOB;
    frpub = lane == 0 ? O_FR + tm * 128 + tn * 8 : OOB;
    frwait = lane < 8 ? O_FR + tm * 128 + lane * 8 : OOB;
    f2pub = lane == 0 ? O_F2 + (tm >> 1) * 128 + ((tm & 1) * 8 + tn) * 8 : OOB;
    f2wait = lane < 16 ? O_F2 + rg * 128 + lane * 8 : OOB;
    asm volatile("" : "+v"(frpub), "+v"(frwait), "+v"(f2pub), "+v"(f2wait));
    asm volatile("" : "+v"(z3off), "+v"(im0), "+v"(io0), "+v"(roff), "+v"(roff3), "+v"(own_r), "+v"(f1pub), "+v"(f1wait));
    asm volatile("" : "+v"(x1m), "+v"(x1s), "+v"(stoff), "+v"(f3pub), "+v"(f3wait));
    asm volatile("" : "+v"(h1m), "+v"(h1s));
  };
  bool wok = false, bok = false;
  if (conv_wg) {
    const int cgp = j < 16 ? x : 8 + (x >> 2), ci = j < 16 ? j : (x & 3) * 4 + (j - 16);
    const int pT0 = 128 * ci + 32 * w + 8 * ((lane >> 3) & 3) + 4 * (lane >> 5), pC0 = 64 * cgp + 8 * (lane & 7);
    const int ch = 64 * cgp + 4 * ci + w;
    v4u kw[7], vw[4], skw[4], kr[4];
    {
      const rsrc_t rk = mkrs(gk, SD2), rv = mkrs(gv, SD2), rs = mkrs(gsk, D_ * 8);
      const unsigned o = (unsigned)(pT0 * D_ + pC0) * 2;
#pragma unroll
      for (int i = 0; i < 7; ++i) kw[i] = ld128(rk, o + (i - 3) * D_ * 2, 0);
#pragma unroll
      for (int i = 0; i < 4; ++i) vw[i] = ld128(rv, o + i * D_ * 2, 0);
#pragma unroll
      for (int i = 0; i < 4; ++i) skw[i] = __builtin_amdgcn_raw_buffer_load_b128(rs, pC0 * 8 + i * 16, 0, 0);
      PIN();
      offsets();
      PIN();
      asm volatile("s_waitcnt vmcnt(4)" ::: "memory");
      const rsrc_t rK = mkrs((const char*)gssm + (size_t)ch * 4096, 4096);
#pragma unroll
      for (int i = 0; i < 4; ++i) kr[i] = ld128(rK, lane * 16 + i * 1024, 0);
    }
    const v2u ep = hld64(ws, lane == 0 ? O_EPOCH : O_FP + x * 256 + (j >= 16) * 128, 0);
    const unsigned dvb = __builtin_amdgcn_raw_buffer_load_b16(mkrs(gD, D_ * 2), ch * 2, 0, 0);
    PIN();
    {
      unsigned pe[8][2];
#pragma unroll
      for (int pr = 0; pr < 4; ++pr) {
        const f2 w0 = {lo2f(skw[pr].x), lo2f(skw[pr].z)}, w1 = {hi2f(skw[pr].x), hi2f(skw[pr].z)};
        const f2 w2 = {lo2f(skw[pr].y), lo2f(skw[pr].w)}, w3 = {hi2f(skw[pr].y), hi2f(skw[pr].w)};
        f2 kf[7], o[4];
#pragma unroll
        for (int i = 0; i < 7; ++i) kf[i] = up2(kw[i][pr]);
#pragma unroll
        for (int i = 0; i < 4; ++i) o[i] = (kf[i + 3] * w0 + kf[i + 2] * w1 + kf[i + 1] * w2 + kf[i] * w3) * up2(vw[i][pr]);
        pe[2 * pr][0] = pk2(o[3].x, o[2].x);
        pe[2 * pr][1] = pk2(o[1].x, o[0].x);
        pe[2 * pr + 1][0] = pk2(o[3].y, o[2].y);
        pe[2 * pr + 1][1] = pk2(o[1].y, o[0].y);
      }
      const unsigned so = (pC0 + 4 * (lane >> 5)) * KVP + (pT0 & ~7) * 2;
#pragma unroll
      for (int e = 0; e < 4; ++e) {
        const auto s0 = __builtin_amdgcn_permlane32_swap(pe[e][0], pe[e + 4][0], false, false);
        const auto s1 = __builtin_amdgcn_permlane32_swap(pe[e][1], pe[e + 4][1], false, false);
        hst128(v4u{s0[1], s1[1], s0[0], s1[0]}, ws, so, O_KVT + e * KVP);
      }
    }
    STAMP(1);
    PIN();
    LAS char* T = L + w * WR;
    if (lane < 8) *(LAS v4u*)(T + lane * CS) = v4u{0u, 0u, 0u, 0u};
#pragma unroll
    for (int i = 0; i < 4; ++i) *(LAS v4u*)(T + 7 * CS + (i * 64 + lane + 1) * 16) = kr[i];
    asm volatile("s_waitcnt lgkmcnt(0)" ::: "memory");
    v4u kl[4];
#pragma unroll
    for (int i = 0; i < 4; ++i) kl[i] = *(LAS v4u*)(T + 7 * CS + (i * 64 + lane) * 16);
    auto tbuild = [&](int i) {
      const unsigned d[8] = {kl[i].x, kl[i].y, kl[i].z, kl[i].w, kr[i].x, kr[i].y, kr[i].z, kr[i].w};
      unsigned a[7];
#pragma unroll
      for (int e = 0; e < 7; ++e) a[e] = __builtin_amdgcn_alignbit(d[e + 1], d[e], 16);
#pragma unroll
      for (int m = 0; m < 7; ++m) {
        const int h = (m + 1) >> 1;
        const v4u o = (m & 1) ? v4u{d[h], d[h + 1], d[h + 2], d[h + 3]} : v4u{a[h], a[h + 1], a[h + 2], a[h + 3]};
        *(LAS v4u*)(T + m * CS + (i * 64 + lane + 1) * 16) = o;
      }
    };
    tbuild(0);
    make_tag(ep);
    DRAIN();
    asm volatile("s_barrier" ::: "memory");
    if (w == 0) {
      unsigned o = OOB;
      if (j < 16 && lane == 0) o = O_FP + x * 256 + ci * 8;
      if (j >= 16 && lane < 4) o = O_FP + ((x & 4) + lane) * 256 + 128 + ci * 8;
      hst64(tag, ws, o, 0);
    }
    STAMP(2);
    tbuild(1);
    tbuild(2);
    tbuild(3);
    if (lane < 32) *(LAS v4u*)(T + UOFF + lane * 16) = v4u{0u, 0u, 0u, 0u};
    STAMP(3);
    if (w == 0) poll64(ws, lane < 16 ? O_FP + x * 256 + (j >= 16) * 128 + lane * 8 : OOB, tag);
    WGBAR();
    STAMP(4);
    const v4u wf = hld128(ws, O_FW + x * 1024 + lane * 16, 0);
    v4u ur[4];
#pragma unroll
    for (int i = 0; i < 4; ++i) ur[i] = hld128(ws, lane * 16 + i * 1024, O_KVT + ch * KVP);
    const LAS char* pa = T + (l15 & 7) * CS + (3 - g + (l15 >> 3)) * 16;
    const LAS char* pv = T + UOFF + 512 + (l15 - 1) * 32 + g * 16;
    v4u fa[2][8], fv[2][8];
#pragma unroll
    for (int a = 0; a < 8; ++a) fa[0][a] = *(const LAS v4u*)(pa + 8 * a * 64);
    f4 acc[8];
    v4u ad;
    {
      const int jd = 8 * g - 9 - l15;
#pragma unroll
      for (int e = 0; e < 4; ++e) ad[e] = jd == 2 * e ? dvb : (jd == 2 * e + 1 ? dvb << 16 : 0u);
      asm volatile("" : "+v"(ad));
    }
    PIN();
#pragma unroll
    for (int i = 0; i < 4; ++i) *(LAS v4u*)(T + UOFF + 512 + i * 1024 + lane * 16) = ur[i];
    asm volatile("s_waitcnt lgkmcnt(0)" ::: "memory");
#pragma unroll
    for (int a = 0; a < 8; ++a) fv[0][a] = *(const LAS v4u*)(pv + a * 512);
    wok = wflags_ok(wf, lane, 40, tag);
    __builtin_amdgcn_fence(__ATOMIC_ACQUIRE, "wavefront");
    const unsigned boff = wok ? lane * 16 : OOB;
    STAMP(5);
#pragma unroll
    for (int q = 0; q < 8; ++q) mfma_v0(acc[q], ad, fv[0][q]);
    PIN();
#pragma unroll
    for (int g0 = 0; g0 < 8; ++g0) {
      int n = 0;
      if (g0 == 0) load_q();
#pragma unroll
      for (int a = 0; a < 8; ++a)
#pragma unroll
        for (int q = a; q < 8; ++q) {
          mfma_v(acc[q], fa[g0 & 1][a], fv[g0 & 1][q - a]);
          ++n;
          if ((n & 1) == 0) {
            const int e = n / 2 - 1;
            if (g0 < 7 && e < 16) {
              if (e < 8) fv[(g0 + 1) & 1][e] = *(const LAS v4u*)(pv + e * 512 - (g0 + 1) * 64);
              else fa[(g0 + 1) & 1][e - 8] = *(const LAS v4u*)(pa + (g0 + 1 + 8 * (e - 8)) * 64);
            }
            PIN();
          }
          if (n % 5 == 0) {
            const int i = g0 * 7 + n / 5 - 13;
            if (i < 0) {
              if (i < -9) zero_h1(i + 12);
            } else if (i < 25) b1[i] = hld128(ws, boff, sb1 + i * 1024);
            else if (i < 50) b2[i - 25] = hld128(ws, boff, sb2 + (i - 25) * 1024);
            PIN();
          }
        }
    }
    const v4u bfl = hld128(ws, O_FB + x * 1024 + lane * 16, 0);
    MFENCE();
    PIN();
    STAMP(6);
#pragma unroll
    for (int q = 0; q < 8; q += 2) {
      const auto s0 = __builtin_amdgcn_permlane16_swap(pk2(acc[q][0], acc[q][1]), pk2(acc[q + 1][0], acc[q + 1][1]), false, false);
      const auto s1 = __builtin_amdgcn_permlane16_swap(pk2(acc[q][2], acc[q][3]), pk2(acc[q + 1][2], acc[q + 1][3]), false, false);
      *(LAS v4u*)(T + 64 * w + (32 * (q + (g & 1)) + 2 * l15 + (g >> 1)) * 16) = v4u{s0[0], s1[0], s0[1], s1[1]};
    }
    WGBAR();
    {
      v4u zo[4];
#pragma unroll
      for (int i = 0; i < 4; ++i) zo[i] = *(LAS v4u*)(L + (lane & 3) * (WR + 64) + (64 * w + 16 * i + (lane >> 2)) * 16);
#pragma unroll
      for (int i = 0; i < 4; ++i) hst128(zo[i], ws, (lane >> 2) * ZP + (lane & 3) * 16, O_ZT + (64 * w + 16 * i) * ZP + (ch - w) * 16);
    }
    YOUNG();
    bok = wflags_ok(bfl, lane, 48, tag);
    __builtin_amdgcn_fence(__ATOMIC_ACQUIRE, "wavefront");
    load_mod(bok);
    PIN();
    ln1();
    PIN();
    WAITV(10);
  } else {
    const int hb = (j - 20) * 8 + x;
    unsigned vn[5];
#pragma unroll
    for (int i = 0; i < 5; ++i) vn[i] = (i * 64 + lane) * 16 + (((i * 64 + lane) * 1639u) >> 16) * 640;
    Job<false> j0, j1;
    j0.load(hb, w, vn, gw1, gw2, gw4);
    const v2u ep = hld64(ws, O_EPOCH, 0);
    const v2u cw = __builtin_amdgcn_raw_buffer_load_b64(mkrs(gc, D_ * 2), tid * 8, 0, 0);
    PIN();
    unsigned vw[7];
#pragma unroll
    for (int i = 0; i < 7; ++i) {
      const unsigned c = i * 64 + lane, row = (c * 1171u) >> 16;
      vw[i] = c * 16 + row * 2560 + (c - row * 56 >= 48);
      KEEP(vw[i]);
    }
    offsets();
    LAS float* sc = (LAS float*)(L + 4 * RT_BYTES);
    {
      const float c4[4] = {lo2f(cw.x), hi2f(cw.x), lo2f(cw.y), hi2f(cw.y)};
      f4 sv;
#pragma unroll
      for (int e = 0; e < 4; ++e) sv[e] = rbf(c4[e] * __builtin_amdgcn_rcpf(1.f + __builtin_amdgcn_exp2f(-1.4426950408889634f * c4[e])));
      *(LAS f4*)(sc + tid * 4) = sv;
    }
    j0.put(w, lane, L);
    make_tag(ep);
    WGBAR();
    j0.emit(w, lane, L, ws);
    if (j == 20 && w == 0) hst128(v4u{tag.x, tag.y, 0u, 0u}, ws, (lane >> 3) * 1024 + (lane & 7) * 16, O_CNT + x * 128);
    asm volatile("" ::"v"(pf) : "memory");
    STAMP(1);
    auto first_pub = [&]() {
      WGBAR();
      if (w == 0) hst64(tag, ws, lane < 8 ? O_FW + lane * 1024 + hb * 8 : OOB, 0);
      STAMP(2);
    };
    if (hb < 60) {
      const rsrc_t rm = mkrs(gwm, D_ * 6 * D_ * 2);
      v4u wm[20];
      YOUNG();
#pragma unroll
      for (int i = 0; i < 8; ++i) wm[i] = ld128(rm, (tid >> 3) * (6 * D_ * 2) + (tid & 7) * 16, hb * 128 + i * 32 * (6 * D_ * 2));
      PIN();
      WAITV(8);
      first_pub();
#pragma unroll
      for (int i = 8; i < 20; ++i) wm[i] = ld128(rm, (tid >> 3) * (6 * D_ * 2) + (tid & 7) * 16, hb * 128 + i * 32 * (6 * D_ * 2));
      j1.load(132 + hb, w, vn, gw1, gw2, gw4);
      load_q();
      float sl[20];
#pragma unroll
      for (int i = 0; i < 20; ++i) sl[i] = sc[(tid >> 3) + 32 * i];
      PIN();
      f2 a[4] = {f2{0.f, 0.f}, f2{0.f, 0.f}, f2{0.f, 0.f}, f2{0.f, 0.f}};
#pragma unroll
      for (int i = 0; i < 20; ++i)
#pragma unroll
        for (int e = 0; e < 4; ++e) a[e] += up2(wm[i][e]) * sl[i];
      LAS char* mred = L + 4 * RT_BYTES + 4096;
      *(LAS f4*)(mred + (tid >> 3) * 272 + (tid & 7) * 32) = f4{a[0].x, a[0].y, a[1].x, a[1].y};
      *(LAS f4*)(mred + (tid >> 3) * 272 + (tid & 7) * 32 + 16) = f4{a[2].x, a[2].y, a[3].x, a[3].y};
      WGBAR();
      {
        const int col = tid >> 2, part = tid & 3;
        float v[8];
#pragma unroll
        for (int r = 0; r < 8; ++r) v[r] = *(LAS float*)(mred + (part * 8 + r) * 272 + col * 4);
        float s = ((v[0] + v[1]) + (v[2] + v[3])) + ((v[4] + v[5]) + (v[6] + v[7]));
        s += dpp<0xB1>(s);
        s += dpp<0x4E>(s);
        s = rbf(s);
        if ((hb >= 10 && hb < 20) || (hb >= 40 && hb < 50)) s = rbf(1.f + s);
        if (part == 0) *((LAS unsigned short*)sc + 8 + col) = (unsigned short)(__builtin_bit_cast(unsigned, s) >> 16);
      }
      j1.put(w, lane, L);
      WGBAR();
      if (w == 0) {
        hst128(*(LAS v4u*)((LAS char*)sc + 16 + (lane & 7) * 16), ws, lane < 8 ? lane * 16 : OOB, O_MOD + hb * 128);
        DRAIN();
        hst64(tag, ws, lane < 8 ? O_FB + lane * 1024 + hb * 8 : OOB, 0);
      }
      STAMP(3);
    } else {
      Job<true> ja, jb, jc;
      YOUNG();
      ja.load(hb - 60, w, vw, gw1, gw2, gw3);
      PIN();
      WAITV(7);
      first_pub();
      jb.load(hb - 24, w, vw, gw1, gw2, gw3);
      if (hb < 68) jc.load(hb + 12, w, vw, gw1, gw2, gw3);
      j1.load(36 + hb, w, vn, gw1, gw2, gw4);
      load_q();
      PIN();
      ja.put(w, lane, L + RT_BYTES);
      jb.put(w, lane, L + 2 * RT_BYTES);
      if (hb < 68) jc.put(w, lane, L + 3 * RT_BYTES);
      WGBAR();
      ja.emit(w, lane, L + RT_BYTES, ws);
      jb.emit(w, lane, L + 2 * RT_BYTES, ws);
      if (hb < 68) jc.emit(w, lane, L + 3 * RT_BYTES, ws);
      j1.put(w, lane, L);
      DRAIN();
      WGBAR();
      if (w == 0) hst64(tag, ws, lane < 8 ? O_FB + lane * 1024 + hb * 8 : OOB, 0);
      STAMP(3);
    }
    pollw(ws, O_FW + x * 1024 + lane * 16, lane, 40, tag);
    j1.emit(w, lane, L, ws);
    zero_h1(0);
    zero_h1(1);
    zero_h1(2);
    if (hb >= 34 && hb < 60) hst128(v4u{0u, 0u, 0u, 0u}, ws, w == 0 ? lane * 16 : OOB, O_FR + (hb - 34) * 1024);
    STAMP(4);
    YOUNG();
#pragma unroll
    for (int i = 0; i < 10; ++i) b1[i] = hld128(ws, lane * 16, sb1 + i * 1024);
    PIN();
    ln1();
    PIN();
    WAITV(10);
  }
  if (conv_wg && !wok) pollw(ws, O_FW + x * 1024 + lane * 16, lane, 40, tag);
  WGBAR();
  STAMP(7);
  if (w == 0 && lane < 8)
    __hip_atomic_fetch_add((GAS unsigned*)((GAS char*)gws + O_CNT + (lane * 8 + x) * 128), 1u, __ATOMIC_RELAXED, __HIP_MEMORY_SCOPE_AGENT);
  if (!wok) {
    if (conv_wg) {
#pragma unroll
      for (int i = 0; i < 10; ++i) b1[i] = hld128(ws, lane * 16, sb1 + i * 1024);
    }
#pragma unroll
    for (int i = 10; i < 25; ++i) b1[i] = hld128(ws, lane * 16, sb1 + i * 1024);
#pragma unroll
    for (int i = 0; i < 19; ++i) b2[i] = hld128(ws, lane * 16, sb2 + i * 1024);
  }
#pragma unroll
  for (int i = 19; i < 25; ++i) b2[i] = hld128(ws, lane * 16, sb2 + i * 1024);
  v4u b3[35], b4[70];
  const unsigned sb3 = O_W3 + (cg * 140 + w * 35) * 1024, sb4 = O_W4 + (tn * 280 + w * 70) * 1024;
#define LB3(lo, hi) _Pragma("unroll") for (int i_ = lo; i_ < hi; ++i_) b3[i_] = hld128(ws, lane * 16 + (i_ & 3) * 1024, sb3 + (i_ >> 2) * 4096)
  auto ks4 = [](int s) { return s < 8 ? 7 * (s >> 2) + (s & 1) + 4 * ((s >> 1) & 1) : 7 * ((s - 8) / 3) + ((s - 8) % 3 == 2 ? 6 : 2 + (s - 8) % 3); };
  auto lb4 = [&](int i) {
    const int f = ks4(i / 5) * 5 + i % 5;
    b4[i] = hld128(ws, lane * 16 + (f & 3) * 1024, sb4 + (f >> 2) * 4096);
  };
#define LB4(lo, hi) _Pragma("unroll") for (int i_ = lo; i_ < hi; ++i_) lb4(i_)
  if (!bok) {
    pollw(ws, O_FB + x * 1024 + lane * 16, lane, 48, tag);
    load_mod(true);
  }
  constexpr int NB3C = 10;
  f2 gh[10];
  float cb1[10], sbv[10];
  auto md = [&](int v, int i) { const unsigned u = i < 8 ? mm[v][(i >> 1) & 3] : mo[v]; return (i & 1) ? hi2f(u) : lo2f(u); };
  auto use_mod = [&]() {
    LB3(0, NB3C);
#pragma unroll
    for (int i = 0; i < 10; ++i) {
      gh[i] = (n1[i] * md(1, i) + md(0, i)) * md(2, i);
      cb1[i] = md(4, i);
      sbv[i] = md(3, i);
      asm volatile("" : "+v"(gh[i]), "+v"(cb1[i]), "+v"(sbv[i]));
    }
  };
  use_mod();
  PIN();
  if (w == 0) {
    poll64(ws, lane < 8 ? O_CNT + (x * 8 + lane) * 128 : OOB, v2u{tag.x + 32, tag.y});
    if (b == 0) hst64(tag, ws, lane == 0 ? O_EPOCH : OOB, 0);
  }
  WGBAR();
  STAMP(8);

  v4u hx[6];
  f4 csp[7];
  auto img3 = [&](int lo, int hi, int hc) {
    const v4u ones = {0x3f803f80u, 0x3f803f80u, 0x3f803f80u, 0x3f803f80u};
    unsigned la = w * 25600 + lane * 16, lb = (75 + w * 10) * 1024 + lane * 16;
    asm volatile("" : "+v"(la), "+v"(lb));
#pragma unroll
    for (int i = lo; i < 35; ++i) {
      if (i < hi) *(LAS v4u*)(L + (i < 25 ? la : lb) + i * 1024) = b3[i];
      if (i < hc) csp[i % 7] = mfma(b3[i], ones, i < 7 ? f4{0.f, 0.f, 0.f, 0.f} : csp[i % 7]);
      PIN();
    }
  };
  {
    v4u zc[3];
#pragma unroll
    for (int i = 0; i < 3; ++i) zc[i] = hld128(ws, i < 2 ? tid * 16 + i * 4096 : z3off, O_ZT + (8 * tm + tn) * ZP);
    PIN();
#pragma unroll
    for (int i = 0; i < 3; ++i)
#pragma unroll
      for (int e = 0; e < 4; ++e) *(LAS unsigned*)(L + RED_OFF + e * 3072 + (tid + 256 * i) * 4) = zc[i][e];
    STAMP(9);
    WGBAR();
    STAMP(10);
    const v4u z0 = *(LAS v4u*)(L + RED_OFF + w * 3072 + rcm * 4), z1 = *(LAS v4u*)(L + RED_OFF + w * 3072 + rcm * 4 + 16);
    const v2u z2 = *(LAS v2u*)(L + RED_OFF + w * 3072 + rco * 4);
    LB3(10, 19);
    f2 xv[10], hv[10], av[10], s, ss, r, mr;
#pragma unroll
    for (int i = 0; i < 10; ++i) {
      xv[i] = gh[i] * up2(i < 4 ? z0[i & 3] : (i < 8 ? z1[i & 3] : z2[i & 1])) + qv[i];
      s = i ? s + xv[i] : xv[i];
      ss = i ? ss + xv[i] * xv[i] : xv[i] * xv[i];
    }
    wave_sum4(s, ss);
    ln_scal(s, ss, r, mr);
#pragma unroll
    for (int i = 0; i < 10; ++i) {
      hv[i] = (xv[i] * r + mr) * cb1[i] + sbv[i];
      s = i ? s + hv[i] : hv[i];
      ss = i ? ss + hv[i] * hv[i] : hv[i] * hv[i];
    }
    wave_sum4(s, ss);
    ln_scal(s, ss, r, mr);
#pragma unroll
    for (int i = 0; i < 10; ++i) av[i] = hv[i] * r + mr;
    auto put = [&](const f2(&v)[10], int img) {
      *(LAS v4u*)(L + im0 + img * 10240) = v4u{pk2(v[0].x, v[1].x), pk2(v[2].x, v[3].x), pk2(v[4].x, v[5].x), pk2(v[6].x, v[7].x)};
      *(LAS v4u*)(L + (im0 ^ 16) + img * 10240) = v4u{pk2(v[0].y, v[1].y), pk2(v[2].y, v[3].y), pk2(v[4].y, v[5].y), pk2(v[6].y, v[7].y)};
      *(LAS unsigned*)(L + io0 + img * 10240) = pk2(v[8].x, v[9].x);
      *(LAS unsigned*)(L + (io0 ^ 16) + img * 10240) = pk2(v[8].y, v[9].y);
    };
    put(av, 0);
    STAMP(11);
    WGBAR();
    {
      v4u oc[3];
#pragma unroll
      for (int i = 0; i < 3; ++i) oc[i] = *(LAS v4u*)(L + RED_OFF + 16384 + i * 4096 + tid * 16);
#pragma unroll
      for (int i = 0; i < 3; ++i) hst128(oc[i], ws, i < 2 ? roff : roff3, i * 8192);
    }
    PIN();
    put(hv, 1);
    put(xv, 2);
    STAMP(12);
    DRAIN();
    WGBAR();
    STAMP(13);
    if (w == 0) hst64(tag, ws, frpub, 0);
    LB3(19, 28);
    PIN();
#pragma unroll
    for (int i = 0; i < 6; ++i) hx[i] = *(LAS v4u*)(L + RED_OFF + 26624 + (i / 3) * 10240 + (i % 3) * 4096 + tid * 16);
    img3(0, 16, 16);
    if (w == 0) poll64(ws, frwait, tag);
    WGBAR();
    STAMP(14);
  }

  {
    f4 acc[5];
    const unsigned vo[4] = {(unsigned)lane * 16, (unsigned)lane * 16, (unsigned)lane * 16, (unsigned)lane * 16};
    gemm_ks(ws, O_A1 + (4 * tm * 20 + 5 * w) * 1024, vo, b1, L, w, lane, acc,
            [&](int n) { if (n < 7) { LB3(28 + n, 29 + n); } else lb4(16 + n); },
            [&](int n, int t) {
              if (t == 3 && n >= 8 && n < 14) hst128(hx[n - 8], ws, ((n - 8) % 3) < 2 ? roff : roff3, (1 + (n - 8) / 3) * SD2 + ((n - 8) % 3) * 8192);
            } STPASS(15));
    STAMP(18);
    f2 v[10];
    unsigned o[10];
#pragma unroll
    for (int t = 0; t < 5; ++t) {
      v[2 * t] = f2{acc[t][0], acc[t][1]};
      v[2 * t + 1] = f2{acc[t][2], acc[t][3]};
    }
    gelu_pack<10>(v, 1.f, 1.f, o, [](int) {});
    const auto s0 = __builtin_amdgcn_permlane16_swap(o[8], o[8], false, false);
    const auto s1 = __builtin_amdgcn_permlane16_swap(o[9], o[9], false, false);
    hst128(v4u{o[0], o[1], o[2], o[3]}, ws, h1m, 0);
    hst128(v4u{o[4], o[5], o[6], o[7]}, ws, h1m + 1024, 0);
    hst128(v4u{s0[0], s1[0], s0[1], s1[1]}, ws, h1s, 0);
    STAMP(19);
    DRAIN();
    WGBAR();
    STAMP(20);
    if (w == 0) hst64(tag, ws, f1pub, 0);
    LB4(0, 9);
    PIN();
    img3(16, 24, 35);
    if (l15 == 0) {
#pragma unroll
      for (int t = 0; t < 7; ++t) *(LAS f4*)(L + (w * 25 + 24) * 1024 + (t * 4 + g) * 16) = csp[t];
    }
    if (w == 0) poll64(ws, f1wait, tag);
    WGBAR();
    STAMP(21);
  }
  v4u xr[2];
  v2u xro;
  unsigned x1p[10];
  {
    v4u hr[2];
    v2u hro;
    f4 acc[5];
    f2 hf[10];
    const unsigned vo[4] = {(unsigned)lane * 16, (unsigned)lane * 16, (unsigned)lane * 16, (unsigned)lane * 16};
    gemm_ks(ws, O_H1 + (4 * tm * 20 + 5 * w) * 1024, vo, b2, L, w, lane, acc, [&](int n) {
      lb4(n < 5 ? 9 + n : 28 + n);
    }, [&](int n, int t) {
      if (t == 3 && n == 2) hr[0] = hld128(ws, lane * 16, O_HP + own_m);
      if (t == 3 && n == 4) hr[1] = hld128(ws, lane * 16 + 1024, O_HP + own_m);
      if (t == 3 && n == 6) hro = hld64(ws, own_r, O_HP + own_s);
      if (n == 18) {
        unsigned h0 = t < 4 ? hr[t >> 1][2 * (t & 1)] : hro.x;
        KEEP(h0);
        hf[2 * t] = up2(h0);
        KEEP(hf[2 * t]);
      }
    } STPASS(22));
#pragma unroll
    for (int t = 0; t < 5; ++t) {
      const unsigned h1 = t < 4 ? hr[t >> 1][2 * (t & 1) + 1] : hro.y;
      const f2 p = f2{acc[t][0], acc[t][1]} + hf[2 * t], q = f2{acc[t][2], acc[t][3]} + up2(h1);
      x1p[2 * t] = pk2(p.x, p.y);
      x1p[2 * t + 1] = pk2(q.x, q.y);
    }
    const auto s0 = __builtin_amdgcn_permlane16_swap(x1p[8], x1p[8], false, false);
    const auto s1 = __builtin_amdgcn_permlane16_swap(x1p[9], x1p[9], false, false);
    hst128(v4u{x1p[0], x1p[1], x1p[2], x1p[3]}, ws, x1m, 0);
    hst128(v4u{x1p[4], x1p[5], x1p[6], x1p[7]}, ws, x1m + 1024, 0);
    hst128(v4u{s0[0], s1[0], s0[1], s1[1]}, ws, x1s, 0);
    float t1 = 0.f, t2 = 0.f;
#pragma unroll
    for (int i = 0; i < 10; ++i) {
      t1 = __builtin_amdgcn_fdot2_f32_bf16(__builtin_bit_cast(bf2, x1p[i]), __builtin_bit_cast(bf2, 0x3f803f80u), t1, false);
      t2 = __builtin_amdgcn_fdot2_f32_bf16(__builtin_bit_cast(bf2, x1p[i]), __builtin_bit_cast(bf2, x1p[i]), t2, false);
    }
    t1 = swap32_add(t1, t2);
    t1 = swap16_add(t1, t1);
    hst32(__builtin_bit_cast(unsigned, t1), ws, stoff, 0);
    STAMP(25);
    f4 cst;
    {
      f4 cp[4];
#pragma unroll
      for (int v = 0; v < 4; ++v) cp[v] = *(LAS f4*)(L + (v * 25 + 24) * 1024 + lane * 16);
      cst = (cp[0] + cp[1]) + (cp[2] + cp[3]);
      KEEP(cst);
    }
    PIN();
    DRAIN();
    WGBAR();
    STAMP(26);
    if (w == 0) hst64(tag, ws, f2pub, 0);
    LB4(14, 23);
    PIN();
    img3(24, 35, 0);
    if (w == 1) *(LAS f4*)(L + CSUM_OFF + lane * 16) = cst;
    if (w == 0) poll64(ws, f2wait, tag);
    WGBAR();
    STAMP(27);
  }
#define PUBK 10
#define PUBN 9
#define SMPN 20
#define CHKN 26
#define WIN4_ 8
  constexpr int WIN4 = WIN4_, RING4 = WIN4 + 1;
  v4u a4[RING4];
  auto lda4 = [&](int n) {
    const int i = n < 32 ? n >> 3 : (n - 32) / 6, k = ks4(n < 32 ? n & 7 : 8 + (n - 32) % 6);
    a4[n % RING4] = hld128(ws, lane * 16 + (k & 3) * 1024, O_H + ((4 * tm + ((w + 1 + i) & 3)) * 56 + 14 * w + (k & ~3)) * 1024);
  };
  {
#define W3_ 6
    constexpr int W3 = W3_;
    v4u a[20][2];
    const unsigned sa = O_X1 + (8 * rg + 2 * w) * 20 * 1024;
    auto lda = [&](int k, int i) { a[k][i] = hld128(ws, lane * 16 + (k & 3) * 1024, sa + i * 20480 + (k >> 2) * 4096); };
#pragma unroll
    for (int k = 0; k < W3; ++k)
#pragma unroll
      for (int i = 0; i < 2; ++i) {
        lda(k, i);
        PIN();
      }
    f4 acc[2][7];
    v2u sv[2][2];
    f2 r, den, mean;
    auto bslot = [](int k, int t) { const int v = k / 5, i = (k % 5) * 7 + t; return (i < 25 ? v * 25 + i : 75 + v * 10 + i) * 1024; };
    const LAS char* Lb = L + lane * 16;
    const LAS char* Lb2 = L + lane * 16 + 65536;
    const LAS char* Lb3 = L + lane * 16 + 131072;
    auto rdb = [&](int k, int t) { const int o = bslot(k, t); return *(const LAS v4u*)(o < 65536 ? Lb + o : (o < 131072 ? Lb2 + (o - 65536) : Lb3 + (o - 131072))); };
    v4u bf[3][4];
#pragma unroll
    for (int t = 0; t < 8; ++t) {
      bf[t >> 2][t & 3] = rdb(t >> 2, t & 3);
      PIN();
    }
    sfor<0, 20>([&](auto ic) {
      constexpr int k = decltype(ic)::v;
      __builtin_amdgcn_s_waitcnt(k < 19 ? 0xC47F : 0xC37F);
#pragma unroll
      for (int t = 0; t < 4; ++t) {
        if (k == 0) mfma_va0(acc[0][t], bf[0][t], a[0][0]);
        else mfma_va(acc[0][t], bf[k % 3][t], a[k][0]);
        if (k < 18) bf[(k + 2) % 3][t] = rdb(k + 2, t);
        else if (t < 3) bf[(k + 2) % 3][t] = rdb(k - 18, 4 + t);
        PIN();
      }
#pragma unroll
      for (int t = 0; t < 4; ++t) {
        if (k == 0) mfma_va0(acc[1][t], bf[0][t], a[0][1]);
        else mfma_va(acc[1][t], bf[k % 3][t], a[k][1]);
        if (t < 2) {
          if constexpr (k + W3 < 20) lda(k + W3, t);
        }
        if (t >= (k + W3 < 20 ? 2 : 0) && t < (k + W3 < 20 ? 4 : 2)) {
          if constexpr (k == 14 || k == 15) sv[k - 14][t & 1] = hld64(ws, l15 * 8 + g * 256 + (t & 1) * 128, O_STAT + (8 * rg + 2 * w + k - 14) * 1024);
          else if constexpr (k >= 16) lb4(40 + 2 * (k - 16) + (t & 1));
        }
        PIN();
      }
      if (k == 0) STAMP(28);
    });
    STAMP(29);
    const unsigned hs = O_H + ((8 * rg + 2 * w) * 56 + 7 * (cg >> 1) + 4 * (cg & 1)) * 1024 + lane * 16;
    unsigned lcs = CSUM_OFF + g * 16;
    KEEP(lcs);
#pragma unroll
    for (int t = 0; t < 4; ++t) csp[t] = *(LAS f4*)(L + lcs + t * 64);
    MFENCE();
    PIN();
    {
      asm volatile("" : "+v"(sv[0][0]), "+v"(sv[0][1]), "+v"(sv[1][0]), "+v"(sv[1][1]));
      const f2 p0 = __builtin_bit_cast(f2, sv[0][0]) + __builtin_bit_cast(f2, sv[0][1]);
      const f2 p1 = __builtin_bit_cast(f2, sv[1][0]) + __builtin_bit_cast(f2, sv[1][1]);
      f2 mr;
      ln_scal(f2{xrow_sum(p0.x), xrow_sum(p1.x)}, f2{xrow_sum(p0.y), xrow_sum(p1.y)}, r, mr, &mean, &den);
    }
#pragma unroll
    for (int i = 0; i < 2; ++i) {
      f2 v[8];
      unsigned o[8];
#pragma unroll
      for (int e = 0; e < 8; ++e) {
        const int t = e >> 1, h = 2 * (e & 1);
        v[e] = f2{acc[i][t][h], acc[i][t][h + 1]} - f2{csp[t][h], csp[t][h + 1]} * mean[i];
      }
      gelu_pack<8>(v, r[i], den[i], o, [&](int m) {
        if (i == 0) lb4(28 + m);
        else if (m == 0) lb4(32);
      });
      hst128(v4u{o[0], o[1], o[2], o[3]}, ws, hs, i * 57344);
      hst128(v4u{o[4], o[5], o[6], o[7]}, ws, hs, i * 57344 + 1024);
    }
    YOUNG();
    STAMP(30);
    sfor<0, 20>([&](auto ic) {
      constexpr int k = decltype(ic)::v;
      __builtin_amdgcn_s_waitcnt(k < 19 ? 0xC37F : 0xC07F);
      if (k == PUBK) {
        WAITV(10);
        hst64(tag, ws, f3pub, 0);
      }
#pragma unroll
      for (int e = 0; e < 6; ++e) {
        const int i = e / 3, t = e % 3;
        if (k == 0) mfma_va0(acc[i][4 + t], bf[2][t], a[0][i]);
        else mfma_va(acc[i][4 + t], bf[(k + 2) % 3][t], a[k][i]);
        if (i == 0 && k < 18) bf[(k + 1) % 3][t] = rdb(k + 2, 4 + t);
        if (k >= 1 && k < 5 && e == 5) lb4(47 + k);
        if (k >= 1 && k < 19 && e == 3) lb4(51 + k);
        PIN();
      }
    });
#define SMPM 0
    v2u fl;
    if (SMPM < 0) fl = hld64(ws, f3wait, 0);
#pragma unroll
    for (int t = 4; t < 7; ++t) csp[t] = *(LAS f4*)(L + lcs + t * 64);
    MFENCE();
    PIN();
    STAMP(37);
    unsigned os[2][2];
#pragma unroll
    for (int i = 0; i < 2; ++i) {
      f2 v[6];
      unsigned o[6];
#pragma unroll
      for (int e = 0; e < 6; ++e) {
        const int t = 4 + (e >> 1), h = 2 * (e & 1);
        v[e] = f2{acc[i][t][h], acc[i][t][h + 1]} - f2{csp[t][h], csp[t][h + 1]} * mean[i];
      }
      gelu_pack<6>(v, r[i], den[i], o, [&](int m) {
        if (i == 0 && m == SMPM) fl = hld64(ws, f3wait, 0);
      });
      hst128(v4u{o[0], o[1], o[2], o[3]}, ws, hs, i * 57344 + 2048);
      os[i][0] = o[4];
      os[i][1] = o[5];
      if (i == 0) {
        PIN();
        KEEP(fl);
        if (!__all(f3wait >= OOB || (fl.x == tag.x && fl.y == tag.y))) poll64(ws, f3wait, tag);
        __builtin_amdgcn_fence(__ATOMIC_ACQUIRE, "wavefront");
        STAMP(32);
#pragma unroll
        for (int n = 0; n < WIN4; ++n) {
          lda4(n);
          PIN();
        }
      }
    }
    {
      const auto s0 = __builtin_amdgcn_permlane16_swap(os[0][0], os[1][0], false, false);
      const auto s1 = __builtin_amdgcn_permlane16_swap(os[0][1], os[1][1], false, false);
      hst128(v4u{s0[0], s1[0], s0[1], s1[1]}, ws, (g & 1) * 57344 + (l15 + 16 * (g >> 1)) * 16,
             O_H + ((8 * rg + 2 * w) * 56 + 7 * (cg >> 1) + 3) * 1024 + (cg & 1) * 512);
    }
    YOUNG();
    STAMP(31);
  }
  {
    v4u gb[2];
    v2u gbo, fl;
    f4 ac[4][5], p[15];
    float gf[20], cf[20];
    unsigned lq = lane;
    KEEP(lq);
    const unsigned lr = RED_OFF + lq * 16, gq4 = lq >> 4;
    unsigned dm = ((64 * tm + 16 * w + (lq & 15)) * D_ + 80 * tn) * 2, ds = dm + 8 * gq4 + ((tn & 1) ? 0 : 128);
    dm += 16 * gq4 + 32 * (tn & 1);
    unsigned xro_off = ((lq & 15) + 16 * (gq4 >> 1)) * 16 + 8 * (gq4 & 1);
    sfor<0, 56>([&](auto ic) {
      constexpr int n = decltype(ic)::v, i = n < 32 ? n >> 3 : (n - 32) / 6, sl = n < 32 ? n & 7 : 8 + (n - 32) % 6;
      if (n == 54) {
        STAMP(35);
        WGBAR();
        STAMP(36);
      }
      if (n == PUBN) {
        WAITV(7);
        hst64(tag, ws, f3pub + (O_F4 - O_F3), 0);
        STAMP(33);
      }
      if (n == 23) asm volatile("" : "+v"(dm), "+v"(ds), "+v"(xro_off));
      if (n == 37) asm volatile("s_barrier" ::: "memory");
      if (n == CHKN) {
        KEEP(fl);
        if (!__all(f3wait >= OOB || (fl.x == tag.x && fl.y == tag.y))) poll64(ws, f3wait + (O_F4 - O_F3), tag);
        __builtin_amdgcn_fence(__ATOMIC_ACQUIRE, "wavefront");
#pragma unroll
        for (int m = 32; m < CHKN + WIN4; ++m) lda4(m);
        PIN();
      }
#pragma unroll
      for (int t = 0; t < 5; ++t) {
        const int f = sl * 5 + t;
        if (sl == 0) mfma_aa0(ac[i][t], b4[f], a4[n % RING4]);
        else if (f >= 23 && f < 48) mfma_va(ac[i][t], b4[f], a4[n % RING4]);
        else mfma_aa(ac[i][t], b4[f], a4[n % RING4]);
        if (t == 1) {
          if constexpr (n + WIN4 < 56) {
            if (n < 32 - WIN4 || n >= CHKN) lda4(n + WIN4);
          }
        }
        if (t == 3) {
          if (n == 30 || n == 31) gb[n & 1] = hld128(ws, gq4 * 16 + (n & 1) * 64, O_MOD + (5 * D_ + 80 * tn + 16 * (tn & 1)) * 2);
          if (n == 32 || n == 33) xr[n & 1] = hld128(ws, lq * 16 + (n & 1) * 1024, O_X + own_m);
          if (n == 34) gbo = hld64(ws, gq4 * 8, O_MOD + (5 * D_ + 80 * tn + ((tn & 1) ? 0 : 64)) * 2);
          if (n == 35) xro = hld64(ws, xro_off, O_X + own_s);
        }
        if constexpr (n >= 44 && n < 54) {
          constexpr int q = n - 44;
          const unsigned gw = q < 8 ? gb[(q >> 2) & 1][q & 3] : gbo[q & 1], xw = q < 8 ? xr[(q >> 2) & 1][q & 3] : xro[q & 1];
          float tmp;
          if (t == 1) asm volatile("v_lshlrev_b32 %0, 16, %2\n\tv_and_b32 %1, 0xffff0000, %2" : "=&v"(gf[2 * q]), "=v"(gf[2 * q + 1]) : "v"(gw));
          if (t == 2) asm volatile("v_lshlrev_b32 %0, 16, %2\n\tv_and_b32 %1, 0xffff0000, %2" : "=&v"(cf[2 * q]), "=v"(cf[2 * q + 1]) : "v"(xw));
          if (t == 3) asm volatile("v_lshlrev_b32 %1, 16, %2\n\tv_fmac_f32 %0, %1, %3" : "+v"(cf[2 * q]), "=&v"(tmp) : "v"(x1p[q]), "v"(gf[2 * q]));
          if (t == 4) asm volatile("v_and_b32 %1, 0xffff0000, %2\n\tv_fmac_f32 %0, %1, %3" : "+v"(cf[2 * q + 1]), "=&v"(tmp) : "v"(x1p[q]), "v"(gf[2 * q + 1]));
        }
        if (n == SMPN && t == 0) fl = hld64(ws, f3wait + (O_F4 - O_F3), 0);
        if (n >= 38 && sl < 11 && (t == 0 || t == 2) && (sl - 8) * 2 + (t == 2) > 0) {
          const int u = (sl - 8) * 2 + (t == 2) - 1;
          *(LAS f4*)(L + (lr + ((w + i) & 3) * 15360) + ((i - 1) * 5 + u) * 1024) = ac[i > 0 ? i - 1 : 0][u];
        }
        if (n == 54) {
          p[t] = *(LAS f4*)(L + (lr + w * 15360) + t * 1024);
          p[5 + t] = *(LAS f4*)(L + (lr + w * 15360) + (5 + t) * 1024);
        }
        if (n == 55) p[10 + t] = *(LAS f4*)(L + (lr + w * 15360) + (10 + t) * 1024);
        PIN();
      }
      if (n == 0) STAMP(34);
    });
    MFENCE();
    PIN();
    unsigned o[10];
#pragma unroll
    for (int t = 0; t < 5; ++t) {
      const f4 y = (ac[3][t] + p[t]) + (p[5 + t] + p[10 + t]);
      const f2 u = f2{y[0], y[1]} * f2{gf[4 * t], gf[4 * t + 1]} + f2{cf[4 * t], cf[4 * t + 1]};
      const f2 v = f2{y[2], y[3]} * f2{gf[4 * t + 2], gf[4 * t + 3]} + f2{cf[4 * t + 2], cf[4 * t + 3]};
      o[2 * t] = pk2(u.x, u.y);
      o[2 * t + 1] = pk2(v.x, v.y);
    }
    const rsrc_t ro = mkrs(gout, SD2);
    __builtin_amdgcn_raw_buffer_store_b128(v4u{o[0], o[1], o[2], o[3]}, ro, dm, 0, 0);
    __builtin_amdgcn_raw_buffer_store_b128(v4u{o[4], o[5], o[6], o[7]}, ro, dm + 64, 0, 0);
    __builtin_amdgcn_raw_buffer_store_b64(v2u{o[8], o[9]}, ro, ds, 0, 0);
  }
  asm volatile(".Lfused_end:" ::: "memory");
}

extern "C" void kernel_launch(void* const* d_in, const int* in_sizes, int n_in, void* d_out, int out_size,
                              void* d_ws, size_t ws_size, hipStream_t stream) {
  (void)hipFuncSetAttribute((const void*)fused, hipFuncAttributeMaxDynamicSharedMemorySize, LDS_BYTES);
  fused<<<256, 256, LDS_BYTES, stream>>>(d_in[0], d_in[1], d_in[2], d_in[3], d_in[4], d_in[5], d_in[7], d_in[8], d_in[9],
                                         d_in[10], d_in[11], d_in[12], d_out, d_ws);
}
```

```cpp
#include <hip/hip_runtime.h>
#include <stdint.h>

typedef unsigned v4u __attribute__((ext_vector_type(4)));
typedef unsigned v2u __attribute__((ext_vector_type(2)));
typedef float f4 __attribute__((ext_vector_type(4)));
typedef float f2 __attribute__((ext_vector_type(2)));
typedef short s8v __attribute__((ext_vector_type(8)));
typedef short s4v __attribute__((ext_vector_type(4)));
typedef __bf16 bf2 __attribute__((ext_vector_type(2)));
typedef __bf16 bf8 __attribute__((ext_vector_type(8)));
typedef __amdgpu_buffer_rsrc_t rsrc_t;
#define LAS __attribute__((address_space(3)))
#define GAS __attribute__((address_space(1)))
#define DI __device__ __forceinline__

static constexpr int S_ = 2048, D_ = 640, F_ = 1728;
static constexpr unsigned OOB = 0x80000000u;

static constexpr unsigned O_EPOCH = 0;
static constexpr unsigned O_FP = 1024;
static constexpr unsigned O_FR = O_FP + 16 * 128;
static constexpr unsigned O_F1 = O_FR + 32 * 128;
static constexpr unsigned O_F2 = O_F1 + 32 * 128;
static constexpr unsigned O_F3 = O_F2 + 16 * 128;
static constexpr unsigned O_F4 = O_F3 + 16 * 512;
static constexpr unsigned O_CNT = O_F4 + 16 * 512;
static constexpr unsigned O_FW = O_CNT + 64 * 128;
static constexpr unsigned O_FB = O_FW + 8192;
static constexpr unsigned O_MOD = 65536;
static constexpr unsigned O_STAT = O_MOD + 16384;
static constexpr unsigned O_KVT = O_STAT + 131072;
static constexpr unsigned SD2 = S_ * D_ * 2;
static constexpr unsigned KVP = 4096 + 128, ZP = 10240 + 128;
static constexpr unsigned O_ZT = O_KVT + D_ * KVP;
static constexpr unsigned O_A1 = O_ZT + 256 * ZP;
static constexpr unsigned O_HP = O_A1 + SD2;
static constexpr unsigned O_X = O_HP + SD2;
static constexpr unsigned O_H1 = O_X + SD2;
static constexpr unsigned O_X1 = O_H1 + SD2;
static constexpr unsigned O_H = O_X1 + SD2;
static constexpr unsigned O_W1 = O_H + 128 * 56 * 1024;
static constexpr unsigned O_W2 = O_W1 + 800 * 1024;
static constexpr unsigned O_W3 = O_W2 + 800 * 1024;
static constexpr unsigned O_W4 = O_W3 + 2240 * 1024;
static constexpr unsigned O_END = O_W4 + 2240 * 1024;
static constexpr unsigned O_DBG = O_END;

static constexpr int CS = 4160;
static constexpr int UOFF = 8 * CS;
static constexpr int WR = 8 * CS + 4608;
static constexpr int LDS_BYTES = 160 * 1024;
static constexpr int RED_OFF = 100 * 1024;
static constexpr int CSUM_OFF = 140 * 1024;
static constexpr unsigned SPIN_CAP = 200000;

template <int V> struct IC { static constexpr int v = V; };
template <int I, int N, class F> DI void sfor(F&& f) {
  if constexpr (I < N) {
    f(IC<I>{});
    sfor<I + 1, N>(f);
  }
}
DI float lo2f(unsigned w) { return __builtin_bit_cast(float, w << 16); }
DI float hi2f(unsigned w) { return __builtin_bit_cast(float, w & 0xffff0000u); }
DI f2 up2(unsigned w) { return f2{lo2f(w), hi2f(w)}; }
DI unsigned pk2(float a, float b) { return __builtin_bit_cast(unsigned, __builtin_convertvector(f2{a, b}, bf2)); }
DI f4 mfma(v4u a, v4u b, f4 c) {
  return __builtin_amdgcn_mfma_f32_16x16x32_bf16(__builtin_bit_cast(bf8, a), __builtin_bit_cast(bf8, b), c, 0, 0, 0);
}
DI void mfma_v(f4& c, v4u a, v4u b) { asm volatile("v_mfma_f32_16x16x32_bf16 %0, %1, %2, %0" : "+v"(c) : "v"(a), "v"(b)); }
DI void mfma_v0(f4& c, v4u a, v4u b) { asm volatile("v_mfma_f32_16x16x32_bf16 %0, %1, %2, 0" : "=&v"(c) : "v"(a), "v"(b)); }
DI void mfma_va(f4& c, v4u a, v4u b) { asm volatile("v_mfma_f32_16x16x32_bf16 %0, %1, %2, %0" : "+v"(c) : "v"(a), "a"(b)); }
DI void mfma_va0(f4& c, v4u a, v4u b) { asm volatile("v_mfma_f32_16x16x32_bf16 %0, %1, %2, 0" : "=&v"(c) : "v"(a), "a"(b)); }
DI void mfma_av(f4& c, v4u a, v4u b) { asm volatile("v_mfma_f32_16x16x32_bf16 %0, %1, %2, %0" : "+v"(c) : "a"(a), "v"(b)); }
DI void mfma_av0(f4& c, v4u a, v4u b) { asm volatile("v_mfma_f32_16x16x32_bf16 %0, %1, %2, 0" : "=&v"(c) : "a"(a), "v"(b)); }
DI void mfma_aa(f4& c, v4u a, v4u b) { asm volatile("v_mfma_f32_16x16x32_bf16 %0, %1, %2, %0" : "+v"(c) : "a"(a), "a"(b)); }
DI void mfma_aa0(f4& c, v4u a, v4u b) { asm volatile("v_mfma_f32_16x16x32_bf16 %0, %1, %2, 0" : "=&v"(c) : "a"(a), "a"(b)); }
#define KEEP(x) asm volatile("" : "+v"(x))
#define MFENCE() asm volatile("s_nop 7\n\ts_nop 1" ::: "memory")
DI float add1(float a, float b) { float r; asm("v_add_f32 %0, %1, %2" : "=v"(r) : "v"(a), "v"(b)); return r; }
DI float fma1(float a, float b, float c) { float r; asm("v_fma_f32 %0, %1, %2, %3" : "=v"(r) : "v"(a), "v"(b), "v"(c)); return r; }
DI rsrc_t mkrs(const void* p, unsigned bytes) { return __builtin_amdgcn_make_buffer_rsrc((void*)p, 0, bytes, 0x00020000); }
#define KAUX 0
#define HDELAY 0
#define INAUX 2
DI v4u ld128(rsrc_t r, unsigned v, unsigned s) { return __builtin_amdgcn_raw_buffer_load_b128(r, v, s, INAUX); }
DI v2u ld64(rsrc_t r, unsigned v, unsigned s) { return __builtin_amdgcn_raw_buffer_load_b64(r, v, s, INAUX); }
DI unsigned ld32(rsrc_t r, unsigned v, unsigned s) { return __builtin_amdgcn_raw_buffer_load_b32(r, v, s, INAUX); }
DI v4u hld128(rsrc_t r, unsigned v, unsigned s) { return __builtin_amdgcn_raw_buffer_load_b128(r, v, s, 16); }
DI v2u hld64(rsrc_t r, unsigned v, unsigned s) { return __builtin_amdgcn_raw_buffer_load_b64(r, v, s, 16); }
DI void hst128(v4u x, rsrc_t r, unsigned v, unsigned s) { __builtin_amdgcn_raw_buffer_store_b128(x, r, v + s, 0, 16); }
DI void hst64(v2u x, rsrc_t r, unsigned v, unsigned s) { __builtin_amdgcn_raw_buffer_store_b64(x, r, v, s, 16); }
DI void hst32(unsigned x, rsrc_t r, unsigned v, unsigned s) { __builtin_amdgcn_raw_buffer_store_b32(x, r, v, s, 16); }
#define DRAIN() do { asm volatile("s_waitcnt vmcnt(0)" ::: "memory"); __builtin_amdgcn_s_waitcnt(0x0F70); } while (0)
#define WGBAR() asm volatile("s_waitcnt lgkmcnt(0)\n\ts_barrier" ::: "memory")
#define PIN() __builtin_amdgcn_sched_barrier(0)

template <int CTRL> DI float dpp(float x) {
  return __builtin_bit_cast(float, __builtin_amdgcn_mov_dpp(__builtin_bit_cast(int, x), CTRL, 0xf, 0xf, true));
}
DI float xrow_sum(float x) {
  auto s = __builtin_amdgcn_permlane16_swap(__builtin_bit_cast(unsigned, x), __builtin_bit_cast(unsigned, x), false, false);
  x = __builtin_bit_cast(float, (unsigned)s[0]) + __builtin_bit_cast(float, (unsigned)s[1]);
  auto t = __builtin_amdgcn_permlane32_swap(__builtin_bit_cast(unsigned, x), __builtin_bit_cast(unsigned, x), false, false);
  return __builtin_bit_cast(float, (unsigned)t[0]) + __builtin_bit_cast(float, (unsigned)t[1]);
}
DI float swap32_add(float a, float b) {
  auto t = __builtin_amdgcn_permlane32_swap(__builtin_bit_cast(unsigned, a), __builtin_bit_cast(unsigned, b), false, false);
  return __builtin_bit_cast(float, (unsigned)t[0]) + __builtin_bit_cast(float, (unsigned)t[1]);
}
DI float swap16_add(float a, float b) {
  auto t = __builtin_amdgcn_permlane16_swap(__builtin_bit_cast(unsigned, a), __builtin_bit_cast(unsigned, b), false, false);
  return __builtin_bit_cast(float, (unsigned)t[0]) + __builtin_bit_cast(float, (unsigned)t[1]);
}
DI float rdl(float v, int l) { return __builtin_bit_cast(float, __builtin_amdgcn_readlane(__builtin_bit_cast(int, v), l)); }
DI void wave_sum4(f2& s, f2& ss) {
  float v = swap16_add(swap32_add(s.x, s.y), swap32_add(ss.x, ss.y));
  v += dpp<0xB1>(v);
  v += dpp<0x4E>(v);
  v += dpp<0x141>(v);
  v += dpp<0x128>(v);
  s = f2{rdl(v, 0), rdl(v, 32)};
  ss = f2{rdl(v, 16), rdl(v, 48)};
}
DI void poll64(rsrc_t ws, unsigned voff, v2u want) {
  const bool idle = voff >= OOB;
  for (unsigned spin = 0; spin < SPIN_CAP; ++spin) {
    asm volatile("" ::: "memory");
    const v2u v = hld64(ws, voff, 0);
    if (__all(idle || (v.x == want.x && v.y == want.y))) break;
  }
  __builtin_amdgcn_fence(__ATOMIC_ACQUIRE, "wavefront");
}

DI bool wflags_ok(v4u a, int lane, int nl, v2u want) {
  return __all(lane >= nl || (a.x == want.x && a.y == want.y && a.z == want.x && a.w == want.y));
}
DI void pollw(rsrc_t ws, unsigned voff, int lane, int nl, v2u want) {
  for (unsigned spin = 0; spin < SPIN_CAP; ++spin) {
    asm volatile("" ::: "memory");
    if (wflags_ok(hld128(ws, voff, 0), lane, nl, want)) break;
    __builtin_amdgcn_s_sleep(4);
  }
  __builtin_amdgcn_fence(__ATOMIC_ACQUIRE, "wavefront");
}
DI float rbf(float x) { return lo2f(pk2(x, 0.f)); }
DI void ln_scal(f2 s, f2 ss, f2& r, f2& mr, f2* pmean = nullptr, f2* pden = nullptr) {
  const f2 m = s * (1.f / D_);
  const f2 var = ss * (1.f / D_) - m * m;
  const f2 v = f2{fmaxf(var.x, 0.f), fmaxf(var.y, 0.f)} + 1e-5f;
  r = f2{__builtin_amdgcn_rsqf(v.x), __builtin_amdgcn_rsqf(v.y)};
  mr = -m * r;
  if (pmean) *pmean = m;
  if (pden) *pden = v * r;
}
template <int N, class Mid> DI void gelu_pack(const f2 (&x)[N], float r, float den, unsigned* o, Mid mid) {
  const float c0 = -2.f * 0.7978845608028654f * 1.4426950408889634f * r, c1 = c0 * 0.044715f * r * r;
  f2 a[N];
#pragma unroll
  for (int i = 0; i < N; ++i) a[i] = x[i] * x[i] * c1 + c0;
  mid(0);
  PIN();
#pragma unroll
  for (int i = 0; i < N; ++i) a[i] *= x[i];
  mid(1);
  PIN();
#pragma unroll
  for (int i = 0; i < N; ++i) a[i] = f2{__builtin_amdgcn_exp2f(a[i].x), __builtin_amdgcn_exp2f(a[i].y)};
  mid(2);
  PIN();
#pragma unroll
  for (int i = 0; i < N; ++i) a[i] = a[i] * den + den;
  PIN();
#pragma unroll
  for (int i = 0; i < N; ++i) a[i] = f2{__builtin_amdgcn_rcpf(a[i].x), __builtin_amdgcn_rcpf(a[i].y)};
  mid(3);
  PIN();
#pragma unroll
  for (int i = 0; i < N; ++i) a[i] *= x[i];
  PIN();
#pragma unroll
  for (int i = 0; i < N; ++i) o[i] = pk2(a[i].x, a[i].y);
}

#define STAMP(i)
#define STARG
#define STPASS(n)

static constexpr int RT_BYTES = 32 * 896;
template <bool WIDE> struct Job {
  static constexpr int NL = WIDE ? 7 : 5, RB = WIDE ? 896 : 640, PITCH = WIDE ? F_ * 2 : D_ * 2;
  v4u r[NL];
  unsigned dst;
  DI void load(int job, int w, const unsigned (&vo)[NL], const void* w1, const void* w2, const void* wx) {
    const bool big = WIDE || job >= 80;
    const int jj = WIDE ? job : (job >= 80 ? job - 80 : (job >= 40 ? job - 40 : job));
    const int gq = WIDE ? jj & 3 : jj & 1, ks = WIDE ? jj >> 2 : jj >> 1;
    const rsrc_t rs = mkrs(big ? wx : (job >= 40 ? w2 : w1), big ? D_ * F_ * 2 : D_ * D_ * 2);
    const unsigned src = (32 * ks + 8 * w) * PITCH + gq * RB;
    dst = (WIDE ? O_W3 : (job >= 80 ? O_W4 : (job >= 40 ? O_W2 : O_W1))) + ((gq * 4 + w) * (!WIDE && job >= 80 ? 56 : 20) + ks) * (NL * 1024);
#pragma unroll
    for (int i = 0; i < NL; ++i)
      r[i] = ld128(rs, (WIDE && gq == 3 && (vo[i] & 1)) ? OOB : (vo[i] & ~1u) + src, 0);
  }
  DI void put(int w, int lane, LAS char* L) {
#pragma unroll
    for (int i = 0; i < NL; ++i) *(LAS v4u*)(L + w * (8 * RB) + lane * 16 + i * 1024) = r[i];
  }
  DI void emit(int w, int lane, LAS char* L, rsrc_t ws) {
    const int g = lane >> 4, q = (lane & 15) >> 2, p = lane & 3;
    LAS char* row = L + (8 * g + q) * RB + w * (RB / 4);
    LAS char* pr = row + 32 * (w & 1) + 16 * p;
    LAS char* sg = row + ((w & 1) ? 0 : RB / 4 - 32) + 8 * p;
    v2u lo[NL], hi[NL];
#pragma unroll
    for (int f = 0; f < NL; ++f) {
      LAS char* c = f < NL - 1 ? pr + 64 * (f >> 1) + 8 * (f & 1) : sg;
      lo[f] = __builtin_bit_cast(v2u, __builtin_amdgcn_ds_read_tr16_b64_v4i16((LAS s4v*)c));
      hi[f] = __builtin_bit_cast(v2u, __builtin_amdgcn_ds_read_tr16_b64_v4i16((LAS s4v*)(c + 4 * RB)));
    }
#pragma unroll
    for (int f = 0; f < NL; ++f) hst128(v4u{lo[f].x, lo[f].y, hi[f].x, hi[f].y}, ws, lane * 16 + dst, f * 1024);
  }
};

template <class Extra, class Gap>
DI void gemm_ks(rsrc_t ws, unsigned abase, const unsigned (&vo)[4], const v4u* b, LAS char* L, int w, int lane, f4 (&acc)[5],
                Extra extra, Gap gap STARG) {
#define WIN12 12
  constexpr int KS = 5, WIN = WIN12, NA = 4 * KS, RING = WIN + 1;
  v4u a[RING];
  unsigned so[4];
#pragma unroll
  for (int i = 0; i < 4; ++i) so[i] = abase + ((w + 1 + i) & 3) * 20480;
  auto lda = [&](int n) { a[n % RING] = hld128(ws, vo[n / KS] + ((n % KS) & 3) * 1024, so[n / KS] + ((n % KS) >> 2) * 4096); };
#pragma unroll
  for (int n = 0; n < WIN; ++n) {
    lda(n);
    PIN();
  }
  f4 ac[2][5], p[15];
  unsigned lr = RED_OFF + lane * 16;
  KEEP(lr);
  sfor<0, NA>([&](auto ic) {
    constexpr int n = decltype(ic)::v, i = n / KS, k = n % KS;
    if (n == NA - 3) {
      STAMP(sb + 1);
      WGBAR();
      STAMP(sb + 2);
    }
#pragma unroll
    for (int t = 0; t < 5; ++t) {
      if (k == 0) mfma_av0(ac[i & 1][t], b[t], a[n % RING]);
      else mfma_av(ac[i & 1][t], b[k * 5 + t], a[n % RING]);
      if (t == 1) {
        if constexpr (n + WIN < NA) lda(n + WIN);
      }
      if (t == (n + WIN < NA ? 4 : 1) && n >= NA - 12) extra(n - (NA - 12));
      if (k == 0 && i > 0 && t > 0) *(LAS f4*)(L + (lr + ((w + i) & 3) * 15360) + ((i - 1) * 5 + t - 1) * 1024) = ac[(i - 1) & 1][t - 1];
      if (k == 1 && i > 0 && t == 0) *(LAS f4*)(L + (lr + ((w + i) & 3) * 15360) + ((i - 1) * 5 + 4) * 1024) = ac[(i - 1) & 1][4];
      if (n == NA - 3) {
        p[t] = *(LAS f4*)(L + (lr + w * 15360) + t * 1024);
        p[5 + t] = *(LAS f4*)(L + (lr + w * 15360) + (5 + t) * 1024);
      }
      if (n == NA - 2) p[10 + t] = *(LAS f4*)(L + (lr + w * 15360) + (10 + t) * 1024);
      if (n == NA - 1) {
        p[t][0] = add1(p[t][0], p[5 + t][0]);
        p[t][1] = add1(p[t][1], p[5 + t][1]);
        KEEP(p[t]);
      }
      gap(n, t);
      PIN();
    }
    if (n == 0) STAMP(sb);
  });
#pragma unroll
  for (int t = 0; t < 5; ++t) {
    p[t][2] += p[5 + t][2];
    p[t][3] += p[5 + t][3];
    p[t] += p[10 + t];
    KEEP(p[t]);
  }
  PIN();
#pragma unroll
  for (int t = 0; t < 5; ++t) acc[t] = ac[1][t] + p[t];
}

#define YOUNG() do { asm volatile("; YOUNG" ::: "memory"); PIN(); } while (0)
#define WAITV(n) asm volatile("s_waitcnt vmcnt(" #n ") ; WAITV" ::: "memory")
__global__ __launch_bounds__(256, 1) void fused(const void*, const void*, const void*, const void*, const void*, const void*,
                                                const void*, const void*, const void*, const void*, const void*, const void*,
                                                void*, void*) {
  extern __shared__ __attribute__((aligned(16))) char smem[];
  LAS char* L = (LAS char*)smem;
  STAMP(0);
  unsigned pf = 0;
  if (blockIdx.x >= 160) {
    unsigned long long pc;
    unsigned off = (((blockIdx.x >> 3) & 1) * 256 + threadIdx.x) * 128;
    asm volatile("s_getpc_b64 %1\n.Lfused_pc:\n\tv_min_u32 %2, .Lfused_end-.Lfused_pc, %2\n\tglobal_load_dword %0, %2, %1"
                 : "=&v"(pf), "=&s"(pc), "+v"(off));
  }
  PIN();
  typedef void* const __attribute__((address_space(4))) * karg_t;
  const karg_t ka = (karg_t)__builtin_amdgcn_kernarg_segment_ptr();
  void *gq = ka[0], *gk = ka[1], *gv = ka[2], *gsk = ka[3], *gssm = ka[4], *gD = ka[5], *gw1 = ka[6], *gw2 = ka[7], *gw3 = ka[8],
       *gw4 = ka[9], *gc = ka[10], *gwm = ka[11], *gout = ka[12], *gws = ka[13];
  asm volatile("" ::"s"(gq), "s"(gk), "s"(gv), "s"(gsk), "s"(gssm), "s"(gD), "s"(gw1), "s"(gw2), "s"(gw3), "s"(gw4),
               "s"(gc), "s"(gwm), "s"(gout), "s"(gws));
  const int tid = threadIdx.x, lane = tid & 63;
  const int w = __builtin_amdgcn_readfirstlane(tid >> 6);
  const int b = blockIdx.x, x = b & 7, j = b >> 3;
  const int tm = 4 * x + (j >> 3), tn = j & 7;
  const int rg = 2 * x + (j >> 4), cg = j & 15;
  const bool conv_wg = j < 20;
  const rsrc_t ws = mkrs(gws, O_END);
  const int g = lane >> 4, l15 = lane & 15;

  const int rt0 = 64 * tm + 8 * tn + 2 * w;
  const int rcm = 8 * lane, rco = 512 + 2 * lane;
  v4u qm[2];
  unsigned qo[2];
  auto load_q = [&]() {
    const rsrc_t rq = mkrs(gq, SD2);
#pragma unroll
    for (int t = 0; t < 2; ++t) {
      qm[t] = ld128(rq, rcm * 2, (rt0 + t) * D_ * 2);
      qo[t] = ld32(rq, rco * 2, (rt0 + t) * D_ * 2);
    }
  };
  v2u tag;
  auto make_tag = [&](v2u ep) {
    tag.x = __builtin_amdgcn_readfirstlane(ep.x) * 0x9E3779B1u + 0x7F4A7C15u;
    tag.y = (__builtin_amdgcn_readfirstlane(ep.y) ^ tag.x) * 0x85EBCA77u + 0x2545F491u;
    tag.x &= 0xffffff00u;
  };
  v4u b1[25], b2[25];
  const unsigned sb1 = O_W1 + (tn * 100 + w * 25) * 1024, sb2 = O_W2 + (tn * 100 + w * 25) * 1024;
  auto zero_h1 = [&](int i) {
    hst128(v4u{0u, 0u, 0u, 0u}, ws, (i < 2 || tid < 128) ? tid * 16 : OOB, O_H1 + b * 10240 + i * 4096);
  };
  v4u mm[5];
  unsigned mo[5];
  auto load_mod = [&](bool ok) {
    unsigned o1 = (ok ? 0u : OOB) | (lane * 16), o2 = (ok ? 0u : OOB) | (lane * 4 + 1024);
    asm volatile("" : "+v"(o1), "+v"(o2));
#pragma unroll
    for (int i = 0; i < 5; ++i) {
      mm[i] = hld128(ws, o1, O_MOD + i * D_ * 2);
      mo[i] = __builtin_amdgcn_raw_buffer_load_b32(ws, o2, O_MOD + i * D_ * 2, 16);
    }
  };
  f2 qv[10], n1[10];
  auto ln1 = [&]() {
#pragma unroll
    for (int i = 0; i < 4; ++i) {
      qv[2 * i] = f2{lo2f(qm[0][i]), lo2f(qm[1][i])};
      qv[2 * i + 1] = f2{hi2f(qm[0][i]), hi2f(qm[1][i])};
    }
    qv[8] = f2{lo2f(qo[0]), lo2f(qo[1])};
    qv[9] = f2{hi2f(qo[0]), hi2f(qo[1])};
    f2 s = qv[0], ss = qv[0] * qv[0];
#pragma unroll
    for (int i = 1; i < 10; ++i) { s += qv[i]; ss += qv[i] * qv[i]; }
    wave_sum4(s, ss);
    f2 r, mr;
    ln_scal(s, ss, r, mr);
#pragma unroll
    for (int i = 0; i < 10; ++i) n1[i] = qv[i] * r + mr;
  };
  unsigned z3off, im0, io0, roff, roff3, own_r, x1m, x1s, stoff, h1m, h1s, f1pub, f1wait, frpub, frwait, f2pub, f2wait, f3pub, f3wait;
  unsigned v2o[4];
  const unsigned own_m = ((4 * tm + w) * 20 + 5 * (tn >> 1) + 3 * (tn & 1)) * 1024;
  const unsigned own_s = ((4 * tm + w) * 20 + 5 * (tn >> 1) + 2) * 1024 + (tn & 1) * 512;
  auto offsets = [&]() {
    z3off = tid < 128 ? tid * 16 + 8192 : OOB;
    im0 = RED_OFF + 16384 + lane * 128 + (((2 * w) ^ (lane & 7)) << 4);
    io0 = RED_OFF + 16384 + (64 + (lane >> 2)) * 128 + (((2 * w) ^ ((lane >> 2) & 7)) << 4) + 4 * (lane & 3);
    roff = O_A1 + ((4 * tm + (tn >> 1)) * 20 + (tid >> 5)) * 1024 + (((tid ^ (tid >> 3)) & 7) + 8 * (tn & 1) + 16 * ((tid >> 3) & 3)) * 16;
    roff3 = tid < 128 ? roff : OOB;
    own_r = (l15 + 16 * (g >> 1)) * 16 + 8 * (g & 1);
    x1m = O_X1 + own_m + lane * 16;
    x1s = (g & 1) ? OOB + SD2 : O_X1 + own_s + (l15 + 16 * (g >> 1)) * 16;
    stoff = (g & 1) ? OOB : O_STAT + ((4 * tm + w) * 8 + tn) * 128 + l15 * 8 + (g >> 1) * 4;
    f3pub = lane == 0 ? O_F3 + rg * 512 + (w >> 1) * 256 + cg * 16 + (w & 1) * 8 : OOB;
    f3wait = lane < 8 ? O_F3 + rg * 512 + (tm & 1) * 256 + w * 64 + lane * 8 : OOB;
    const int sh = (tn >> 1) == 0 ? 1 : ((tn >> 1) == 1 ? -1 : ((tn >> 1) == 2 ? 2 : -2));
    const int t2 = 64 * tm + 16 * w + l15 + sh;
    const bool ok = t2 >= 0 && t2 < S_;
    const unsigned d = O_H1 + ((t2 >> 4) * 20 + 5 * (tn >> 1)) * 1024 + (t2 & 15) * 16;
    h1m = ok ? d + 3072 * (tn & 1) + 256 * g : OOB;
    h1s = (ok && !(g & 1)) ? d + 2048 + 512 * (tn & 1) + 256 * (g >> 1) : OOB;
    const int nb = tm + (sh > 0 ? 1 : -1);
    f1pub = OOB;
    if (lane == 0) f1pub = O_F1 + tm * 128 + tn * 8;
    if (lane == 1 && nb >= 0 && nb < 32) f1pub = O_F1 + nb * 128 + 64 + tn * 8;
    const bool up = ((lane >> 1) & 1) == 0;
    f1wait = (lane < 8 || (lane < 16 && (up ? tm > 0 : tm < 31))) ? O_F1 + tm * 128 + lane * 8 : OOB;
    frpub = lane == 0 ? O_FR + tm * 128 + tn * 8 : OOB;
    frwait = lane < 8 ? O_FR + tm * 128 + lane * 8 : OOB;
    f2pub = lane == 0 ? O_F2 + (tm >> 1) * 128 + ((tm & 1) * 8 + tn) * 8 : OOB;
    f2wait = lane < 16 ? O_F2 + rg * 128 + lane * 8 : OOB;
    asm volatile("" : "+v"(frpub), "+v"(frwait), "+v"(f2pub), "+v"(f2wait));
    asm volatile("" : "+v"(z3off), "+v"(im0), "+v"(io0), "+v"(roff), "+v"(roff3), "+v"(own_r), "+v"(f1pub), "+v"(f1wait));
    asm volatile("" : "+v"(x1m), "+v"(x1s), "+v"(stoff), "+v"(f3pub), "+v"(f3wait));
    asm volatile("" : "+v"(h1m), "+v"(h1s));
  };
  constexpr int NB2 = 19;
  bool wok = false, bok = false;
  if (conv_wg) {
    const int cgp = j < 16 ? x : 8 + (x >> 2), ci = j < 16 ? j : (x & 3) * 4 + (j - 16);
    const int pT0 = 128 * ci + 32 * w + 8 * ((lane >> 3) & 3) + 4 * (lane >> 5), pC0 = 64 * cgp + 8 * (lane & 7);
    const int ch = 64 * cgp + 4 * ci + w;
    v4u kw[7], vw[4], skw[4], kr[4];
    {
      const rsrc_t rk = mkrs(gk, SD2), rv = mkrs(gv, SD2), rs = mkrs(gsk, D_ * 8);
      const unsigned o = (unsigned)(pT0 * D_ + pC0) * 2;
#pragma unroll
      for (int i = 0; i < 7; ++i) kw[i] = __builtin_amdgcn_raw_buffer_load_b128(rk, o + (i - 3) * D_ * 2, 0, KAUX);
#pragma unroll
      for (int i = 0; i < 4; ++i) vw[i] = ld128(rv, o + i * D_ * 2, 0);
#pragma unroll
      for (int i = 0; i < 4; ++i) skw[i] = __builtin_amdgcn_raw_buffer_load_b128(rs, pC0 * 8 + i * 16, 0, 0);
      PIN();
      offsets();
      PIN();
      asm volatile("s_waitcnt vmcnt(4)" ::: "memory");
      const rsrc_t rK = mkrs((const char*)gssm + (size_t)ch * 4096, 4096);
#pragma unroll
      for (int i = 0; i < 4; ++i) kr[i] = ld128(rK, lane * 16 + i * 1024, 0);
    }
    const v2u ep = hld64(ws, lane == 0 ? O_EPOCH : O_FP + x * 256 + (j >= 16) * 128, 0);
    const unsigned dvb = __builtin_amdgcn_raw_buffer_load_b16(mkrs(gD, D_ * 2), ch * 2, 0, 0);
    PIN();
    {
      unsigned pe[8][2];
#pragma unroll
      for (int pr = 0; pr < 4; ++pr) {
        const f2 w0 = {lo2f(skw[pr].x), lo2f(skw[pr].z)}, w1 = {hi2f(skw[pr].x), hi2f(skw[pr].z)};
        const f2 w2 = {lo2f(skw[pr].y), lo2f(skw[pr].w)}, w3 = {hi2f(skw[pr].y), hi2f(skw[pr].w)};
        f2 kf[7], o[4];
#pragma unroll
        for (int i = 0; i < 7; ++i) kf[i] = up2(kw[i][pr]);
#pragma unroll
        for (int i = 0; i < 4; ++i) o[i] = (kf[i + 3] * w0 + kf[i + 2] * w1 + kf[i + 1] * w2 + kf[i] * w3) * up2(vw[i][pr]);
        pe[2 * pr][0] = pk2(o[3].x, o[2].x);
        pe[2 * pr][1] = pk2(o[1].x, o[0].x);
        pe[2 * pr + 1][0] = pk2(o[3].y, o[2].y);
        pe[2 * pr + 1][1] = pk2(o[1].y, o[0].y);
      }
      const unsigned so = (pC0 + 4 * (lane >> 5)) * KVP + (pT0 & ~7) * 2;
#pragma unroll
      for (int e = 0; e < 4; ++e) {
        const auto s0 = __builtin_amdgcn_permlane32_swap(pe[e][0], pe[e + 4][0], false, false);
        const auto s1 = __builtin_amdgcn_permlane32_swap(pe[e][1], pe[e + 4][1], false, false);
        hst128(v4u{s0[1], s1[1], s0[0], s1[0]}, ws, so, O_KVT + e * KVP);
      }
    }
    STAMP(1);
    PIN();
    LAS char* T = L + w * WR;
    if (lane < 8) *(LAS v4u*)(T + lane * CS) = v4u{0u, 0u, 0u, 0u};
#pragma unroll
    for (int i = 0; i < 4; ++i) *(LAS v4u*)(T + 7 * CS + (i * 64 + lane + 1) * 16) = kr[i];
    asm volatile("s_waitcnt lgkmcnt(0)" ::: "memory");
    v4u kl[4];
#pragma unroll
    for (int i = 0; i < 4; ++i) kl[i] = *(LAS v4u*)(T + 7 * CS + (i * 64 + lane) * 16);
    auto tbuild = [&](int i) {
      const unsigned d[8] = {kl[i].x, kl[i].y, kl[i].z, kl[i].w, kr[i].x, kr[i].y, kr[i].z, kr[i].w};
      unsigned a[7];
#pragma unroll
      for (int e = 0; e < 7; ++e) a[e] = __builtin_amdgcn_alignbit(d[e + 1], d[e], 16);
#pragma unroll
      for (int m = 0; m < 7; ++m) {
        const int h = (m + 1) >> 1;
        const v4u o = (m & 1) ? v4u{d[h], d[h + 1], d[h + 2], d[h + 3]} : v4u{a[h], a[h + 1], a[h + 2], a[h + 3]};
        *(LAS v4u*)(T + m * CS + (i * 64 + lane + 1) * 16) = o;
      }
    };
    tbuild(0);
    make_tag(ep);
    DRAIN();
    asm volatile("s_barrier" ::: "memory");
    if (w == 0) {
      unsigned o = OOB;
      if (j < 16 && lane == 0) o = O_FP + x * 256 + ci * 8;
      if (j >= 16 && lane < 4) o = O_FP + ((x & 4) + lane) * 256 + 128 + ci * 8;
      hst64(tag, ws, o, 0);
    }
    STAMP(2);
    tbuild(1);
    tbuild(2);
    tbuild(3);
    if (lane < 32) *(LAS v4u*)(T + UOFF + lane * 16) = v4u{0u, 0u, 0u, 0u};
    STAMP(3);
    if (w == 0) poll64(ws, lane < 16 ? O_FP + x * 256 + (j >= 16) * 128 + lane * 8 : OOB, tag);
    WGBAR();
    STAMP(4);
    const v4u wf = hld128(ws, O_FW + x * 1024 + lane * 16, 0);
    v4u ur[4];
#pragma unroll
    for (int i = 0; i < 4; ++i) ur[i] = hld128(ws, lane * 16 + i * 1024, O_KVT + ch * KVP);
    const LAS char* pa = T + (l15 & 7) * CS + (3 - g + (l15 >> 3)) * 16;
    const LAS char* pv = T + UOFF + 512 + (l15 - 1) * 32 + g * 16;
    v4u fa[2][8], fv[2][8];
#pragma unroll
    for (int a = 0; a < 8; ++a) fa[0][a] = *(const LAS v4u*)(pa + 8 * a * 64);
    f4 acc[8];
    v4u ad;
    {
      const int jd = 8 * g - 9 - l15;
#pragma unroll
      for (int e = 0; e < 4; ++e) ad[e] = jd == 2 * e ? dvb : (jd == 2 * e + 1 ? dvb << 16 : 0u);
      asm volatile("" : "+v"(ad));
    }
    PIN();
#pragma unroll
    for (int i = 0; i < 4; ++i) *(LAS v4u*)(T + UOFF + 512 + i * 1024 + lane * 16) = ur[i];
    asm volatile("s_waitcnt lgkmcnt(0)" ::: "memory");
#pragma unroll
    for (int a = 0; a < 8; ++a) fv[0][a] = *(const LAS v4u*)(pv + a * 512);
    wok = wflags_ok(wf, lane, 40, tag);
    __builtin_amdgcn_fence(__ATOMIC_ACQUIRE, "wavefront");
    const unsigned boff = wok ? lane * 16 : OOB;
    STAMP(5);
#pragma unroll
    for (int q = 0; q < 8; ++q) mfma_v0(acc[q], ad, fv[0][q]);
    PIN();
#pragma unroll
    for (int g0 = 0; g0 < 8; ++g0) {
      int n = 0;
      if (g0 == 0) load_q();
#pragma unroll
      for (int a = 0; a < 8; ++a)
#pragma unroll
        for (int q = a; q < 8; ++q) {
          mfma_v(acc[q], fa[g0 & 1][a], fv[g0 & 1][q - a]);
          ++n;
          if ((n & 1) == 0) {
            const int e = n / 2 - 1;
            if (g0 < 7 && e < 16) {
              if (e < 8) fv[(g0 + 1) & 1][e] = *(const LAS v4u*)(pv + e * 512 - (g0 + 1) * 64);
              else fa[(g0 + 1) & 1][e - 8] = *(const LAS v4u*)(pa + (g0 + 1 + 8 * (e - 8)) * 64);
            }
            PIN();
          }
          if (n % 5 == 0) {
            const int i = g0 * 7 + n / 5 - 13;
            if (i < -9) zero_h1(i + 12);
            if (i >= 0 && i < 25) b1[i] = hld128(ws, boff + (i & 3) * 1024, sb1 + (i >> 2) * 4096);
            if (i >= 25 && i < 50) b2[i - 25] = hld128(ws, boff + ((i - 25) & 3) * 1024, sb2 + ((i - 25) >> 2) * 4096);
            PIN();
          }
        }
    }
    const v4u bfl = hld128(ws, O_FB + x * 1024 + lane * 16, 0);
    MFENCE();
    PIN();
    STAMP(6);
#pragma unroll
    for (int q = 0; q < 8; q += 2) {
      const auto s0 = __builtin_amdgcn_permlane16_swap(pk2(acc[q][0], acc[q][1]), pk2(acc[q + 1][0], acc[q + 1][1]), false, false);
      const auto s1 = __builtin_amdgcn_permlane16_swap(pk2(acc[q][2], acc[q][3]), pk2(acc[q + 1][2], acc[q + 1][3]), false, false);
      *(LAS v4u*)(T + 64 * w + (32 * (q + (g & 1)) + 2 * l15 + (g >> 1)) * 16) = v4u{s0[0], s1[0], s0[1], s1[1]};
    }
    WGBAR();
    {
      v4u zo[4];
#pragma unroll
      for (int i = 0; i < 4; ++i) zo[i] = *(LAS v4u*)(L + (lane & 3) * (WR + 64) + (64 * w + 16 * i + (lane >> 2)) * 16);
#pragma unroll
      for (int i = 0; i < 4; ++i) hst128(zo[i], ws, (lane >> 2) * ZP + (lane & 3) * 16, O_ZT + (64 * w + 16 * i) * ZP + (ch - w) * 16);
    }
    YOUNG();
    bok = wflags_ok(bfl, lane, 48, tag);
    __builtin_amdgcn_fence(__ATOMIC_ACQUIRE, "wavefront");
    load_mod(bok);
    PIN();
    ln1();
    PIN();
    WAITV(10);
  } else {
    const int hb = (j - 20) * 8 + x;
    unsigned vn[5];
#pragma unroll
    for (int i = 0; i < 5; ++i) vn[i] = (i * 64 + lane) * 16 + (((i * 64 + lane) * 1639u) >> 16) * 640;
    Job<false> j0, j1;
    if (HDELAY) __builtin_amdgcn_s_sleep(HDELAY);
    j0.load(hb, w, vn, gw1, gw2, gw4);
    const v2u ep = hld64(ws, O_EPOCH, 0);
    const v2u cw = __builtin_amdgcn_raw_buffer_load_b64(mkrs(gc, D_ * 2), tid * 8, 0, 0);
    PIN();
    unsigned vw[7];
#pragma unroll
    for (int i = 0; i < 7; ++i) {
      const unsigned c = i * 64 + lane, row = (c * 1171u) >> 16;
      vw[i] = c * 16 + row * 2560 + (c - row * 56 >= 48);
      KEEP(vw[i]);
    }
    offsets();
    LAS float* sc = (LAS float*)(L + 4 * RT_BYTES);
    {
      const float c4[4] = {lo2f(cw.x), hi2f(cw.x), lo2f(cw.y), hi2f(cw.y)};
      f4 sv;
#pragma unroll
      for (int e = 0; e < 4; ++e) sv[e] = rbf(c4[e] * __builtin_amdgcn_rcpf(1.f + __builtin_amdgcn_exp2f(-1.4426950408889634f * c4[e])));
      *(LAS f4*)(sc + tid * 4) = sv;
    }
    j0.put(w, lane, L);
    make_tag(ep);
    WGBAR();
    j0.emit(w, lane, L, ws);
    if (j == 20 && w == 0) hst128(v4u{tag.x, tag.y, 0u, 0u}, ws, (lane >> 3) * 1024 + (lane & 7) * 16, O_CNT + x * 128);
    asm volatile("" ::"v"(pf) : "memory");
    STAMP(1);
    auto first_pub = [&]() {
      WGBAR();
      if (w == 0) hst64(tag, ws, lane < 8 ? O_FW + lane * 1024 + hb * 8 : OOB, 0);
      STAMP(2);
    };
    if (hb < 60) {
      const rsrc_t rm = mkrs(gwm, D_ * 6 * D_ * 2);
      v4u wm[20];
      YOUNG();
#pragma unroll
      for (int i = 0; i < 8; ++i) wm[i] = ld128(rm, (tid >> 3) * (6 * D_ * 2) + (tid & 7) * 16, hb * 128 + i * 32 * (6 * D_ * 2));
      PIN();
      WAITV(8);
      first_pub();
#pragma unroll
      for (int i = 8; i < 20; ++i) wm[i] = ld128(rm, (tid >> 3) * (6 * D_ * 2) + (tid & 7) * 16, hb * 128 + i * 32 * (6 * D_ * 2));
      j1.load(132 + hb, w, vn, gw1, gw2, gw4);
      load_q();
      float sl[20];
#pragma unroll
      for (int i = 0; i < 20; ++i) sl[i] = sc[(tid >> 3) + 32 * i];
      PIN();
      f2 a[4] = {f2{0.f, 0.f}, f2{0.f, 0.f}, f2{0.f, 0.f}, f2{0.f, 0.f}};
#pragma unroll
      for (int i = 0; i < 20; ++i)
#pragma unroll
        for (int e = 0; e < 4; ++e) a[e] += up2(wm[i][e]) * sl[i];
      LAS char* mred = L + 4 * RT_BYTES + 4096;
      *(LAS f4*)(mred + (tid >> 3) * 272 + (tid & 7) * 32) = f4{a[0].x, a[0].y, a[1].x, a[1].y};
      *(LAS f4*)(mred + (tid >> 3) * 272 + (tid & 7) * 32 + 16) = f4{a[2].x, a[2].y, a[3].x, a[3].y};
      WGBAR();
      {
        const int col = tid >> 2, part = tid & 3;
        float v[8];
#pragma unroll
        for (int r = 0; r < 8; ++r) v[r] = *(LAS float*)(mred + (part * 8 + r) * 272 + col * 4);
        float s = ((v[0] + v[1]) + (v[2] + v[3])) + ((v[4] + v[5]) + (v[6] + v[7]));
        s += dpp<0xB1>(s);
        s += dpp<0x4E>(s);
        s = rbf(s);
        if ((hb >= 10 && hb < 20) || (hb >= 40 && hb < 50)) s = rbf(1.f + s);
        if (part == 0) *((LAS unsigned short*)sc + 8 + col) = (unsigned short)(__builtin_bit_cast(unsigned, s) >> 16);
      }
      j1.put(w, lane, L);
      WGBAR();
      if (w == 0) {
        hst128(*(LAS v4u*)((LAS char*)sc + 16 + (lane & 7) * 16), ws, lane < 8 ? lane * 16 : OOB, O_MOD + hb * 128);
        DRAIN();
        hst64(tag, ws, lane < 8 ? O_FB + lane * 1024 + hb * 8 : OOB, 0);
      }
      STAMP(3);
    } else {
      Job<true> ja, jb, jc;
      YOUNG();
      ja.load(hb - 60, w, vw, gw1, gw2, gw3);
      PIN();
      WAITV(7);
      first_pub();
      jb.load(hb - 24, w, vw, gw1, gw2, gw3);
      if (hb < 68) jc.load(hb + 12, w, vw, gw1, gw2, gw3);
      j1.load(36 + hb, w, vn, gw1, gw2, gw4);
      load_q();
      PIN();
      ja.put(w, lane, L + RT_BYTES);
      jb.put(w, lane, L + 2 * RT_BYTES);
      if (hb < 68) jc.put(w, lane, L + 3 * RT_BYTES);
      WGBAR();
      ja.emit(w, lane, L + RT_BYTES, ws);
      jb.emit(w, lane, L + 2 * RT_BYTES, ws);
      if (hb < 68) jc.emit(w, lane, L + 3 * RT_BYTES, ws);
      j1.put(w, lane, L);
      DRAIN();
      WGBAR();
      if (w == 0) hst64(tag, ws, lane < 8 ? O_FB + lane * 1024 + hb * 8 : OOB, 0);
      STAMP(3);
    }
    pollw(ws, O_FW + x * 1024 + lane * 16, lane, 40, tag);
    j1.emit(w, lane, L, ws);
    zero_h1(0);
    zero_h1(1);
    zero_h1(2);
    if (hb >= 34 && hb < 60) hst128(v4u{0u, 0u, 0u, 0u}, ws, w == 0 ? lane * 16 : OOB, O_FR + (hb - 34) * 1024);
    STAMP(4);
    YOUNG();
#pragma unroll
    for (int i = 0; i < 10; ++i) b1[i] = hld128(ws, lane * 16, sb1 + i * 1024);
    PIN();
    ln1();
    PIN();
    WAITV(10);
  }
  if (conv_wg && !wok) pollw(ws, O_FW + x * 1024 + lane * 16, lane, 40, tag);
  WGBAR();
  STAMP(7);
  if (w == 0 && lane < 8)
    __hip_atomic_fetch_add((GAS unsigned*)((GAS char*)gws + O_CNT + (lane * 8 + x) * 128), 1u, __ATOMIC_RELAXED, __HIP_MEMORY_SCOPE_AGENT);
  if (!wok) {
    if (conv_wg) {
#pragma unroll
      for (int i = 0; i < 10; ++i) b1[i] = hld128(ws, lane * 16, sb1 + i * 1024);
    }
#pragma unroll
    for (int i = 10; i < 25; ++i) b1[i] = hld128(ws, lane * 16, sb1 + i * 1024);
#pragma unroll
    for (int i = 0; i < NB2; ++i) b2[i] = hld128(ws, lane * 16, sb2 + i * 1024);
  }
#pragma unroll
  for (int i = NB2; i < 25; ++i) b2[i] = hld128(ws, lane * 16, sb2 + i * 1024);
  v4u b3[35], b4[70];
  const unsigned sb3 = O_W3 + (cg * 140 + w * 35) * 1024, sb4 = O_W4 + (tn * 280 + w * 70) * 1024;
#define LB3(lo, hi) _Pragma("unroll") for (int i_ = lo; i_ < hi; ++i_) b3[i_] = hld128(ws, lane * 16 + (i_ & 3) * 1024, sb3 + (i_ >> 2) * 4096)
  auto ks4 = [](int s) { return s < 8 ? 7 * (s >> 2) + (s & 1) + 4 * ((s >> 1) & 1) : 7 * ((s - 8) / 3) + ((s - 8) % 3 == 2 ? 6 : 2 + (s - 8) % 3); };
  auto lb4 = [&](int i) {
    const int f = ks4(i / 5) * 5 + i % 5;
    b4[i] = hld128(ws, lane * 16 + (f & 3) * 1024, sb4 + (f >> 2) * 4096);
  };
#define LB4(lo, hi) _Pragma("unroll") for (int i_ = lo; i_ < hi; ++i_) lb4(i_)
  if (!bok) {
    pollw(ws, O_FB + x * 1024 + lane * 16, lane, 48, tag);
    load_mod(true);
  }
  constexpr int NB3C = 10;
  f2 gh[10];
  float cb1[10], sbv[10];
  auto md = [&](int v, int i) { const unsigned u = i < 8 ? mm[v][(i >> 1) & 3] : mo[v]; return (i & 1) ? hi2f(u) : lo2f(u); };
  auto use_mod = [&]() {
    LB3(0, NB3C);
#pragma unroll
    for (int i = 0; i < 10; ++i) {
      gh[i] = (n1[i] * md(1, i) + md(0, i)) * md(2, i);
      cb1[i] = md(4, i);
      sbv[i] = md(3, i);
      asm volatile("" : "+v"(gh[i]), "+v"(cb1[i]), "+v"(sbv[i]));
    }
  };
  use_mod();
  PIN();
  if (w == 0) {
    poll64(ws, lane < 8 ? O_CNT + (x * 8 + lane) * 128 : OOB, v2u{tag.x + 32, tag.y});
    if (b == 0) hst64(tag, ws, lane == 0 ? O_EPOCH : OOB, 0);
  }
  WGBAR();
  STAMP(8);

  v4u hx[6];
  f4 csp[7];
  auto img3 = [&](int lo, int hi, int hc) {
    const v4u ones = {0x3f803f80u, 0x3f803f80u, 0x3f803f80u, 0x3f803f80u};
    unsigned la = w * 25600 + lane * 16, lb = (75 + w * 10) * 1024 + lane * 16;
    asm volatile("" : "+v"(la), "+v"(lb));
#pragma unroll
    for (int i = lo; i < 35; ++i) {
      if (i < hi) *(LAS v4u*)(L + (i < 25 ? la : lb) + i * 1024) = b3[i];
      if (i < hc) csp[i % 7] = mfma(b3[i], ones, i < 7 ? f4{0.f, 0.f, 0.f, 0.f} : csp[i % 7]);
      PIN();
    }
  };
  {
    v4u zc[3];
#pragma unroll
    for (int i = 0; i < 3; ++i) zc[i] = hld128(ws, i < 2 ? tid * 16 + i * 4096 : z3off, O_ZT + (8 * tm + tn) * ZP);
    PIN();
#pragma unroll
    for (int i = 0; i < 3; ++i)
#pragma unroll
      for (int e = 0; e < 4; ++e) *(LAS unsigned*)(L + RED_OFF + e * 3072 + (tid + 256 * i) * 4) = zc[i][e];
    STAMP(9);
    WGBAR();
    STAMP(10);
    const v4u z0 = *(LAS v4u*)(L + RED_OFF + w * 3072 + rcm * 4), z1 = *(LAS v4u*)(L + RED_OFF + w * 3072 + rcm * 4 + 16);
    const v2u z2 = *(LAS v2u*)(L + RED_OFF + w * 3072 + rco * 4);
    LB3(10, 19);
    f2 xv[10], hv[10], av[10], s, ss, r, mr;
#pragma unroll
    for (int i = 0; i < 10; ++i) {
      xv[i] = gh[i] * up2(i < 4 ? z0[i & 3] : (i < 8 ? z1[i & 3] : z2[i & 1])) + qv[i];
      s = i ? s + xv[i] : xv[i];
      ss = i ? ss + xv[i] * xv[i] : xv[i] * xv[i];
    }
    wave_sum4(s, ss);
    ln_scal(s, ss, r, mr);
#pragma unroll
    for (int i = 0; i < 10; ++i) {
      hv[i] = (xv[i] * r + mr) * cb1[i] + sbv[i];
      s = i ? s + hv[i] : hv[i];
      ss = i ? ss + hv[i] * hv[i] : hv[i] * hv[i];
    }
    wave_sum4(s, ss);
    ln_scal(s, ss, r, mr);
#pragma unroll
    for (int i = 0; i < 10; ++i) av[i] = hv[i] * r + mr;
    auto put = [&](const f2(&v)[10], int img) {
      *(LAS v4u*)(L + im0 + img * 10240) = v4u{pk2(v[0].x, v[1].x), pk2(v[2].x, v[3].x), pk2(v[4].x, v[5].x), pk2(v[6].x, v[7].x)};
      *(LAS v4u*)(L + (im0 ^ 16) + img * 10240) = v4u{pk2(v[0].y, v[1].y), pk2(v[2].y, v[3].y), pk2(v[4].y, v[5].y), pk2(v[6].y, v[7].y)};
      *(LAS unsigned*)(L + io0 + img * 10240) = pk2(v[8].x, v[9].x);
      *(LAS unsigned*)(L + (io0 ^ 16) + img * 10240) = pk2(v[8].y, v[9].y);
    };
    put(av, 0);
    STAMP(11);
    WGBAR();
    {
      v4u oc[3];
#pragma unroll
      for (int i = 0; i < 3; ++i) oc[i] = *(LAS v4u*)(L + RED_OFF + 16384 + i * 4096 + tid * 16);
#pragma unroll
      for (int i = 0; i < 3; ++i) hst128(oc[i], ws, i < 2 ? roff : roff3, i * 8192);
    }
    PIN();
    put(hv, 1);
    put(xv, 2);
    STAMP(12);
    DRAIN();
    WGBAR();
    STAMP(13);
    if (w == 0) hst64(tag, ws, frpub, 0);
    LB3(19, 28);
    PIN();
#pragma unroll
    for (int i = 0; i < 6; ++i) hx[i] = *(LAS v4u*)(L + RED_OFF + 26624 + (i / 3) * 10240 + (i % 3) * 4096 + tid * 16);
    img3(0, 16, 16);
    if (w == 0) poll64(ws, frwait, tag);
    WGBAR();
    STAMP(14);
  }

  {
    f4 acc[5];
    const unsigned vo[4] = {(unsigned)lane * 16, (unsigned)lane * 16, (unsigned)lane * 16, (unsigned)lane * 16};
    gemm_ks(ws, O_A1 + (4 * tm * 20 + 5 * w) * 1024, vo, b1, L, w, lane, acc,
            [&](int n) { if (n < 7) { LB3(28 + n, 29 + n); } else lb4(16 + n); },
            [&](int n, int t) {
              if (t == 3 && n >= 8 && n < 14) hst128(hx[n - 8], ws, ((n - 8) % 3) < 2 ? roff : roff3, (1 + (n - 8) / 3) * SD2 + ((n - 8) % 3) * 8192);
            } STPASS(15));
    STAMP(18);
    f2 v[10];
    unsigned o[10];
#pragma unroll
    for (int t = 0; t < 5; ++t) {
      v[2 * t] = f2{acc[t][0], acc[t][1]};
      v[2 * t + 1] = f2{acc[t][2], acc[t][3]};
    }
    gelu_pack<10>(v, 1.f, 1.f, o, [](int) {});
    const auto s0 = __builtin_amdgcn_permlane16_swap(o[8], o[8], false, false);
    const auto s1 = __builtin_amdgcn_permlane16_swap(o[9], o[9], false, false);
    hst128(v4u{o[0], o[1], o[2], o[3]}, ws, h1m, 0);
    hst128(v4u{o[4], o[5], o[6], o[7]}, ws, h1m + 1024, 0);
    hst128(v4u{s0[0], s1[0], s0[1], s1[1]}, ws, h1s, 0);
    STAMP(19);
    DRAIN();
    WGBAR();
    STAMP(20);
    if (w == 0) hst64(tag, ws, f1pub, 0);
    LB4(0, 9);
    PIN();
    img3(16, 24, 35);
    if (l15 == 0) {
#pragma unroll
      for (int t = 0; t < 7; ++t) *(LAS f4*)(L + (w * 25 + 24) * 1024 + (t * 4 + g) * 16) = csp[t];
    }
    if (w == 0) poll64(ws, f1wait, tag);
    WGBAR();
    STAMP(21);
  }
  v4u xr[2];
  v2u xro;
  unsigned x1p[10];
  {
    v4u hr[2];
    v2u hro;
    f4 acc[5];
    f2 hf[10];
    const unsigned vo[4] = {(unsigned)lane * 16, (unsigned)lane * 16, (unsigned)lane * 16, (unsigned)lane * 16};
    gemm_ks(ws, O_H1 + (4 * tm * 20 + 5 * w) * 1024, vo, b2, L, w, lane, acc, [&](int n) {
      lb4(n < 5 ? 9 + n : 28 + n);
    }, [&](int n, int t) {
      if (t == 3 && n == 2) hr[0] = hld128(ws, lane * 16, O_HP + own_m);
      if (t == 3 && n == 4) hr[1] = hld128(ws, lane * 16 + 1024, O_HP + own_m);
      if (t == 3 && n == 6) hro = hld64(ws, own_r, O_HP + own_s);
      if (n == 18) {
        unsigned h0 = t < 4 ? hr[t >> 1][2 * (t & 1)] : hro.x;
        KEEP(h0);
        hf[2 * t] = up2(h0);
        KEEP(hf[2 * t]);
      }
    } STPASS(22));
#pragma unroll
    for (int t = 0; t < 5; ++t) {
      const unsigned h1 = t < 4 ? hr[t >> 1][2 * (t & 1) + 1] : hro.y;
      const f2 p = f2{acc[t][0], acc[t][1]} + hf[2 * t], q = f2{acc[t][2], acc[t][3]} + up2(h1);
      x1p[2 * t] = pk2(p.x, p.y);
      x1p[2 * t + 1] = pk2(q.x, q.y);
    }
    const auto s0 = __builtin_amdgcn_permlane16_swap(x1p[8], x1p[8], false, false);
    const auto s1 = __builtin_amdgcn_permlane16_swap(x1p[9], x1p[9], false, false);
    hst128(v4u{x1p[0], x1p[1], x1p[2], x1p[3]}, ws, x1m, 0);
    hst128(v4u{x1p[4], x1p[5], x1p[6], x1p[7]}, ws, x1m + 1024, 0);
    hst128(v4u{s0[0], s1[0], s0[1], s1[1]}, ws, x1s, 0);
    float t1 = 0.f, t2 = 0.f;
#pragma unroll
    for (int i = 0; i < 10; ++i) {
      t1 = __builtin_amdgcn_fdot2_f32_bf16(__builtin_bit_cast(bf2, x1p[i]), __builtin_bit_cast(bf2, 0x3f803f80u), t1, false);
      t2 = __builtin_amdgcn_fdot2_f32_bf16(__builtin_bit_cast(bf2, x1p[i]), __builtin_bit_cast(bf2, x1p[i]), t2, false);
    }
    t1 = swap32_add(t1, t2);
    t1 = swap16_add(t1, t1);
    hst32(__builtin_bit_cast(unsigned, t1), ws, stoff, 0);
    STAMP(25);
    f4 cst;
    {
      f4 cp[4];
#pragma unroll
      for (int v = 0; v < 4; ++v) cp[v] = *(LAS f4*)(L + (v * 25 + 24) * 1024 + lane * 16);
      cst = (cp[0] + cp[1]) + (cp[2] + cp[3]);
      KEEP(cst);
    }
    PIN();
    DRAIN();
    WGBAR();
    STAMP(26);
    if (w == 0) hst64(tag, ws, f2pub, 0);
    LB4(14, 23);
    PIN();
    img3(24, 35, 0);
    if (w == 1) *(LAS f4*)(L + CSUM_OFF + lane * 16) = cst;
    if (w == 0) poll64(ws, f2wait, tag);
    WGBAR();
    STAMP(27);
  }
#define PUBK 10
#define PUBN 9
#define SMPN 20
#define CHKN 26
#define WIN4_ 8
  constexpr int WIN4 = WIN4_, RING4 = WIN4 + 1;
  v4u a4[RING4];
  auto lda4 = [&](int n) {
    const int i = n < 32 ? n >> 3 : (n - 32) / 6, k = ks4(n < 32 ? n & 7 : 8 + (n - 32) % 6);
    a4[n % RING4] = hld128(ws, lane * 16 + (k & 3) * 1024, O_H + ((4 * tm + ((w + 1 + i) & 3)) * 56 + 14 * w + (k & ~3)) * 1024);
  };
  {
#define W3_ 6
    constexpr int W3 = W3_;
    v4u a[20][2];
    const unsigned sa = O_X1 + (8 * rg + 2 * w) * 20 * 1024;
    auto lda = [&](int k, int i) { a[k][i] = hld128(ws, lane * 16 + (k & 3) * 1024, sa + i * 20480 + (k >> 2) * 4096); };
#pragma unroll
    for (int k = 0; k < W3; ++k)
#pragma unroll
      for (int i = 0; i < 2; ++i) {
        lda(k, i);
        PIN();
      }
    f4 acc[2][7];
    v2u sv[2][2];
    f2 r, den, mean;
    auto bslot = [](int k, int t) { const int v = k / 5, i = (k % 5) * 7 + t; return (i < 25 ? v * 25 + i : 75 + v * 10 + i) * 1024; };
    const LAS char* Lb = L + lane * 16;
    const LAS char* Lb2 = L + lane * 16 + 65536;
    const LAS char* Lb3 = L + lane * 16 + 131072;
    auto rdb = [&](int k, int t) { const int o = bslot(k, t); return *(const LAS v4u*)(o < 65536 ? Lb + o : (o < 131072 ? Lb2 + (o - 65536) : Lb3 + (o - 131072))); };
    v4u bf[3][4];
#pragma unroll
    for (int t = 0; t < 8; ++t) {
      bf[t >> 2][t & 3] = rdb(t >> 2, t & 3);
      PIN();
    }
    sfor<0, 20>([&](auto ic) {
      constexpr int k = decltype(ic)::v;
      __builtin_amdgcn_s_waitcnt(k < 19 ? 0xC47F : 0xC37F);
#pragma unroll
      for (int t = 0; t < 4; ++t) {
        if (k == 0) mfma_va0(acc[0][t], bf[0][t], a[0][0]);
        else mfma_va(acc[0][t], bf[k % 3][t], a[k][0]);
        if (k < 18) bf[(k + 2) % 3][t] = rdb(k + 2, t);
        else if (t < 3) bf[(k + 2) % 3][t] = rdb(k - 18, 4 + t);
        PIN();
      }
#pragma unroll
      for (int t = 0; t < 4; ++t) {
        if (k == 0) mfma_va0(acc[1][t], bf[0][t], a[0][1]);
        else mfma_va(acc[1][t], bf[k % 3][t], a[k][1]);
        if (t < 2) {
          if constexpr (k + W3 < 20) lda(k + W3, t);
        }
        if (t >= (k + W3 < 20 ? 2 : 0) && t < (k + W3 < 20 ? 4 : 2)) {
          if constexpr (k == 14 || k == 15) sv[k - 14][t & 1] = hld64(ws, l15 * 8 + g * 256 + (t & 1) * 128, O_STAT + (8 * rg + 2 * w + k - 14) * 1024);
          else if constexpr (k >= 16) lb4(40 + 2 * (k - 16) + (t & 1));
        }
        PIN();
      }
      if (k == 0) STAMP(28);
    });
    STAMP(29);
    const unsigned hs = O_H + ((8 * rg + 2 * w) * 56 + 7 * (cg >> 1) + 4 * (cg & 1)) * 1024 + lane * 16;
    unsigned lcs = CSUM_OFF + g * 16;
    KEEP(lcs);
#pragma unroll
    for (int t = 0; t < 4; ++t) csp[t] = *(LAS f4*)(L + lcs + t * 64);
    MFENCE();
    PIN();
    {
      asm volatile("" : "+v"(sv[0][0]), "+v"(sv[0][1]), "+v"(sv[1][0]), "+v"(sv[1][1]));
      const f2 p0 = __builtin_bit_cast(f2, sv[0][0]) + __builtin_bit_cast(f2, sv[0][1]);
      const f2 p1 = __builtin_bit_cast(f2, sv[1][0]) + __builtin_bit_cast(f2, sv[1][1]);
      f2 mr;
      ln_scal(f2{xrow_sum(p0.x), xrow_sum(p1.x)}, f2{xrow_sum(p0.y), xrow_sum(p1.y)}, r, mr, &mean, &den);
    }
#pragma unroll
    for (int i = 0; i < 2; ++i) {
      f2 v[8];
      unsigned o[8];
#pragma unroll
      for (int e = 0; e < 8; ++e) {
        const int t = e >> 1, h = 2 * (e & 1);
        v[e] = f2{acc[i][t][h], acc[i][t][h + 1]} - f2{csp[t][h], csp[t][h + 1]} * mean[i];
      }
      gelu_pack<8>(v, r[i], den[i], o, [&](int m) {
        if (i == 0) lb4(28 + m);
        else if (m == 0) lb4(32);
      });
      hst128(v4u{o[0], o[1], o[2], o[3]}, ws, hs, i * 57344);
      hst128(v4u{o[4], o[5], o[6], o[7]}, ws, hs, i * 57344 + 1024);
    }
    YOUNG();
    STAMP(30);
    sfor<0, 20>([&](auto ic) {
      constexpr int k = decltype(ic)::v;
      __builtin_amdgcn_s_waitcnt(k < 19 ? 0xC37F : 0xC07F);
      if (k == PUBK) {
        WAITV(10);
        hst64(tag, ws, f3pub, 0);
      }
#pragma unroll
      for (int e = 0; e < 6; ++e) {
        const int i = e / 3, t = e % 3;
        if (k == 0) mfma_va0(acc[i][4 + t], bf[2][t], a[0][i]);
        else mfma_va(acc[i][4 + t], bf[(k + 2) % 3][t], a[k][i]);
        if (i == 0 && k < 18) bf[(k + 1) % 3][t] = rdb(k + 2, 4 + t);
        if (k >= 1 && k < 5 && e == 5) lb4(47 + k);
        if (k >= 1 && k < 19 && e == 3) lb4(51 + k);
        PIN();
      }
    });
#define SMPM 0
    v2u fl;
    if (SMPM < 0) fl = hld64(ws, f3wait, 0);
#pragma unroll
    for (int t = 4; t < 7; ++t) csp[t] = *(LAS f4*)(L + lcs + t * 64);
    MFENCE();
    PIN();
    STAMP(37);
    unsigned os[2][2];
#pragma unroll
    for (int i = 0; i < 2; ++i) {
      f2 v[6];
      unsigned o[6];
#pragma unroll
      for (int e = 0; e < 6; ++e) {
        const int t = 4 + (e >> 1), h = 2 * (e & 1);
        v[e] = f2{acc[i][t][h], acc[i][t][h + 1]} - f2{csp[t][h], csp[t][h + 1]} * mean[i];
      }
      gelu_pack<6>(v, r[i], den[i], o, [&](int m) {
        if (i == 0 && m == SMPM) fl = hld64(ws, f3wait, 0);
      });
      hst128(v4u{o[0], o[1], o[2], o[3]}, ws, hs, i * 57344 + 2048);
      os[i][0] = o[4];
      os[i][1] = o[5];
      if (i == 0) {
        PIN();
        KEEP(fl);
        if (!__all(f3wait >= OOB || (fl.x == tag.x && fl.y == tag.y))) poll64(ws, f3wait, tag);
        __builtin_amdgcn_fence(__ATOMIC_ACQUIRE, "wavefront");
        STAMP(32);
#pragma unroll
        for (int n = 0; n < WIN4; ++n) {
          lda4(n);
          PIN();
        }
      }
    }
    {
      const auto s0 = __builtin_amdgcn_permlane16_swap(os[0][0], os[1][0], false, false);
      const auto s1 = __builtin_amdgcn_permlane16_swap(os[0][1], os[1][1], false, false);
      hst128(v4u{s0[0], s1[0], s0[1], s1[1]}, ws, (g & 1) * 57344 + (l15 + 16 * (g >> 1)) * 16,
             O_H + ((8 * rg + 2 * w) * 56 + 7 * (cg >> 1) + 3) * 1024 + (cg & 1) * 512);
    }
    YOUNG();
    STAMP(31);
  }
  {
    v4u gb[2];
    v2u gbo, fl;
    f4 ac[4][5], p[15];
    float gf[20], cf[20];
    unsigned lq = lane;
    KEEP(lq);
    const unsigned lr = RED_OFF + lq * 16, gq4 = lq >> 4;
    unsigned dm = ((64 * tm + 16 * w + (lq & 15)) * D_ + 80 * tn) * 2, ds = dm + 8 * gq4 + ((tn & 1) ? 0 : 128);
    dm += 16 * gq4 + 32 * (tn & 1);
    unsigned xro_off = ((lq & 15) + 16 * (gq4 >> 1)) * 16 + 8 * (gq4 & 1);
    sfor<0, 56>([&](auto ic) {
      constexpr int n = decltype(ic)::v, i = n < 32 ? n >> 3 : (n - 32) / 6, sl = n < 32 ? n & 7 : 8 + (n - 32) % 6;
      if (n == 54) {
        STAMP(35);
        WGBAR();
        STAMP(36);
      }
      if (n == PUBN) {
        WAITV(7);
        hst64(tag, ws, f3pub + (O_F4 - O_F3), 0);
        STAMP(33);
      }
      if (n == 23) asm volatile("" : "+v"(dm), "+v"(ds), "+v"(xro_off));
      if (n == 37) asm volatile("s_barrier" ::: "memory");
      if (n == CHKN) {
        KEEP(fl);
        if (!__all(f3wait >= OOB || (fl.x == tag.x && fl.y == tag.y))) poll64(ws, f3wait + (O_F4 - O_F3), tag);
        __builtin_amdgcn_fence(__ATOMIC_ACQUIRE, "wavefront");
#pragma unroll
        for (int m = 32; m < CHKN + WIN4; ++m) lda4(m);
        PIN();
      }
#pragma unroll
      for (int t = 0; t < 5; ++t) {
        const int f = sl * 5 + t;
        if (sl == 0) mfma_aa0(ac[i][t], b4[f], a4[n % RING4]);
        else if (f >= 23 && f < 48) mfma_va(ac[i][t], b4[f], a4[n % RING4]);
        else mfma_aa(ac[i][t], b4[f], a4[n % RING4]);
        if (t == 1) {
          if constexpr (n + WIN4 < 56) {
            if (n < 32 - WIN4 || n >= CHKN) lda4(n + WIN4);
          }
        }
        if (t == 3) {
          if (n == 30 || n == 31) gb[n & 1] = hld128(ws, gq4 * 16 + (n & 1) * 64, O_MOD + (5 * D_ + 80 * tn + 16 * (tn & 1)) * 2);
          if (n == 32 || n == 33) xr[n & 1] = hld128(ws, lq * 16 + (n & 1) * 1024, O_X + own_m);
          if (n == 34) gbo = hld64(ws, gq4 * 8, O_MOD + (5 * D_ + 80 * tn + ((tn & 1) ? 0 : 64)) * 2);
          if (n == 35) xro = hld64(ws, xro_off, O_X + own_s);
        }
        if constexpr (n >= 44 && n < 54) {
          constexpr int q = n - 44;
          const unsigned gw = q < 8 ? gb[(q >> 2) & 1][q & 3] : gbo[q & 1], xw = q < 8 ? xr[(q >> 2) & 1][q & 3] : xro[q & 1];
          float tmp;
          if (t == 1) asm volatile("v_lshlrev_b32 %0, 16, %2\n\tv_and_b32 %1, 0xffff0000, %2" : "=&v"(gf[2 * q]), "=v"(gf[2 * q + 1]) : "v"(gw));
          if (t == 2) asm volatile("v_lshlrev_b32 %0, 16, %2\n\tv_and_b32 %1, 0xffff0000, %2" : "=&v"(cf[2 * q]), "=v"(cf[2 * q + 1]) : "v"(xw));
          if (t == 3) asm volatile("v_lshlrev_b32 %1, 16, %2\n\tv_fmac_f32 %0, %1, %3" : "+v"(cf[2 * q]), "=&v"(tmp) : "v"(x1p[q]), "v"(gf[2 * q]));
          if (t == 4) asm volatile("v_and_b32 %1, 0xffff0000, %2\n\tv_fmac_f32 %0, %1, %3" : "+v"(cf[2 * q + 1]), "=&v"(tmp) : "v"(x1p[q]), "v"(gf[2 * q + 1]));
        }
        if (n == SMPN && t == 0) fl = hld64(ws, f3wait + (O_F4 - O_F3), 0);
        if (n >= 38 && sl < 11 && (t == 0 || t == 2) && (sl - 8) * 2 + (t == 2) > 0) {
          const int u = (sl - 8) * 2 + (t == 2) - 1;
          *(LAS f4*)(L + (lr + ((w + i) & 3) * 15360) + ((i - 1) * 5 + u) * 1024) = ac[i > 0 ? i - 1 : 0][u];
        }
        if (n == 54) {
          p[t] = *(LAS f4*)(L + (lr + w * 15360) + t * 1024);
          p[5 + t] = *(LAS f4*)(L + (lr + w * 15360) + (5 + t) * 1024);
        }
        if (n == 55) p[10 + t] = *(LAS f4*)(L + (lr + w * 15360) + (10 + t) * 1024);
        PIN();
      }
      if (n == 0) STAMP(34);
    });
    MFENCE();
    PIN();
    unsigned o[10];
#pragma unroll
    for (int t = 0; t < 5; ++t) {
      const f4 y = (ac[3][t] + p[t]) + (p[5 + t] + p[10 + t]);
      const f2 u = f2{y[0], y[1]} * f2{gf[4 * t], gf[4 * t + 1]} + f2{cf[4 * t], cf[4 * t + 1]};
      const f2 v = f2{y[2], y[3]} * f2{gf[4 * t + 2], gf[4 * t + 3]} + f2{cf[4 * t + 2], cf[4 * t + 3]};
      o[2 * t] = pk2(u.x, u.y);
      o[2 * t + 1] = pk2(v.x, v.y);
    }
    const rsrc_t ro = mkrs(gout, SD2);
    __builtin_amdgcn_raw_buffer_store_b128(v4u{o[0], o[1], o[2], o[3]}, ro, dm, 0, 0);
    __builtin_amdgcn_raw_buffer_store_b128(v4u{o[4], o[5], o[6], o[7]}, ro, dm + 64, 0, 0);
    __builtin_amdgcn_raw_buffer_store_b64(v2u{o[8], o[9]}, ro, ds, 0, 0);
  }
  asm volatile(".Lfused_end:" ::: "memory");
}

extern "C" void kernel_launch(void* const* d_in, const int* in_sizes, int n_in, void* d_out, int out_size,
                              void* d_ws, size_t ws_size, hipStream_t stream) {
  (void)hipFuncSetAttribute((const void*)fused, hipFuncAttributeMaxDynamicSharedMemorySize, LDS_BYTES);
  fused<<<256, 256, LDS_BYTES, stream>>>(d_in[0], d_in[1], d_in[2], d_in[3], d_in[4], d_in[5], d_in[7], d_in[8], d_in[9],
                                         d_in[10], d_in[11], d_in[12], d_out, d_ws);
}
```

```cpp
#include <hip/hip_runtime.h>
#include <stdint.h>

typedef unsigned v4u __attribute__((ext_vector_type(4)));
typedef unsigned v2u __attribute__((ext_vector_type(2)));
typedef float f4 __attribute__((ext_vector_type(4)));
typedef float f2 __attribute__((ext_vector_type(2)));
typedef short s8v __attribute__((ext_vector_type(8)));
typedef short s4v __attribute__((ext_vector_type(4)));
typedef __bf16 bf2 __attribute__((ext_vector_type(2)));
typedef __bf16 bf8 __attribute__((ext_vector_type(8)));
typedef __amdgpu_buffer_rsrc_t rsrc_t;
#define LAS __attribute__((address_space(3)))
#define GAS __attribute__((address_space(1)))
#define DI __device__ __forceinline__

static constexpr int S_ = 2048, D_ = 640, F_ = 1728;
static constexpr unsigned OOB = 0x80000000u;

static constexpr unsigned O_EPOCH = 0;
static constexpr unsigned O_FP = 1024;
static constexpr unsigned O_FR = O_FP + 16 * 128;
static constexpr unsigned O_F1 = O_FR + 32 * 128;
static constexpr unsigned O_F2 = O_F1 + 32 * 128;
static constexpr unsigned O_F3 = O_F2 + 16 * 128;
static constexpr unsigned O_F4 = O_F3 + 16 * 512;
static constexpr unsigned O_CNT = O_F4 + 16 * 512;
static constexpr unsigned O_FW = O_CNT + 64 * 128;
static constexpr unsigned O_FB = O_FW + 8192;
static constexpr unsigned O_MOD = 65536;
static constexpr unsigned O_STAT = O_MOD + 16384;
static constexpr unsigned O_KVT = O_STAT + 131072;
static constexpr unsigned SD2 = S_ * D_ * 2;
static constexpr unsigned KVP = 4096 + 128, ZP = 10240 + 128;
static constexpr unsigned O_ZT = O_KVT + D_ * KVP;
static constexpr unsigned O_A1 = O_ZT + 256 * ZP;
static constexpr unsigned O_HP = O_A1 + SD2;
static constexpr unsigned O_X = O_HP + SD2;
static constexpr unsigned O_H1 = O_X + SD2;
static constexpr unsigned O_X1 = O_H1 + SD2;
static constexpr unsigned O_H = O_X1 + SD2;
static constexpr unsigned O_W1 = O_H + 128 * 56 * 1024;
static constexpr unsigned O_W2 = O_W1 + 800 * 1024;
static constexpr unsigned O_W3 = O_W2 + 800 * 1024;
static constexpr unsigned O_W4 = O_W3 + 2240 * 1024;
static constexpr unsigned O_END = O_W4 + 2240 * 1024;
static constexpr unsigned O_DBG = O_END;

static constexpr int CS = 4160;
static constexpr int UOFF = 8 * CS;
static constexpr int WR = 8 * CS + 4608;
static constexpr int LDS_BYTES = 160 * 1024;
static constexpr int RED_OFF = 100 * 1024;
static constexpr int CSUM_OFF = 140 * 1024;
static constexpr unsigned SPIN_CAP = 200000;

template <int V> struct IC { static constexpr int v = V; };
template <int I, int N, class F> DI void sfor(F&& f) {
  if constexpr (I < N) {
    f(IC<I>{});
    sfor<I + 1, N>(f);
  }
}
DI float lo2f(unsigned w) { return __builtin_bit_cast(float, w << 16); }
DI float hi2f(unsigned w) { return __builtin_bit_cast(float, w & 0xffff0000u); }
DI f2 up2(unsigned w) { return f2{lo2f(w), hi2f(w)}; }
DI unsigned pk2(float a, float b) { return __builtin_bit_cast(unsigned, __builtin_convertvector(f2{a, b}, bf2)); }
DI f4 mfma(v4u a, v4u b, f4 c) {
  return __builtin_amdgcn_mfma_f32_16x16x32_bf16(__builtin_bit_cast(bf8, a), __builtin_bit_cast(bf8, b), c, 0, 0, 0);
}
DI void mfma_v(f4& c, v4u a, v4u b) { asm volatile("v_mfma_f32_16x16x32_bf16 %0, %1, %2, %0" : "+v"(c) : "v"(a), "v"(b)); }
DI void mfma_v0(f4& c, v4u a, v4u b) { asm volatile("v_mfma_f32_16x16x32_bf16 %0, %1, %2, 0" : "=&v"(c) : "v"(a), "v"(b)); }
DI void mfma_va(f4& c, v4u a, v4u b) { asm volatile("v_mfma_f32_16x16x32_bf16 %0, %1, %2, %0" : "+v"(c) : "v"(a), "a"(b)); }
DI void mfma_va0(f4& c, v4u a, v4u b) { asm volatile("v_mfma_f32_16x16x32_bf16 %0, %1, %2, 0" : "=&v"(c) : "v"(a), "a"(b)); }
DI void mfma_av(f4& c, v4u a, v4u b) { asm volatile("v_mfma_f32_16x16x32_bf16 %0, %1, %2, %0" : "+v"(c) : "a"(a), "v"(b)); }
DI void mfma_av0(f4& c, v4u a, v4u b) { asm volatile("v_mfma_f32_16x16x32_bf16 %0, %1, %2, 0" : "=&v"(c) : "a"(a), "v"(b)); }
DI void mfma_aa(f4& c, v4u a, v4u b) { asm volatile("v_mfma_f32_16x16x32_bf16 %0, %1, %2, %0" : "+v"(c) : "a"(a), "a"(b)); }
DI void mfma_aa0(f4& c, v4u a, v4u b) { asm volatile("v_mfma_f32_16x16x32_bf16 %0, %1, %2, 0" : "=&v"(c) : "a"(a), "a"(b)); }
#define KEEP(x) asm volatile("" : "+v"(x))
#define MFENCE() asm volatile("s_nop 7\n\ts_nop 1" ::: "memory")
DI float add1(float a, float b) { float r; asm("v_add_f32 %0, %1, %2" : "=v"(r) : "v"(a), "v"(b)); return r; }
DI float fma1(float a, float b, float c) { float r; asm("v_fma_f32 %0, %1, %2, %3" : "=v"(r) : "v"(a), "v"(b), "v"(c)); return r; }
DI rsrc_t mkrs(const void* p, unsigned bytes) { return __builtin_amdgcn_make_buffer_rsrc((void*)p, 0, bytes, 0x00020000); }
#define KAUX 0
#define HDELAY 0
#define INAUX 2
DI v4u ld128(rsrc_t r, unsigned v, unsigned s) { return __builtin_amdgcn_raw_buffer_load_b128(r, v, s, INAUX); }
DI v2u ld64(rsrc_t r, unsigned v, unsigned s) { return __builtin_amdgcn_raw_buffer_load_b64(r, v, s, INAUX); }
DI unsigned ld32(rsrc_t r, unsigned v, unsigned s) { return __builtin_amdgcn_raw_buffer_load_b32(r, v, s, INAUX); }
DI v4u hld128(rsrc_t r, unsigned v, unsigned s) { return __builtin_amdgcn_raw_buffer_load_b128(r, v, s, 16); }
DI v2u hld64(rsrc_t r, unsigned v, unsigned s) { return __builtin_amdgcn_raw_buffer_load_b64(r, v, s, 16); }
DI void hst128(v4u x, rsrc_t r, unsigned v, unsigned s) { __builtin_amdgcn_raw_buffer_store_b128(x, r, v + s, 0, 16); }
DI void hst64(v2u x, rsrc_t r, unsigned v, unsigned s) { __builtin_amdgcn_raw_buffer_store_b64(x, r, v, s, 16); }
DI void hst32(unsigned x, rsrc_t r, unsigned v, unsigned s) { __builtin_amdgcn_raw_buffer_store_b32(x, r, v, s, 16); }
#define DRAIN() do { asm volatile("s_waitcnt vmcnt(0)" ::: "memory"); __builtin_amdgcn_s_waitcnt(0x0F70); } while (0)
#define WGBAR() asm volatile("s_waitcnt lgkmcnt(0)\n\ts_barrier" ::: "memory")
#define PIN() __builtin_amdgcn_sched_barrier(0)

template <int CTRL> DI float dpp(float x) {
  return __builtin_bit_cast(float, __builtin_amdgcn_mov_dpp(__builtin_bit_cast(int, x), CTRL, 0xf, 0xf, true));
}
DI float xrow_sum(float x) {
  auto s = __builtin_amdgcn_permlane16_swap(__builtin_bit_cast(unsigned, x), __builtin_bit_cast(unsigned, x), false, false);
  x = __builtin_bit_cast(float, (unsigned)s[0]) + __builtin_bit_cast(float, (unsigned)s[1]);
  auto t = __builtin_amdgcn_permlane32_swap(__builtin_bit_cast(unsigned, x), __builtin_bit_cast(unsigned, x), false, false);
  return __builtin_bit_cast(float, (unsigned)t[0]) + __builtin_bit_cast(float, (unsigned)t[1]);
}
DI float swap32_add(float a, float b) {
  auto t = __builtin_amdgcn_permlane32_swap(__builtin_bit_cast(unsigned, a), __builtin_bit_cast(unsigned, b), false, false);
  return __builtin_bit_cast(float, (unsigned)t[0]) + __builtin_bit_cast(float, (unsigned)t[1]);
}
DI float swap16_add(float a, float b) {
  auto t = __builtin_amdgcn_permlane16_swap(__builtin_bit_cast(unsigned, a), __builtin_bit_cast(unsigned, b), false, false);
  return __builtin_bit_cast(float, (unsigned)t[0]) + __builtin_bit_cast(float, (unsigned)t[1]);
}
DI float rdl(float v, int l) { return __builtin_bit_cast(float, __builtin_amdgcn_readlane(__builtin_bit_cast(int, v), l)); }
DI void wave_sum4(f2& s, f2& ss) {
  float v = swap16_add(swap32_add(s.x, s.y), swap32_add(ss.x, ss.y));
  v += dpp<0xB1>(v);
  v += dpp<0x4E>(v);
  v += dpp<0x141>(v);
  v += dpp<0x128>(v);
  s = f2{rdl(v, 0), rdl(v, 32)};
  ss = f2{rdl(v, 16), rdl(v, 48)};
}
DI void poll64(rsrc_t ws, unsigned voff, v2u want) {
  const bool idle = voff >= OOB;
  for (unsigned spin = 0; spin < SPIN_CAP; ++spin) {
    asm volatile("" ::: "memory");
    const v2u v = hld64(ws, voff, 0);
    if (__all(idle || (v.x == want.x && v.y == want.y))) break;
  }
  __builtin_amdgcn_fence(__ATOMIC_ACQUIRE, "wavefront");
}

DI bool wflags_ok(v4u a, int lane, int nl, v2u want) {
  return __all(lane >= nl || (a.x == want.x && a.y == want.y && a.z == want.x && a.w == want.y));
}
DI void pollw(rsrc_t ws, unsigned voff, int lane, int nl, v2u want) {
  for (unsigned spin = 0; spin < SPIN_CAP; ++spin) {
    asm volatile("" ::: "memory");
    if (wflags_ok(hld128(ws, voff, 0), lane, nl, want)) break;
    __builtin_amdgcn_s_sleep(4);
  }
  __builtin_amdgcn_fence(__ATOMIC_ACQUIRE, "wavefront");
}
DI float rbf(float x) { return lo2f(pk2(x, 0.f)); }
DI void ln_scal(f2 s, f2 ss, f2& r, f2& mr, f2* pmean = nullptr, f2* pden = nullptr) {
  const f2 m = s * (1.f / D_);
  const f2 var = ss * (1.f / D_) - m * m;
  const f2 v = f2{fmaxf(var.x, 0.f), fmaxf(var.y, 0.f)} + 1e-5f;
  r = f2{__builtin_amdgcn_rsqf(v.x), __builtin_amdgcn_rsqf(v.y)};
  mr = -m * r;
  if (pmean) *pmean = m;
  if (pden) *pden = v * r;
}
template <int N, class Mid> DI void gelu_pack(const f2 (&x)[N], float r, float den, unsigned* o, Mid mid) {
  const float c0 = -2.f * 0.7978845608028654f * 1.4426950408889634f * r, c1 = c0 * 0.044715f * r * r;
  f2 a[N];
#pragma unroll
  for (int i = 0; i < N; ++i) a[i] = x[i] * x[i] * c1 + c0;
  mid(0);
  PIN();
#pragma unroll
  for (int i = 0; i < N; ++i) a[i] *= x[i];
  mid(1);
  PIN();
#pragma unroll
  for (int i = 0; i < N; ++i) a[i] = f2{__builtin_amdgcn_exp2f(a[i].x), __builtin_amdgcn_exp2f(a[i].y)};
  mid(2);
  PIN();
#pragma unroll
  for (int i = 0; i < N; ++i) a[i] = a[i] * den + den;
  PIN();
#pragma unroll
  for (int i = 0; i < N; ++i) a[i] = f2{__builtin_amdgcn_rcpf(a[i].x), __builtin_amdgcn_rcpf(a[i].y)};
  mid(3);
  PIN();
#pragma unroll
  for (int i = 0; i < N; ++i) a[i] *= x[i];
  PIN();
#pragma unroll
  for (int i = 0; i < N; ++i) o[i] = pk2(a[i].x, a[i].y);
}

#define STAMP(i)
#define STARG
#define STPASS(n)

static constexpr int RT_BYTES = 32 * 896;
template <bool WIDE> struct Job {
  static constexpr int NL = WIDE ? 7 : 5, RB = WIDE ? 896 : 640, PITCH = WIDE ? F_ * 2 : D_ * 2;
  v4u r[NL];
  unsigned dst;
  DI void load(int job, int w, const unsigned (&vo)[NL], const void* w1, const void* w2, const void* wx) {
    const bool big = WIDE || job >= 80;
    const int jj = WIDE ? job : (job >= 80 ? job - 80 : (job >= 40 ? job - 40 : job));
    const int gq = WIDE ? jj & 3 : jj & 1, ks = WIDE ? jj >> 2 : jj >> 1;
    const rsrc_t rs = mkrs(big ? wx : (job >= 40 ? w2 : w1), big ? D_ * F_ * 2 : D_ * D_ * 2);
    const unsigned src = (32 * ks + 8 * w) * PITCH + gq * RB;
    dst = (WIDE ? O_W3 : (job >= 80 ? O_W4 : (job >= 40 ? O_W2 : O_W1))) + ((gq * 4 + w) * (!WIDE && job >= 80 ? 56 : 20) + ks) * (NL * 1024);
#pragma unroll
    for (int i = 0; i < NL; ++i)
      r[i] = ld128(rs, (WIDE && gq == 3 && (vo[i] & 1)) ? OOB : (vo[i] & ~1u) + src, 0);
  }
  DI void put(int w, int lane, LAS char* L) {
#pragma unroll
    for (int i = 0; i < NL; ++i) *(LAS v4u*)(L + w * (8 * RB) + lane * 16 + i * 1024) = r[i];
  }
  DI void emit(int w, int lane, LAS char* L, rsrc_t ws) {
    const int g = lane >> 4, q = (lane & 15) >> 2, p = lane & 3;
    LAS char* row = L + (8 * g + q) * RB + w * (RB / 4);
    LAS char* pr = row + 32 * (w & 1) + 16 * p;
    LAS char* sg = row + ((w & 1) ? 0 : RB / 4 - 32) + 8 * p;
    v2u lo[NL], hi[NL];
#pragma unroll
    for (int f = 0; f < NL; ++f) {
      LAS char* c = f < NL - 1 ? pr + 64 * (f >> 1) + 8 * (f & 1) : sg;
      lo[f] = __builtin_bit_cast(v2u, __builtin_amdgcn_ds_read_tr16_b64_v4i16((LAS s4v*)c));
      hi[f] = __builtin_bit_cast(v2u, __builtin_amdgcn_ds_read_tr16_b64_v4i16((LAS s4v*)(c + 4 * RB)));
    }
#pragma unroll
    for (int f = 0; f < NL; ++f) hst128(v4u{lo[f].x, lo[f].y, hi[f].x, hi[f].y}, ws, lane * 16 + dst, f * 1024);
  }
};

template <class Extra, class Gap>
DI void gemm_ks(rsrc_t ws, unsigned abase, const unsigned (&vo)[4], const v4u* b, LAS char* L, int w, int lane, f4 (&acc)[5],
                Extra extra, Gap gap STARG) {
#define WIN12 12
  constexpr int KS = 5, WIN = WIN12, NA = 4 * KS, RING = WIN + 1;
  v4u a[RING];
  unsigned so[4];
#pragma unroll
  for (int i = 0; i < 4; ++i) so[i] = abase + ((w + 1 + i) & 3) * 20480;
  auto lda = [&](int n) { a[n % RING] = hld128(ws, vo[n / KS] + ((n % KS) & 3) * 1024, so[n / KS] + ((n % KS) >> 2) * 4096); };
#pragma unroll
  for (int n = 0; n < WIN; ++n) {
    lda(n);
    PIN();
  }
  f4 ac[2][5], p[15];
  unsigned lr = RED_OFF + lane * 16;
  KEEP(lr);
  sfor<0, NA>([&](auto ic) {
    constexpr int n = decltype(ic)::v, i = n / KS, k = n % KS;
    if (n == NA - 3) {
      STAMP(sb + 1);
      WGBAR();
      STAMP(sb + 2);
    }
#pragma unroll
    for (int t = 0; t < 5; ++t) {
      if (k == 0) mfma_av0(ac[i & 1][t], b[t], a[n % RING]);
      else mfma_av(ac[i & 1][t], b[k * 5 + t], a[n % RING]);
      if (t == 1) {
        if constexpr (n + WIN < NA) lda(n + WIN);
      }
      if (t == (n + WIN < NA ? 4 : 1) && n >= NA - 12) extra(n - (NA - 12));
      if (k == 0 && i > 0 && t > 0) *(LAS f4*)(L + (lr + ((w + i) & 3) * 15360) + ((i - 1) * 5 + t - 1) * 1024) = ac[(i - 1) & 1][t - 1];
      if (k == 1 && i > 0 && t == 0) *(LAS f4*)(L + (lr + ((w + i) & 3) * 15360) + ((i - 1) * 5 + 4) * 1024) = ac[(i - 1) & 1][4];
      if (n == NA - 3) {
        p[t] = *(LAS f4*)(L + (lr + w * 15360) + t * 1024);
        p[5 + t] = *(LAS f4*)(L + (lr + w * 15360) + (5 + t) * 1024);
      }
      if (n == NA - 2) p[10 + t] = *(LAS f4*)(L + (lr + w * 15360) + (10 + t) * 1024);
      if (n == NA - 1) {
        p[t][0] = add1(p[t][0], p[5 + t][0]);
        p[t][1] = add1(p[t][1], p[5 + t][1]);
        KEEP(p[t]);
      }
      gap(n, t);
      PIN();
    }
    if (n == 0) STAMP(sb);
  });
#pragma unroll
  for (int t = 0; t < 5; ++t) {
    p[t][2] += p[5 + t][2];
    p[t][3] += p[5 + t][3];
    p[t] += p[10 + t];
    KEEP(p[t]);
  }
  PIN();
#pragma unroll
  for (int t = 0; t < 5; ++t) acc[t] = ac[1][t] + p[t];
}

#define YOUNG() do { asm volatile("; YOUNG" ::: "memory"); PIN(); } while (0)
#define WAITV(n) asm volatile("s_waitcnt vmcnt(" #n ") ; WAITV" ::: "memory")
__global__ __launch_bounds__(256, 1) void fused(const void*, const void*, const void*, const void*, const void*, const void*,
                                                const void*, const void*, const void*, const void*, const void*, const void*,
                                                void*, void*) {
  extern __shared__ __attribute__((aligned(16))) char smem[];
  LAS char* L = (LAS char*)smem;
  STAMP(0);
  const unsigned tstart = (unsigned)__builtin_amdgcn_s_memrealtime();
  unsigned pf = 0;
  if (blockIdx.x >= 160) {
    unsigned long long pc;
    unsigned off = (((blockIdx.x >> 3) & 1) * 256 + threadIdx.x) * 128;
    asm volatile("s_getpc_b64 %1\n.Lfused_pc:\n\tv_min_u32 %2, .Lfused_end-.Lfused_pc, %2\n\tglobal_load_dword %0, %2, %1"
                 : "=&v"(pf), "=&s"(pc), "+v"(off));
  }
  PIN();
  typedef void* const __attribute__((address_space(4))) * karg_t;
  const karg_t ka = (karg_t)__builtin_amdgcn_kernarg_segment_ptr();
  void *gq = ka[0], *gk = ka[1], *gv = ka[2], *gsk = ka[3], *gssm = ka[4], *gD = ka[5], *gw1 = ka[6], *gw2 = ka[7], *gw3 = ka[8],
       *gw4 = ka[9], *gc = ka[10], *gwm = ka[11], *gout = ka[12], *gws = ka[13];
  asm volatile("" ::"s"(gq), "s"(gk), "s"(gv), "s"(gsk), "s"(gssm), "s"(gD), "s"(gw1), "s"(gw2), "s"(gw3), "s"(gw4),
               "s"(gc), "s"(gwm), "s"(gout), "s"(gws));
  const int tid = threadIdx.x, lane = tid & 63;
  const int w = __builtin_amdgcn_readfirstlane(tid >> 6);
  const int b = blockIdx.x, x = b & 7, j = b >> 3;
  const int tm = 4 * x + (j >> 3), tn = j & 7;
  const int rg = 2 * x + (j >> 4), cg = j & 15;
  const bool conv_wg = j < 20;
  const rsrc_t ws = mkrs(gws, O_END);
  const int g = lane >> 4, l15 = lane & 15;

  const int rt0 = 64 * tm + 8 * tn + 2 * w;
  const int rcm = 8 * lane, rco = 512 + 2 * lane;
  v4u qm[2];
  unsigned qo[2];
  auto load_q = [&]() {
    const rsrc_t rq = mkrs(gq, SD2);
#pragma unroll
    for (int t = 0; t < 2; ++t) {
      qm[t] = ld128(rq, rcm * 2, (rt0 + t) * D_ * 2);
      qo[t] = ld32(rq, rco * 2, (rt0 + t) * D_ * 2);
    }
  };
  v2u tag;
  auto make_tag = [&](v2u ep) {
    tag.x = __builtin_amdgcn_readfirstlane(ep.x) * 0x9E3779B1u + 0x7F4A7C15u;
    tag.y = (__builtin_amdgcn_readfirstlane(ep.y) ^ tag.x) * 0x85EBCA77u + 0x2545F491u;
    tag.x &= 0xffffff00u;
  };
  v4u b1[25], b2[25];
  const unsigned sb1 = O_W1 + (tn * 100 + w * 25) * 1024, sb2 = O_W2 + (tn * 100 + w * 25) * 1024;
  auto zero_h1 = [&](int i) {
    hst128(v4u{0u, 0u, 0u, 0u}, ws, (i < 2 || tid < 128) ? tid * 16 : OOB, O_H1 + b * 10240 + i * 4096);
  };
  v4u mm[5];
  unsigned mo[5];
  auto load_mod = [&](bool ok) {
    unsigned o1 = (ok ? 0u : OOB) | (lane * 16), o2 = (ok ? 0u : OOB) | (lane * 4 + 1024);
    asm volatile("" : "+v"(o1), "+v"(o2));
#pragma unroll
    for (int i = 0; i < 5; ++i) {
      mm[i] = hld128(ws, o1, O_MOD + i * D_ * 2);
      mo[i] = __builtin_amdgcn_raw_buffer_load_b32(ws, o2, O_MOD + i * D_ * 2, 16);
    }
  };
  f2 qv[10], n1[10];
  auto ln1 = [&]() {
#pragma unroll
    for (int i = 0; i < 4; ++i) {
      qv[2 * i] = f2{lo2f(qm[0][i]), lo2f(qm[1][i])};
      qv[2 * i + 1] = f2{hi2f(qm[0][i]), hi2f(qm[1][i])};
    }
    qv[8] = f2{lo2f(qo[0]), lo2f(qo[1])};
    qv[9] = f2{hi2f(qo[0]), hi2f(qo[1])};
    f2 s = qv[0], ss = qv[0] * qv[0];
#pragma unroll
    for (int i = 1; i < 10; ++i) { s += qv[i]; ss += qv[i] * qv[i]; }
    wave_sum4(s, ss);
    f2 r, mr;
    ln_scal(s, ss, r, mr);
#pragma unroll
    for (int i = 0; i < 10; ++i) n1[i] = qv[i] * r + mr;
  };
  unsigned z3off, im0, io0, roff, roff3, own_r, x1m, x1s, stoff, h1m, h1s, f1pub, f1wait, frpub, frwait, f2pub, f2wait, f3pub, f3wait;
  unsigned v2o[4];
  const unsigned own_m = ((4 * tm + w) * 20 + 5 * (tn >> 1) + 3 * (tn & 1)) * 1024;
  const unsigned own_s = ((4 * tm + w) * 20 + 5 * (tn >> 1) + 2) * 1024 + (tn & 1) * 512;
  auto offsets = [&]() {
    z3off = tid < 128 ? tid * 16 + 8192 : OOB;
    im0 = RED_OFF + 16384 + lane * 128 + (((2 * w) ^ (lane & 7)) << 4);
    io0 = RED_OFF + 16384 + (64 + (lane >> 2)) * 128 + (((2 * w) ^ ((lane >> 2) & 7)) << 4) + 4 * (lane & 3);
    roff = O_A1 + ((4 * tm + (tn >> 1)) * 20 + (tid >> 5)) * 1024 + (((tid ^ (tid >> 3)) & 7) + 8 * (tn & 1) + 16 * ((tid >> 3) & 3)) * 16;
    roff3 = tid < 128 ? roff : OOB;
    own_r = (l15 + 16 * (g >> 1)) * 16 + 8 * (g & 1);
    x1m = O_X1 + own_m + lane * 16;
    x1s = (g & 1) ? OOB + SD2 : O_X1 + own_s + (l15 + 16 * (g >> 1)) * 16;
    stoff = (g & 1) ? OOB : O_STAT + ((4 * tm + w) * 8 + tn) * 128 + l15 * 8 + (g >> 1) * 4;
    f3pub = lane == 0 ? O_F3 + rg * 512 + (w >> 1) * 256 + cg * 16 + (w & 1) * 8 : OOB;
    f3wait = lane < 8 ? O_F3 + rg * 512 + (tm & 1) * 256 + w * 64 + lane * 8 : OOB;
    const int sh = (tn >> 1) == 0 ? 1 : ((tn >> 1) == 1 ? -1 : ((tn >> 1) == 2 ? 2 : -2));
    const int t2 = 64 * tm + 16 * w + l15 + sh;
    const bool ok = t2 >= 0 && t2 < S_;
    const unsigned d = O_H1 + ((t2 >> 4) * 20 + 5 * (tn >> 1)) * 1024 + (t2 & 15) * 16;
    h1m = ok ? d + 3072 * (tn & 1) + 256 * g : OOB;
    h1s = (ok && !(g & 1)) ? d + 2048 + 512 * (tn & 1) + 256 * (g >> 1) : OOB;
    const int nb = tm + (sh > 0 ? 1 : -1);
    f1pub = OOB;
    if (lane == 0) f1pub = O_F1 + tm * 128 + tn * 8;
    if (lane == 1 && nb >= 0 && nb < 32) f1pub = O_F1 + nb * 128 + 64 + tn * 8;
    const bool up = ((lane >> 1) & 1) == 0;
    f1wait = (lane < 8 || (lane < 16 && (up ? tm > 0 : tm < 31))) ? O_F1 + tm * 128 + lane * 8 : OOB;
    frpub = lane == 0 ? O_FR + tm * 128 + tn * 8 : OOB;
    frwait = lane < 8 ? O_FR + tm * 128 + lane * 8 : OOB;
    f2pub = lane == 0 ? O_F2 + (tm >> 1) * 128 + ((tm & 1) * 8 + tn) * 8 : OOB;
    f2wait = lane < 16 ? O_F2 + rg * 128 + lane * 8 : OOB;
    asm volatile("" : "+v"(frpub), "+v"(frwait), "+v"(f2pub), "+v"(f2wait));
    asm volatile("" : "+v"(z3off), "+v"(im0), "+v"(io0), "+v"(roff), "+v"(roff3), "+v"(own_r), "+v"(f1pub), "+v"(f1wait));
    asm volatile("" : "+v"(x1m), "+v"(x1s), "+v"(stoff), "+v"(f3pub), "+v"(f3wait));
    asm volatile("" : "+v"(h1m), "+v"(h1s));
  };
  constexpr int NB2 = 19;
  hst32(tstart, ws, tid == 0 ? O_MOD + 12288 + b * 4 : OOB, 0);
  bool wok = false, bok = false;
  if (conv_wg) {
    const int cgp = j < 16 ? x : 8 + (x >> 2), ci = j < 16 ? j : (x & 3) * 4 + (j - 16);
    const int pT0 = 128 * ci + 32 * w + 8 * ((lane >> 3) & 3) + 4 * (lane >> 5), pC0 = 64 * cgp + 8 * (lane & 7);
    const int ch = 64 * cgp + 4 * ci + w;
    v4u kw[7], vw[4], skw[4], kr[4];
    {
      const rsrc_t rk = mkrs(gk, SD2), rv = mkrs(gv, SD2), rs = mkrs(gsk, D_ * 8);
      const unsigned o = (unsigned)(pT0 * D_ + pC0) * 2;
#pragma unroll
      for (int i = 0; i < 7; ++i) kw[i] = __builtin_amdgcn_raw_buffer_load_b128(rk, o + (i - 3) * D_ * 2, 0, KAUX);
#pragma unroll
      for (int i = 0; i < 4; ++i) vw[i] = ld128(rv, o + i * D_ * 2, 0);
#pragma unroll
      for (int i = 0; i < 4; ++i) skw[i] = __builtin_amdgcn_raw_buffer_load_b128(rs, pC0 * 8 + i * 16, 0, 0);
      PIN();
      offsets();
      PIN();
      asm volatile("s_waitcnt vmcnt(4)" ::: "memory");
      const rsrc_t rK = mkrs((const char*)gssm + (size_t)ch * 4096, 4096);
#pragma unroll
      for (int i = 0; i < 4; ++i) kr[i] = ld128(rK, lane * 16 + i * 1024, 0);
    }
    const v2u ep = hld64(ws, lane == 0 ? O_EPOCH : O_FP + x * 256 + (j >= 16) * 128, 0);
    const unsigned dvb = __builtin_amdgcn_raw_buffer_load_b16(mkrs(gD, D_ * 2), ch * 2, 0, 0);
    PIN();
    {
      unsigned pe[8][2];
#pragma unroll
      for (int pr = 0; pr < 4; ++pr) {
        const f2 w0 = {lo2f(skw[pr].x), lo2f(skw[pr].z)}, w1 = {hi2f(skw[pr].x), hi2f(skw[pr].z)};
        const f2 w2 = {lo2f(skw[pr].y), lo2f(skw[pr].w)}, w3 = {hi2f(skw[pr].y), hi2f(skw[pr].w)};
        f2 kf[7], o[4];
#pragma unroll
        for (int i = 0; i < 7; ++i) kf[i] = up2(kw[i][pr]);
#pragma unroll
        for (int i = 0; i < 4; ++i) o[i] = (kf[i + 3] * w0 + kf[i + 2] * w1 + kf[i + 1] * w2 + kf[i] * w3) * up2(vw[i][pr]);
        pe[2 * pr][0] = pk2(o[3].x, o[2].x);
        pe[2 * pr][1] = pk2(o[1].x, o[0].x);
        pe[2 * pr + 1][0] = pk2(o[3].y, o[2].y);
        pe[2 * pr + 1][1] = pk2(o[1].y, o[0].y);
      }
      const unsigned so = (pC0 + 4 * (lane >> 5)) * KVP + (pT0 & ~7) * 2;
#pragma unroll
      for (int e = 0; e < 4; ++e) {
        const auto s0 = __builtin_amdgcn_permlane32_swap(pe[e][0], pe[e + 4][0], false, false);
        const auto s1 = __builtin_amdgcn_permlane32_swap(pe[e][1], pe[e + 4][1], false, false);
        hst128(v4u{s0[1], s1[1], s0[0], s1[0]}, ws, so, O_KVT + e * KVP);
      }
    }
    STAMP(1);
    PIN();
    LAS char* T = L + w * WR;
    if (lane < 8) *(LAS v4u*)(T + lane * CS) = v4u{0u, 0u, 0u, 0u};
#pragma unroll
    for (int i = 0; i < 4; ++i) *(LAS v4u*)(T + 7 * CS + (i * 64 + lane + 1) * 16) = kr[i];
    asm volatile("s_waitcnt lgkmcnt(0)" ::: "memory");
    v4u kl[4];
#pragma unroll
    for (int i = 0; i < 4; ++i) kl[i] = *(LAS v4u*)(T + 7 * CS + (i * 64 + lane) * 16);
    auto tbuild = [&](int i) {
      const unsigned d[8] = {kl[i].x, kl[i].y, kl[i].z, kl[i].w, kr[i].x, kr[i].y, kr[i].z, kr[i].w};
      unsigned a[7];
#pragma unroll
      for (int e = 0; e < 7; ++e) a[e] = __builtin_amdgcn_alignbit(d[e + 1], d[e], 16);
#pragma unroll
      for (int m = 0; m < 7; ++m) {
        const int h = (m + 1) >> 1;
        const v4u o = (m & 1) ? v4u{d[h], d[h + 1], d[h + 2], d[h + 3]} : v4u{a[h], a[h + 1], a[h + 2], a[h + 3]};
        *(LAS v4u*)(T + m * CS + (i * 64 + lane + 1) * 16) = o;
      }
    };
    tbuild(0);
    make_tag(ep);
    DRAIN();
    asm volatile("s_barrier" ::: "memory");
    if (w == 0) {
      unsigned o = OOB;
      if (j < 16 && lane == 0) o = O_FP + x * 256 + ci * 8;
      if (j >= 16 && lane < 4) o = O_FP + ((x & 4) + lane) * 256 + 128 + ci * 8;
      hst64(tag, ws, o, 0);
    }
    STAMP(2);
    tbuild(1);
    tbuild(2);
    tbuild(3);
    if (lane < 32) *(LAS v4u*)(T + UOFF + lane * 16) = v4u{0u, 0u, 0u, 0u};
    STAMP(3);
    if (w == 0) poll64(ws, lane < 16 ? O_FP + x * 256 + (j >= 16) * 128 + lane * 8 : OOB, tag);
    WGBAR();
    STAMP(4);
    const v4u wf = hld128(ws, O_FW + x * 1024 + lane * 16, 0);
    v4u ur[4];
#pragma unroll
    for (int i = 0; i < 4; ++i) ur[i] = hld128(ws, lane * 16 + i * 1024, O_KVT + ch * KVP);
    const LAS char* pa = T + (l15 & 7) * CS + (3 - g + (l15 >> 3)) * 16;
    const LAS char* pv = T + UOFF + 512 + (l15 - 1) * 32 + g * 16;
    v4u fa[2][8], fv[2][8];
#pragma unroll
    for (int a = 0; a < 8; ++a) fa[0][a] = *(const LAS v4u*)(pa + 8 * a * 64);
    f4 acc[8];
    v4u ad;
    {
      const int jd = 8 * g - 9 - l15;
#pragma unroll
      for (int e = 0; e < 4; ++e) ad[e] = jd == 2 * e ? dvb : (jd == 2 * e + 1 ? dvb << 16 : 0u);
      asm volatile("" : "+v"(ad));
    }
    PIN();
#pragma unroll
    for (int i = 0; i < 4; ++i) *(LAS v4u*)(T + UOFF + 512 + i * 1024 + lane * 16) = ur[i];
    asm volatile("s_waitcnt lgkmcnt(0)" ::: "memory");
#pragma unroll
    for (int a = 0; a < 8; ++a) fv[0][a] = *(const LAS v4u*)(pv + a * 512);
    wok = wflags_ok(wf, lane, 40, tag);
    __builtin_amdgcn_fence(__ATOMIC_ACQUIRE, "wavefront");
    const unsigned boff = wok ? lane * 16 : OOB;
    STAMP(5);
#pragma unroll
    for (int q = 0; q < 8; ++q) mfma_v0(acc[q], ad, fv[0][q]);
    PIN();
#pragma unroll
    for (int g0 = 0; g0 < 8; ++g0) {
      int n = 0;
      if (g0 == 0) load_q();
#pragma unroll
      for (int a = 0; a < 8; ++a)
#pragma unroll
        for (int q = a; q < 8; ++q) {
          mfma_v(acc[q], fa[g0 & 1][a], fv[g0 & 1][q - a]);
          ++n;
          if ((n & 1) == 0) {
            const int e = n / 2 - 1;
            if (g0 < 7 && e < 16) {
              if (e < 8) fv[(g0 + 1) & 1][e] = *(const LAS v4u*)(pv + e * 512 - (g0 + 1) * 64);
              else fa[(g0 + 1) & 1][e - 8] = *(const LAS v4u*)(pa + (g0 + 1 + 8 * (e - 8)) * 64);
            }
            PIN();
          }
          if (n % 5 == 0) {
            const int i = g0 * 7 + n / 5 - 13;
            if (i < -9) zero_h1(i + 12);
            if (i >= 0 && i < 25) b1[i] = hld128(ws, boff + (i & 3) * 1024, sb1 + (i >> 2) * 4096);
            if (i >= 25 && i < 50) b2[i - 25] = hld128(ws, boff + ((i - 25) & 3) * 1024, sb2 + ((i - 25) >> 2) * 4096);
            PIN();
          }
        }
    }
    const v4u bfl = hld128(ws, O_FB + x * 1024 + lane * 16, 0);
    MFENCE();
    PIN();
    STAMP(6);
#pragma unroll
    for (int q = 0; q < 8; q += 2) {
      const auto s0 = __builtin_amdgcn_permlane16_swap(pk2(acc[q][0], acc[q][1]), pk2(acc[q + 1][0], acc[q + 1][1]), false, false);
      const auto s1 = __builtin_amdgcn_permlane16_swap(pk2(acc[q][2], acc[q][3]), pk2(acc[q + 1][2], acc[q + 1][3]), false, false);
      *(LAS v4u*)(T + 64 * w + (32 * (q + (g & 1)) + 2 * l15 + (g >> 1)) * 16) = v4u{s0[0], s1[0], s0[1], s1[1]};
    }
    WGBAR();
    {
      v4u zo[4];
#pragma unroll
      for (int i = 0; i < 4; ++i) zo[i] = *(LAS v4u*)(L + (lane & 3) * (WR + 64) + (64 * w + 16 * i + (lane >> 2)) * 16);
#pragma unroll
      for (int i = 0; i < 4; ++i) hst128(zo[i], ws, (lane >> 2) * ZP + (lane & 3) * 16, O_ZT + (64 * w + 16 * i) * ZP + (ch - w) * 16);
    }
    YOUNG();
    bok = wflags_ok(bfl, lane, 48, tag);
    __builtin_amdgcn_fence(__ATOMIC_ACQUIRE, "wavefront");
    load_mod(bok);
    PIN();
    ln1();
    PIN();
    WAITV(10);
  } else {
    const int hb = (j - 20) * 8 + x;
    unsigned vn[5];
#pragma unroll
    for (int i = 0; i < 5; ++i) vn[i] = (i * 64 + lane) * 16 + (((i * 64 + lane) * 1639u) >> 16) * 640;
    Job<false> j0, j1;
    if (HDELAY) __builtin_amdgcn_s_sleep(HDELAY);
    j0.load(hb, w, vn, gw1, gw2, gw4);
    const v2u ep = hld64(ws, O_EPOCH, 0);
    const v2u cw = __builtin_amdgcn_raw_buffer_load_b64(mkrs(gc, D_ * 2), tid * 8, 0, 0);
    PIN();
    unsigned vw[7];
#pragma unroll
    for (int i = 0; i < 7; ++i) {
      const unsigned c = i * 64 + lane, row = (c * 1171u) >> 16;
      vw[i] = c * 16 + row * 2560 + (c - row * 56 >= 48);
      KEEP(vw[i]);
    }
    offsets();
    LAS float* sc = (LAS float*)(L + 4 * RT_BYTES);
    {
      const float c4[4] = {lo2f(cw.x), hi2f(cw.x), lo2f(cw.y), hi2f(cw.y)};
      f4 sv;
#pragma unroll
      for (int e = 0; e < 4; ++e) sv[e] = rbf(c4[e] * __builtin_amdgcn_rcpf(1.f + __builtin_amdgcn_exp2f(-1.4426950408889634f * c4[e])));
      *(LAS f4*)(sc + tid * 4) = sv;
    }
    j0.put(w, lane, L);
    make_tag(ep);
    WGBAR();
    j0.emit(w, lane, L, ws);
    if (j == 20 && w == 0) hst128(v4u{tag.x, tag.y, 0u, 0u}, ws, (lane >> 3) * 1024 + (lane & 7) * 16, O_CNT + x * 128);
    asm volatile("" ::"v"(pf) : "memory");
    STAMP(1);
    auto first_pub = [&]() {
      WGBAR();
      if (w == 0) hst64(tag, ws, lane < 8 ? O_FW + lane * 1024 + hb * 8 : OOB, 0);
      STAMP(2);
    };
    if (hb < 60) {
      const rsrc_t rm = mkrs(gwm, D_ * 6 * D_ * 2);
      v4u wm[20];
      YOUNG();
#pragma unroll
      for (int i = 0; i < 8; ++i) wm[i] = ld128(rm, (tid >> 3) * (6 * D_ * 2) + (tid & 7) * 16, hb * 128 + i * 32 * (6 * D_ * 2));
      PIN();
      WAITV(8);
      first_pub();
#pragma unroll
      for (int i = 8; i < 20; ++i) wm[i] = ld128(rm, (tid >> 3) * (6 * D_ * 2) + (tid & 7) * 16, hb * 128 + i * 32 * (6 * D_ * 2));
      j1.load(132 + hb, w, vn, gw1, gw2, gw4);
      load_q();
      float sl[20];
#pragma unroll
      for (int i = 0; i < 20; ++i) sl[i] = sc[(tid >> 3) + 32 * i];
      PIN();
      f2 a[4] = {f2{0.f, 0.f}, f2{0.f, 0.f}, f2{0.f, 0.f}, f2{0.f, 0.f}};
#pragma unroll
      for (int i = 0; i < 20; ++i)
#pragma unroll
        for (int e = 0; e < 4; ++e) a[e] += up2(wm[i][e]) * sl[i];
      LAS char* mred = L + 4 * RT_BYTES + 4096;
      *(LAS f4*)(mred + (tid >> 3) * 272 + (tid & 7) * 32) = f4{a[0].x, a[0].y, a[1].x, a[1].y};
      *(LAS f4*)(mred + (tid >> 3) * 272 + (tid & 7) * 32 + 16) = f4{a[2].x, a[2].y, a[3].x, a[3].y};
      WGBAR();
      {
        const int col = tid >> 2, part = tid & 3;
        float v[8];
#pragma unroll
        for (int r = 0; r < 8; ++r) v[r] = *(LAS float*)(mred + (part * 8 + r) * 272 + col * 4);
        float s = ((v[0] + v[1]) + (v[2] + v[3])) + ((v[4] + v[5]) + (v[6] + v[7]));
        s += dpp<0xB1>(s);
        s += dpp<0x4E>(s);
        s = rbf(s);
        if ((hb >= 10 && hb < 20) || (hb >= 40 && hb < 50)) s = rbf(1.f + s);
        if (part == 0) *((LAS unsigned short*)sc + 8 + col) = (unsigned short)(__builtin_bit_cast(unsigned, s) >> 16);
      }
      j1.put(w, lane, L);
      WGBAR();
      if (w == 0) {
        hst128(*(LAS v4u*)((LAS char*)sc + 16 + (lane & 7) * 16), ws, lane < 8 ? lane * 16 : OOB, O_MOD + hb * 128);
        DRAIN();
        hst64(tag, ws, lane < 8 ? O_FB + lane * 1024 + hb * 8 : OOB, 0);
      }
      STAMP(3);
    } else {
      Job<true> ja, jb, jc;
      YOUNG();
      ja.load(hb - 60, w, vw, gw1, gw2, gw3);
      PIN();
      WAITV(7);
      first_pub();
      jb.load(hb - 24, w, vw, gw1, gw2, gw3);
      if (hb < 68) jc.load(hb + 12, w, vw, gw1, gw2, gw3);
      j1.load(36 + hb, w, vn, gw1, gw2, gw4);
      load_q();
      PIN();
      ja.put(w, lane, L + RT_BYTES);
      jb.put(w, lane, L + 2 * RT_BYTES);
      if (hb < 68) jc.put(w, lane, L + 3 * RT_BYTES);
      WGBAR();
      ja.emit(w, lane, L + RT_BYTES, ws);
      jb.emit(w, lane, L + 2 * RT_BYTES, ws);
      if (hb < 68) jc.emit(w, lane, L + 3 * RT_BYTES, ws);
      j1.put(w, lane, L);
      DRAIN();
      WGBAR();
      if (w == 0) hst64(tag, ws, lane < 8 ? O_FB + lane * 1024 + hb * 8 : OOB, 0);
      STAMP(3);
    }
    pollw(ws, O_FW + x * 1024 + lane * 16, lane, 40, tag);
    j1.emit(w, lane, L, ws);
    zero_h1(0);
    zero_h1(1);
    zero_h1(2);
    if (hb >= 34 && hb < 60) hst128(v4u{0u, 0u, 0u, 0u}, ws, w == 0 ? lane * 16 : OOB, O_FR + (hb - 34) * 1024);
    STAMP(4);
    YOUNG();
#pragma unroll
    for (int i = 0; i < 10; ++i) b1[i] = hld128(ws, lane * 16, sb1 + i * 1024);
    PIN();
    ln1();
    PIN();
    WAITV(10);
  }
  if (conv_wg && !wok) pollw(ws, O_FW + x * 1024 + lane * 16, lane, 40, tag);
  WGBAR();
  STAMP(7);
  if (w == 0 && lane < 8)
    __hip_atomic_fetch_add((GAS unsigned*)((GAS char*)gws + O_CNT + (lane * 8 + x) * 128), 1u, __ATOMIC_RELAXED, __HIP_MEMORY_SCOPE_AGENT);
  if (!wok) {
    if (conv_wg) {
#pragma unroll
      for (int i = 0; i < 10; ++i) b1[i] = hld128(ws, lane * 16, sb1 + i * 1024);
    }
#pragma unroll
    for (int i = 10; i < 25; ++i) b1[i] = hld128(ws, lane * 16, sb1 + i * 1024);
#pragma unroll
    for (int i = 0; i < NB2; ++i) b2[i] = hld128(ws, lane * 16, sb2 + i * 1024);
  }
#pragma unroll
  for (int i = NB2; i < 25; ++i) b2[i] = hld128(ws, lane * 16, sb2 + i * 1024);
  v4u b3[35], b4[70];
  const unsigned sb3 = O_W3 + (cg * 140 + w * 35) * 1024, sb4 = O_W4 + (tn * 280 + w * 70) * 1024;
#define LB3(lo, hi) _Pragma("unroll") for (int i_ = lo; i_ < hi; ++i_) b3[i_] = hld128(ws, lane * 16 + (i_ & 3) * 1024, sb3 + (i_ >> 2) * 4096)
  auto ks4 = [](int s) { return s < 8 ? 7 * (s >> 2) + (s & 1) + 4 * ((s >> 1) & 1) : 7 * ((s - 8) / 3) + ((s - 8) % 3 == 2 ? 6 : 2 + (s - 8) % 3); };
  auto lb4 = [&](int i) {
    const int f = ks4(i / 5) * 5 + i % 5;
    b4[i] = hld128(ws, lane * 16 + (f & 3) * 1024, sb4 + (f >> 2) * 4096);
  };
#define LB4(lo, hi) _Pragma("unroll") for (int i_ = lo; i_ < hi; ++i_) lb4(i_)
  if (!bok) {
    pollw(ws, O_FB + x * 1024 + lane * 16, lane, 48, tag);
    load_mod(true);
  }
  constexpr int NB3C = 10;
  f2 gh[10];
  float cb1[10], sbv[10];
  auto md = [&](int v, int i) { const unsigned u = i < 8 ? mm[v][(i >> 1) & 3] : mo[v]; return (i & 1) ? hi2f(u) : lo2f(u); };
  auto use_mod = [&]() {
    LB3(0, NB3C);
#pragma unroll
    for (int i = 0; i < 10; ++i) {
      gh[i] = (n1[i] * md(1, i) + md(0, i)) * md(2, i);
      cb1[i] = md(4, i);
      sbv[i] = md(3, i);
      asm volatile("" : "+v"(gh[i]), "+v"(cb1[i]), "+v"(sbv[i]));
    }
  };
  use_mod();
  PIN();
  if (w == 0) {
    poll64(ws, lane < 8 ? O_CNT + (x * 8 + lane) * 128 : OOB, v2u{tag.x + 32, tag.y});
    if (b == 0) hst64(tag, ws, lane == 0 ? O_EPOCH : OOB, 0);
  }
  WGBAR();
  STAMP(8);

  v4u hx[6];
  f4 csp[7];
  auto img3 = [&](int lo, int hi, int hc) {
    const v4u ones = {0x3f803f80u, 0x3f803f80u, 0x3f803f80u, 0x3f803f80u};
    unsigned la = w * 25600 + lane * 16, lb = (75 + w * 10) * 1024 + lane * 16;
    asm volatile("" : "+v"(la), "+v"(lb));
#pragma unroll
    for (int i = lo; i < 35; ++i) {
      if (i < hi) *(LAS v4u*)(L + (i < 25 ? la : lb) + i * 1024) = b3[i];
      if (i < hc) csp[i % 7] = mfma(b3[i], ones, i < 7 ? f4{0.f, 0.f, 0.f, 0.f} : csp[i % 7]);
      PIN();
    }
  };
  {
    v4u zc[3];
#pragma unroll
    for (int i = 0; i < 3; ++i) zc[i] = hld128(ws, i < 2 ? tid * 16 + i * 4096 : z3off, O_ZT + (8 * tm + tn) * ZP);
    PIN();
#pragma unroll
    for (int i = 0; i < 3; ++i)
#pragma unroll
      for (int e = 0; e < 4; ++e) *(LAS unsigned*)(L + RED_OFF + e * 3072 + (tid + 256 * i) * 4) = zc[i][e];
    STAMP(9);
    WGBAR();
    STAMP(10);
    const v4u z0 = *(LAS v4u*)(L + RED_OFF + w * 3072 + rcm * 4), z1 = *(LAS v4u*)(L + RED_OFF + w * 3072 + rcm * 4 + 16);
    const v2u z2 = *(LAS v2u*)(L + RED_OFF + w * 3072 + rco * 4);
    LB3(10, 19);
    f2 xv[10], hv[10], av[10], s, ss, r, mr;
#pragma unroll
    for (int i = 0; i < 10; ++i) {
      xv[i] = gh[i] * up2(i < 4 ? z0[i & 3] : (i < 8 ? z1[i & 3] : z2[i & 1])) + qv[i];
      s = i ? s + xv[i] : xv[i];
      ss = i ? ss + xv[i] * xv[i] : xv[i] * xv[i];
    }
    wave_sum4(s, ss);
    ln_scal(s, ss, r, mr);
#pragma unroll
    for (int i = 0; i < 10; ++i) {
      hv[i] = (xv[i] * r + mr) * cb1[i] + sbv[i];
      s = i ? s + hv[i] : hv[i];
      ss = i ? ss + hv[i] * hv[i] : hv[i] * hv[i];
    }
    wave_sum4(s, ss);
    ln_scal(s, ss, r, mr);
#pragma unroll
    for (int i = 0; i < 10; ++i) av[i] = hv[i] * r + mr;
    auto put = [&](const f2(&v)[10], int img) {
      *(LAS v4u*)(L + im0 + img * 10240) = v4u{pk2(v[0].x, v[1].x), pk2(v[2].x, v[3].x), pk2(v[4].x, v[5].x), pk2(v[6].x, v[7].x)};
      *(LAS v4u*)(L + (im0 ^ 16) + img * 10240) = v4u{pk2(v[0].y, v[1].y), pk2(v[2].y, v[3].y), pk2(v[4].y, v[5].y), pk2(v[6].y, v[7].y)};
      *(LAS unsigned*)(L + io0 + img * 10240) = pk2(v[8].x, v[9].x);
      *(LAS unsigned*)(L + (io0 ^ 16) + img * 10240) = pk2(v[8].y, v[9].y);
    };
    put(av, 0);
    STAMP(11);
    WGBAR();
    {
      v4u oc[3];
#pragma unroll
      for (int i = 0; i < 3; ++i) oc[i] = *(LAS v4u*)(L + RED_OFF + 16384 + i * 4096 + tid * 16);
#pragma unroll
      for (int i = 0; i < 3; ++i) hst128(oc[i], ws, i < 2 ? roff : roff3, i * 8192);
    }
    PIN();
    put(hv, 1);
    put(xv, 2);
    STAMP(12);
    DRAIN();
    WGBAR();
    STAMP(13);
    if (w == 0) hst64(tag, ws, frpub, 0);
    LB3(19, 28);
    PIN();
#pragma unroll
    for (int i = 0; i < 6; ++i) hx[i] = *(LAS v4u*)(L + RED_OFF + 26624 + (i / 3) * 10240 + (i % 3) * 4096 + tid * 16);
    img3(0, 16, 16);
    if (w == 0) poll64(ws, frwait, tag);
    WGBAR();
    STAMP(14);
  }

  {
    f4 acc[5];
    const unsigned vo[4] = {(unsigned)lane * 16, (unsigned)lane * 16, (unsigned)lane * 16, (unsigned)lane * 16};
    gemm_ks(ws, O_A1 + (4 * tm * 20 + 5 * w) * 1024, vo, b1, L, w, lane, acc,
            [&](int n) { if (n < 7) { LB3(28 + n, 29 + n); } else lb4(16 + n); },
            [&](int n, int t) {
              if (t == 3 && n >= 8 && n < 14) hst128(hx[n - 8], ws, ((n - 8) % 3) < 2 ? roff : roff3, (1 + (n - 8) / 3) * SD2 + ((n - 8) % 3) * 8192);
            } STPASS(15));
    STAMP(18);
    f2 v[10];
    unsigned o[10];
#pragma unroll
    for (int t = 0; t < 5; ++t) {
      v[2 * t] = f2{acc[t][0], acc[t][1]};
      v[2 * t + 1] = f2{acc[t][2], acc[t][3]};
    }
    gelu_pack<10>(v, 1.f, 1.f, o, [](int) {});
    const auto s0 = __builtin_amdgcn_permlane16_swap(o[8], o[8], false, false);
    const auto s1 = __builtin_amdgcn_permlane16_swap(o[9], o[9], false, false);
    hst128(v4u{o[0], o[1], o[2], o[3]}, ws, h1m, 0);
    hst128(v4u{o[4], o[5], o[6], o[7]}, ws, h1m + 1024, 0);
    hst128(v4u{s0[0], s1[0], s0[1], s1[1]}, ws, h1s, 0);
    STAMP(19);
    DRAIN();
    WGBAR();
    STAMP(20);
    if (w == 0) hst64(tag, ws, f1pub, 0);
    LB4(0, 9);
    PIN();
    img3(16, 24, 35);
    if (l15 == 0) {
#pragma unroll
      for (int t = 0; t < 7; ++t) *(LAS f4*)(L + (w * 25 + 24) * 1024 + (t * 4 + g) * 16) = csp[t];
    }
    if (w == 0) poll64(ws, f1wait, tag);
    WGBAR();
    STAMP(21);
  }
  v4u xr[2];
  v2u xro;
  unsigned x1p[10];
  {
    v4u hr[2];
    v2u hro;
    f4 acc[5];
    f2 hf[10];
    const unsigned vo[4] = {(unsigned)lane * 16, (unsigned)lane * 16, (unsigned)lane * 16, (unsigned)lane * 16};
    gemm_ks(ws, O_H1 + (4 * tm * 20 + 5 * w) * 1024, vo, b2, L, w, lane, acc, [&](int n) {
      lb4(n < 5 ? 9 + n : 28 + n);
    }, [&](int n, int t) {
      if (t == 3 && n == 2) hr[0] = hld128(ws, lane * 16, O_HP + own_m);
      if (t == 3 && n == 4) hr[1] = hld128(ws, lane * 16 + 1024, O_HP + own_m);
      if (t == 3 && n == 6) hro = hld64(ws, own_r, O_HP + own_s);
      if (n == 18) {
        unsigned h0 = t < 4 ? hr[t >> 1][2 * (t & 1)] : hro.x;
        KEEP(h0);
        hf[2 * t] = up2(h0);
        KEEP(hf[2 * t]);
      }
    } STPASS(22));
#pragma unroll
    for (int t = 0; t < 5; ++t) {
      const unsigned h1 = t < 4 ? hr[t >> 1][2 * (t & 1) + 1] : hro.y;
      const f2 p = f2{acc[t][0], acc[t][1]} + hf[2 * t], q = f2{acc[t][2], acc[t][3]} + up2(h1);
      x1p[2 * t] = pk2(p.x, p.y);
      x1p[2 * t + 1] = pk2(q.x, q.y);
    }
    const auto s0 = __builtin_amdgcn_permlane16_swap(x1p[8], x1p[8], false, false);
    const auto s1 = __builtin_amdgcn_permlane16_swap(x1p[9], x1p[9], false, false);
    hst128(v4u{x1p[0], x1p[1], x1p[2], x1p[3]}, ws, x1m, 0);
    hst128(v4u{x1p[4], x1p[5], x1p[6], x1p[7]}, ws, x1m + 1024, 0);
    hst128(v4u{s0[0], s1[0], s0[1], s1[1]}, ws, x1s, 0);
    float t1 = 0.f, t2 = 0.f;
#pragma unroll
    for (int i = 0; i < 10; ++i) {
      t1 = __builtin_amdgcn_fdot2_f32_bf16(__builtin_bit_cast(bf2, x1p[i]), __builtin_bit_cast(bf2, 0x3f803f80u), t1, false);
      t2 = __builtin_amdgcn_fdot2_f32_bf16(__builtin_bit_cast(bf2, x1p[i]), __builtin_bit_cast(bf2, x1p[i]), t2, false);
    }
    t1 = swap32_add(t1, t2);
    t1 = swap16_add(t1, t1);
    hst32(__builtin_bit_cast(unsigned, t1), ws, stoff, 0);
    STAMP(25);
    f4 cst;
    {
      f4 cp[4];
#pragma unroll
      for (int v = 0; v < 4; ++v) cp[v] = *(LAS f4*)(L + (v * 25 + 24) * 1024 + lane * 16);
      cst = (cp[0] + cp[1]) + (cp[2] + cp[3]);
      KEEP(cst);
    }
    PIN();
    DRAIN();
    WGBAR();
    STAMP(26);
    if (w == 0) hst64(tag, ws, f2pub, 0);
    LB4(14, 23);
    PIN();
    img3(24, 35, 0);
    if (w == 1) *(LAS f4*)(L + CSUM_OFF + lane * 16) = cst;
    if (w == 0) poll64(ws, f2wait, tag);
    WGBAR();
    STAMP(27);
  }
#define PUBK 10
#define PUBN 9
#define SMPN 20
#define CHKN 26
#define WIN4_ 8
  constexpr int WIN4 = WIN4_, RING4 = WIN4 + 1;
  v4u a4[RING4];
  auto lda4 = [&](int n) {
    const int i = n < 32 ? n >> 3 : (n - 32) / 6, k = ks4(n < 32 ? n & 7 : 8 + (n - 32) % 6);
    a4[n % RING4] = hld128(ws, lane * 16 + (k & 3) * 1024, O_H + ((4 * tm + ((w + 1 + i) & 3)) * 56 + 14 * w + (k & ~3)) * 1024);
  };
  {
#define W3_ 6
    constexpr int W3 = W3_;
    v4u a[20][2];
    const unsigned sa = O_X1 + (8 * rg + 2 * w) * 20 * 1024;
    auto lda = [&](int k, int i) { a[k][i] = hld128(ws, lane * 16 + (k & 3) * 1024, sa + i * 20480 + (k >> 2) * 4096); };
#pragma unroll
    for (int k = 0; k < W3; ++k)
#pragma unroll
      for (int i = 0; i < 2; ++i) {
        lda(k, i);
        PIN();
      }
    f4 acc[2][7];
    v2u sv[2][2];
    f2 r, den, mean;
    auto bslot = [](int k, int t) { const int v = k / 5, i = (k % 5) * 7 + t; return (i < 25 ? v * 25 + i : 75 + v * 10 + i) * 1024; };
    const LAS char* Lb = L + lane * 16;
    const LAS char* Lb2 = L + lane * 16 + 65536;
    const LAS char* Lb3 = L + lane * 16 + 131072;
    auto rdb = [&](int k, int t) { const int o = bslot(k, t); return *(const LAS v4u*)(o < 65536 ? Lb + o : (o < 131072 ? Lb2 + (o - 65536) : Lb3 + (o - 131072))); };
    v4u bf[3][4];
#pragma unroll
    for (int t = 0; t < 8; ++t) {
      bf[t >> 2][t & 3] = rdb(t >> 2, t & 3);
      PIN();
    }
    sfor<0, 20>([&](auto ic) {
      constexpr int k = decltype(ic)::v;
      __builtin_amdgcn_s_waitcnt(k < 19 ? 0xC47F : 0xC37F);
#pragma unroll
      for (int t = 0; t < 4; ++t) {
        if (k == 0) mfma_va0(acc[0][t], bf[0][t], a[0][0]);
        else mfma_va(acc[0][t], bf[k % 3][t], a[k][0]);
        if (k < 18) bf[(k + 2) % 3][t] = rdb(k + 2, t);
        else if (t < 3) bf[(k + 2) % 3][t] = rdb(k - 18, 4 + t);
        PIN();
      }
#pragma unroll
      for (int t = 0; t < 4; ++t) {
        if (k == 0) mfma_va0(acc[1][t], bf[0][t], a[0][1]);
        else mfma_va(acc[1][t], bf[k % 3][t], a[k][1]);
        if (t < 2) {
          if constexpr (k + W3 < 20) lda(k + W3, t);
        }
        if (t >= (k + W3 < 20 ? 2 : 0) && t < (k + W3 < 20 ? 4 : 2)) {
          if constexpr (k == 14 || k == 15) sv[k - 14][t & 1] = hld64(ws, l15 * 8 + g * 256 + (t & 1) * 128, O_STAT + (8 * rg + 2 * w + k - 14) * 1024);
          else if constexpr (k >= 16) lb4(40 + 2 * (k - 16) + (t & 1));
        }
        PIN();
      }
      if (k == 0) STAMP(28);
    });
    STAMP(29);
    const unsigned hs = O_H + ((8 * rg + 2 * w) * 56 + 7 * (cg >> 1) + 4 * (cg & 1)) * 1024 + lane * 16;
    unsigned lcs = CSUM_OFF + g * 16;
    KEEP(lcs);
#pragma unroll
    for (int t = 0; t < 4; ++t) csp[t] = *(LAS f4*)(L + lcs + t * 64);
    MFENCE();
    PIN();
    {
      asm volatile("" : "+v"(sv[0][0]), "+v"(sv[0][1]), "+v"(sv[1][0]), "+v"(sv[1][1]));
      const f2 p0 = __builtin_bit_cast(f2, sv[0][0]) + __builtin_bit_cast(f2, sv[0][1]);
      const f2 p1 = __builtin_bit_cast(f2, sv[1][0]) + __builtin_bit_cast(f2, sv[1][1]);
      f2 mr;
      ln_scal(f2{xrow_sum(p0.x), xrow_sum(p1.x)}, f2{xrow_sum(p0.y), xrow_sum(p1.y)}, r, mr, &mean, &den);
    }
#pragma unroll
    for (int i = 0; i < 2; ++i) {
      f2 v[8];
      unsigned o[8];
#pragma unroll
      for (int e = 0; e < 8; ++e) {
        const int t = e >> 1, h = 2 * (e & 1);
        v[e] = f2{acc[i][t][h], acc[i][t][h + 1]} - f2{csp[t][h], csp[t][h + 1]} * mean[i];
      }
      gelu_pack<8>(v, r[i], den[i], o, [&](int m) {
        if (i == 0) lb4(28 + m);
        else if (m == 0) lb4(32);
      });
      hst128(v4u{o[0], o[1], o[2], o[3]}, ws, hs, i * 57344);
      hst128(v4u{o[4], o[5], o[6], o[7]}, ws, hs, i * 57344 + 1024);
    }
    YOUNG();
    STAMP(30);
    sfor<0, 20>([&](auto ic) {
      constexpr int k = decltype(ic)::v;
      __builtin_amdgcn_s_waitcnt(k < 19 ? 0xC37F : 0xC07F);
      if (k == PUBK) {
        WAITV(10);
        hst64(tag, ws, f3pub, 0);
      }
#pragma unroll
      for (int e = 0; e < 6; ++e) {
        const int i = e / 3, t = e % 3;
        if (k == 0) mfma_va0(acc[i][4 + t], bf[2][t], a[0][i]);
        else mfma_va(acc[i][4 + t], bf[(k + 2) % 3][t], a[k][i]);
        if (i == 0 && k < 18) bf[(k + 1) % 3][t] = rdb(k + 2, 4 + t);
        if (k >= 1 && k < 5 && e == 5) lb4(47 + k);
        if (k >= 1 && k < 19 && e == 3) lb4(51 + k);
        PIN();
      }
    });
#define SMPM 0
    v2u fl;
    if (SMPM < 0) fl = hld64(ws, f3wait, 0);
#pragma unroll
    for (int t = 4; t < 7; ++t) csp[t] = *(LAS f4*)(L + lcs + t * 64);
    MFENCE();
    PIN();
    STAMP(37);
    unsigned os[2][2];
#pragma unroll
    for (int i = 0; i < 2; ++i) {
      f2 v[6];
      unsigned o[6];
#pragma unroll
      for (int e = 0; e < 6; ++e) {
        const int t = 4 + (e >> 1), h = 2 * (e & 1);
        v[e] = f2{acc[i][t][h], acc[i][t][h + 1]} - f2{csp[t][h], csp[t][h + 1]} * mean[i];
      }
      gelu_pack<6>(v, r[i], den[i], o, [&](int m) {
        if (i == 0 && m == SMPM) fl = hld64(ws, f3wait, 0);
      });
      hst128(v4u{o[0], o[1], o[2], o[3]}, ws, hs, i * 57344 + 2048);
      os[i][0] = o[4];
      os[i][1] = o[5];
      if (i == 0) {
        PIN();
        KEEP(fl);
        if (!__all(f3wait >= OOB || (fl.x == tag.x && fl.y == tag.y))) poll64(ws, f3wait, tag);
        __builtin_amdgcn_fence(__ATOMIC_ACQUIRE, "wavefront");
        STAMP(32);
#pragma unroll
        for (int n = 0; n < WIN4; ++n) {
          lda4(n);
          PIN();
        }
      }
    }
    {
      const auto s0 = __builtin_amdgcn_permlane16_swap(os[0][0], os[1][0], false, false);
      const auto s1 = __builtin_amdgcn_permlane16_swap(os[0][1], os[1][1], false, false);
      hst128(v4u{s0[0], s1[0], s0[1], s1[1]}, ws, (g & 1) * 57344 + (l15 + 16 * (g >> 1)) * 16,
             O_H + ((8 * rg + 2 * w) * 56 + 7 * (cg >> 1) + 3) * 1024 + (cg & 1) * 512);
    }
    YOUNG();
    STAMP(31);
  }
  {
    v4u gb[2];
    v2u gbo, fl;
    f4 ac[4][5], p[15];
    float gf[20], cf[20];
    unsigned lq = lane;
    KEEP(lq);
    const unsigned lr = RED_OFF + lq * 16, gq4 = lq >> 4;
    unsigned dm = ((64 * tm + 16 * w + (lq & 15)) * D_ + 80 * tn) * 2, ds = dm + 8 * gq4 + ((tn & 1) ? 0 : 128);
    dm += 16 * gq4 + 32 * (tn & 1);
    unsigned xro_off = ((lq & 15) + 16 * (gq4 >> 1)) * 16 + 8 * (gq4 & 1);
    sfor<0, 56>([&](auto ic) {
      constexpr int n = decltype(ic)::v, i = n < 32 ? n >> 3 : (n - 32) / 6, sl = n < 32 ? n & 7 : 8 + (n - 32) % 6;
      if (n == 54) {
        STAMP(35);
        WGBAR();
        STAMP(36);
      }
      if (n == PUBN) {
        WAITV(7);
        hst64(tag, ws, f3pub + (O_F4 - O_F3), 0);
        STAMP(33);
      }
      if (n == 23) asm volatile("" : "+v"(dm), "+v"(ds), "+v"(xro_off));
      if (n == 37) asm volatile("s_barrier" ::: "memory");
      if (n == CHKN) {
        KEEP(fl);
        if (!__all(f3wait >= OOB || (fl.x == tag.x && fl.y == tag.y))) poll64(ws, f3wait + (O_F4 - O_F3), tag);
        __builtin_amdgcn_fence(__ATOMIC_ACQUIRE, "wavefront");
#pragma unroll
        for (int m = 32; m < CHKN + WIN4; ++m) lda4(m);
        PIN();
      }
#pragma unroll
      for (int t = 0; t < 5; ++t) {
        const int f = sl * 5 + t;
        if (sl == 0) mfma_aa0(ac[i][t], b4[f], a4[n % RING4]);
        else if (f >= 23 && f < 48) mfma_va(ac[i][t], b4[f], a4[n % RING4]);
        else mfma_aa(ac[i][t], b4[f], a4[n % RING4]);
        if (t == 1) {
          if constexpr (n + WIN4 < 56) {
            if (n < 32 - WIN4 || n >= CHKN) lda4(n + WIN4);
          }
        }
        if (t == 3) {
          if (n == 30 || n == 31) gb[n & 1] = hld128(ws, gq4 * 16 + (n & 1) * 64, O_MOD + (5 * D_ + 80 * tn + 16 * (tn & 1)) * 2);
          if (n == 32 || n == 33) xr[n & 1] = hld128(ws, lq * 16 + (n & 1) * 1024, O_X + own_m);
          if (n == 34) gbo = hld64(ws, gq4 * 8, O_MOD + (5 * D_ + 80 * tn + ((tn & 1) ? 0 : 64)) * 2);
          if (n == 35) xro = hld64(ws, xro_off, O_X + own_s);
        }
        if constexpr (n >= 44 && n < 54) {
          constexpr int q = n - 44;
          const unsigned gw = q < 8 ? gb[(q >> 2) & 1][q & 3] : gbo[q & 1], xw = q < 8 ? xr[(q >> 2) & 1][q & 3] : xro[q & 1];
          float tmp;
          if (t == 1) asm volatile("v_lshlrev_b32 %0, 16, %2\n\tv_and_b32 %1, 0xffff0000, %2" : "=&v"(gf[2 * q]), "=v"(gf[2 * q + 1]) : "v"(gw));
          if (t == 2) asm volatile("v_lshlrev_b32 %0, 16, %2\n\tv_and_b32 %1, 0xffff0000, %2" : "=&v"(cf[2 * q]), "=v"(cf[2 * q + 1]) : "v"(xw));
          if (t == 3) asm volatile("v_lshlrev_b32 %1, 16, %2\n\tv_fmac_f32 %0, %1, %3" : "+v"(cf[2 * q]), "=&v"(tmp) : "v"(x1p[q]), "v"(gf[2 * q]));
          if (t == 4) asm volatile("v_and_b32 %1, 0xffff0000, %2\n\tv_fmac_f32 %0, %1, %3" : "+v"(cf[2 * q + 1]), "=&v"(tmp) : "v"(x1p[q]), "v"(gf[2 * q + 1]));
        }
        if (n == SMPN && t == 0) fl = hld64(ws, f3wait + (O_F4 - O_F3), 0);
        if (n >= 38 && sl < 11 && (t == 0 || t == 2) && (sl - 8) * 2 + (t == 2) > 0) {
          const int u = (sl - 8) * 2 + (t == 2) - 1;
          *(LAS f4*)(L + (lr + ((w + i) & 3) * 15360) + ((i - 1) * 5 + u) * 1024) = ac[i > 0 ? i - 1 : 0][u];
        }
        if (n == 54) {
          p[t] = *(LAS f4*)(L + (lr + w * 15360) + t * 1024);
          p[5 + t] = *(LAS f4*)(L + (lr + w * 15360) + (5 + t) * 1024);
        }
        if (n == 55) p[10 + t] = *(LAS f4*)(L + (lr + w * 15360) + (10 + t) * 1024);
        PIN();
      }
      if (n == 0) STAMP(34);
    });
    MFENCE();
    PIN();
    unsigned o[10];
#pragma unroll
    for (int t = 0; t < 5; ++t) {
      const f4 y = (ac[3][t] + p[t]) + (p[5 + t] + p[10 + t]);
      const f2 u = f2{y[0], y[1]} * f2{gf[4 * t], gf[4 * t + 1]} + f2{cf[4 * t], cf[4 * t + 1]};
      const f2 v = f2{y[2], y[3]} * f2{gf[4 * t + 2], gf[4 * t + 3]} + f2{cf[4 * t + 2], cf[4 * t + 3]};
      o[2 * t] = pk2(u.x, u.y);
      o[2 * t + 1] = pk2(v.x, v.y);
    }
    const rsrc_t ro = mkrs(gout, SD2);
    __builtin_amdgcn_raw_buffer_store_b128(v4u{o[0], o[1], o[2], o[3]}, ro, dm, 0, 0);
    __builtin_amdgcn_raw_buffer_store_b128(v4u{o[4], o[5], o[6], o[7]}, ro, dm + 64, 0, 0);
    __builtin_amdgcn_raw_buffer_store_b64(v2u{o[8], o[9]}, ro, ds, 0, 0);
  }
  asm volatile(".Lfused_end:" ::: "memory");
  {
    v4u t4 = hld128(ws, O_MOD + 12288 + lane * 16, 0);
    int m = min(min((int)(t4.x - tstart), (int)(t4.y - tstart)), min((int)(t4.z - tstart), (int)(t4.w - tstart)));
    for (int o = 32; o; o >>= 1) m = min(m, __shfl_xor(m, o));
    const unsigned t0 = tstart + __builtin_amdgcn_readfirstlane(m);
    while ((unsigned)__builtin_amdgcn_s_memrealtime() - t0 < 6000u) __builtin_amdgcn_s_sleep(1);
  }
}

extern "C" void kernel_launch(void* const* d_in, const int* in_sizes, int n_in, void* d_out, int out_size,
                              void* d_ws, size_t ws_size, hipStream_t stream) {
  (void)hipFuncSetAttribute((const void*)fused, hipFuncAttributeMaxDynamicSharedMemorySize, LDS_BYTES);
  fused<<<256, 256, LDS_BYTES, stream>>>(d_in[0], d_in[1], d_in[2], d_in[3], d_in[4], d_in[5], d_in[7], d_in[8], d_in[9],
                                         d_in[10], d_in[11], d_in[12], d_out, d_ws);
}
```

```cpp
#include <hip/hip_runtime.h>
#include <stdint.h>

typedef unsigned v4u __attribute__((ext_vector_type(4)));
typedef unsigned v2u __attribute__((ext_vector_type(2)));
typedef float f4 __attribute__((ext_vector_type(4)));
typedef float f2 __attribute__((ext_vector_type(2)));
typedef short s4v __attribute__((ext_vector_type(4)));
typedef __bf16 bf2 __attribute__((ext_vector_type(2)));
typedef __bf16 bfx8 __attribute__((ext_vector_type(8)));
typedef __amdgpu_buffer_rsrc_t rsrc_t;
#define LAS __attribute__((address_space(3)))
#define GAS __attribute__((address_space(1)))
#define DI __device__ __forceinline__

static constexpr int S_ = 2048, D_ = 640, F_ = 1728;
static constexpr unsigned OOB = 0x80000000u;

static constexpr unsigned O_EPOCH = 0;
static constexpr unsigned O_FP = 1024;
static constexpr unsigned O_FR = O_FP + 16 * 128;
static constexpr unsigned O_F1 = O_FR + 32 * 128;
static constexpr unsigned O_F2 = O_F1 + 32 * 128;
static constexpr unsigned O_F3 = O_F2 + 16 * 128;
static constexpr unsigned O_F4 = O_F3 + 16 * 512;
static constexpr unsigned O_CNT = O_F4 + 16 * 512;
static constexpr unsigned O_FW = O_CNT + 64 * 128;
static constexpr unsigned O_FB = O_FW + 8192;
static constexpr unsigned O_MOD = 65536;
static constexpr unsigned O_STAT = O_MOD + 16384;
static constexpr unsigned O_KVT = O_STAT + 131072;
static constexpr unsigned SD2 = S_ * D_ * 2;
static constexpr unsigned KVP = 4096 + 128, ZP = 10240 + 128;
static constexpr unsigned O_ZT = O_KVT + D_ * KVP;
static constexpr unsigned O_A1 = O_ZT + 256 * ZP;
static constexpr unsigned O_HP = O_A1 + SD2;
static constexpr unsigned O_X = O_HP + SD2;
static constexpr unsigned O_H1 = O_X + SD2;
static constexpr unsigned O_X1 = O_H1 + SD2;
static constexpr unsigned O_H = O_X1 + SD2;
static constexpr unsigned O_W1 = O_H + 128 * 56 * 1024;
static constexpr unsigned O_W2 = O_W1 + 800 * 1024;
static constexpr unsigned O_W3 = O_W2 + 800 * 1024;
static constexpr unsigned O_W4 = O_W3 + 2240 * 1024;
static constexpr unsigned O_END = O_W4 + 2240 * 1024;

static constexpr int CS = 4160;
static constexpr int UOFF = 8 * CS;
static constexpr int WR = 8 * CS + 4608;
static constexpr int LDS_BYTES = 160 * 1024;
static constexpr int RED_OFF = 100 * 1024;
static constexpr int CSUM_OFF = 140 * 1024;
static constexpr unsigned SPIN_CAP = 200000;

template <int V> struct IC { static constexpr int v = V; };
template <int I, int N, class F> DI void sfor(F&& f) {
  if constexpr (I < N) {
    f(IC<I>{});
    sfor<I + 1, N>(f);
  }
}
DI float lo2f(unsigned w) { return __builtin_bit_cast(float, w << 16); }
DI float hi2f(unsigned w) { return __builtin_bit_cast(float, w & 0xffff0000u); }
DI f2 up2(unsigned w) { return f2{lo2f(w), hi2f(w)}; }
DI unsigned pk2(float a, float b) { return __builtin_bit_cast(unsigned, __builtin_convertvector(f2{a, b}, bf2)); }
DI f4 mfma(v4u a, v4u b, f4 c) {
  return __builtin_amdgcn_mfma_f32_16x16x32_bf16(__builtin_bit_cast(bfx8, a), __builtin_bit_cast(bfx8, b), c, 0, 0, 0);
}
DI void mfma_v(f4& c, v4u a, v4u b) { asm volatile("v_mfma_f32_16x16x32_bf16 %0, %1, %2, %0" : "+v"(c) : "v"(a), "v"(b)); }
DI void mfma_v0(f4& c, v4u a, v4u b) { asm volatile("v_mfma_f32_16x16x32_bf16 %0, %1, %2, 0" : "=&v"(c) : "v"(a), "v"(b)); }
DI void mfma_va(f4& c, v4u a, v4u b) { asm volatile("v_mfma_f32_16x16x32_bf16 %0, %1, %2, %0" : "+v"(c) : "v"(a), "a"(b)); }
DI void mfma_va0(f4& c, v4u a, v4u b) { asm volatile("v_mfma_f32_16x16x32_bf16 %0, %1, %2, 0" : "=&v"(c) : "v"(a), "a"(b)); }
DI void mfma_av(f4& c, v4u a, v4u b) { asm volatile("v_mfma_f32_16x16x32_bf16 %0, %1, %2, %0" : "+v"(c) : "a"(a), "v"(b)); }
DI void mfma_av0(f4& c, v4u a, v4u b) { asm volatile("v_mfma_f32_16x16x32_bf16 %0, %1, %2, 0" : "=&v"(c) : "a"(a), "v"(b)); }
DI void mfma_aa(f4& c, v4u a, v4u b) { asm volatile("v_mfma_f32_16x16x32_bf16 %0, %1, %2, %0" : "+v"(c) : "a"(a), "a"(b)); }
DI void mfma_aa0(f4& c, v4u a, v4u b) { asm volatile("v_mfma_f32_16x16x32_bf16 %0, %1, %2, 0" : "=&v"(c) : "a"(a), "a"(b)); }
#define KEEP(x) asm volatile("" : "+v"(x))
#define MFENCE() asm volatile("s_nop 7\n\ts_nop 1" ::: "memory")
DI float add1(float a, float b) { float r; asm("v_add_f32 %0, %1, %2" : "=v"(r) : "v"(a), "v"(b)); return r; }
DI rsrc_t mkrs(const void* p, unsigned bytes) { return __builtin_amdgcn_make_buffer_rsrc((void*)p, 0, bytes, 0x00020000); }
DI v4u ld128(rsrc_t r, unsigned v, unsigned s) { return __builtin_amdgcn_raw_buffer_load_b128(r, v, s, 2); }
DI unsigned ld32(rsrc_t r, unsigned v, unsigned s) { return __builtin_amdgcn_raw_buffer_load_b32(r, v, s, 2); }
DI v4u hld128(rsrc_t r, unsigned v, unsigned s) { return __builtin_amdgcn_raw_buffer_load_b128(r, v, s, 16); }
DI v2u hld64(rsrc_t r, unsigned v, unsigned s) { return __builtin_amdgcn_raw_buffer_load_b64(r, v, s, 16); }
DI void hst128(v4u x, rsrc_t r, unsigned v, unsigned s) { __builtin_amdgcn_raw_buffer_store_b128(x, r, v + s, 0, 16); }
DI void hst64(v2u x, rsrc_t r, unsigned v, unsigned s) { __builtin_amdgcn_raw_buffer_store_b64(x, r, v, s, 16); }
DI void hst32(unsigned x, rsrc_t r, unsigned v, unsigned s) { __builtin_amdgcn_raw_buffer_store_b32(x, r, v, s, 16); }
#define DRAIN() do { asm volatile("s_waitcnt vmcnt(0)" ::: "memory"); __builtin_amdgcn_s_waitcnt(0x0F70); } while (0)
#define WGBAR() asm volatile("s_waitcnt lgkmcnt(0)\n\ts_barrier" ::: "memory")
#define PIN() __builtin_amdgcn_sched_barrier(0)

template <int CTRL> DI float dpp(float x) {
  return __builtin_bit_cast(float, __builtin_amdgcn_mov_dpp(__builtin_bit_cast(int, x), CTRL, 0xf, 0xf, true));
}
DI float xrow_sum(float x) {
  auto s = __builtin_amdgcn_permlane16_swap(__builtin_bit_cast(unsigned, x), __builtin_bit_cast(unsigned, x), false, false);
  x = __builtin_bit_cast(float, (unsigned)s[0]) + __builtin_bit_cast(float, (unsigned)s[1]);
  auto t = __builtin_amdgcn_permlane32_swap(__builtin_bit_cast(unsigned, x), __builtin_bit_cast(unsigned, x), false, false);
  return __builtin_bit_cast(float, (unsigned)t[0]) + __builtin_bit_cast(float, (unsigned)t[1]);
}
DI float swap32_add(float a, float b) {
  auto t = __builtin_amdgcn_permlane32_swap(__builtin_bit_cast(unsigned, a), __builtin_bit_cast(unsigned, b), false, false);
  return __builtin_bit_cast(float, (unsigned)t[0]) + __builtin_bit_cast(float, (unsigned)t[1]);
}
DI float swap16_add(float a, float b) {
  auto t = __builtin_amdgcn_permlane16_swap(__builtin_bit_cast(unsigned, a), __builtin_bit_cast(unsigned, b), false, false);
  return __builtin_bit_cast(float, (unsigned)t[0]) + __builtin_bit_cast(float, (unsigned)t[1]);
}
DI float rdl(float v, int l) { return __builtin_bit_cast(float, __builtin_amdgcn_readlane(__builtin_bit_cast(int, v), l)); }
DI void wave_sum4(f2& s, f2& ss) {
  float v = swap16_add(swap32_add(s.x, s.y), swap32_add(ss.x, ss.y));
  v += dpp<0xB1>(v);
  v += dpp<0x4E>(v);
  v += dpp<0x141>(v);
  v += dpp<0x128>(v);
  s = f2{rdl(v, 0), rdl(v, 32)};
  ss = f2{rdl(v, 16), rdl(v, 48)};
}
DI void poll64(rsrc_t ws, unsigned voff, v2u want) {
  const bool idle = voff >= OOB;
  for (unsigned spin = 0; spin < SPIN_CAP; ++spin) {
    asm volatile("" ::: "memory");
    const v2u v = hld64(ws, voff, 0);
    if (__all(idle || (v.x == want.x && v.y == want.y))) break;
  }
  __builtin_amdgcn_fence(__ATOMIC_ACQUIRE, "wavefront");
}

DI bool wflags_ok(v4u a, int lane, int nl, v2u want) {
  return __all(lane >= nl || (a.x == want.x && a.y == want.y && a.z == want.x && a.w == want.y));
}
DI void pollw(rsrc_t ws, unsigned voff, int lane, int nl, v2u want) {
  for (unsigned spin = 0; spin < SPIN_CAP; ++spin) {
    asm volatile("" ::: "memory");
    if (wflags_ok(hld128(ws, voff, 0), lane, nl, want)) break;
    __builtin_amdgcn_s_sleep(4);
  }
  __builtin_amdgcn_fence(__ATOMIC_ACQUIRE, "wavefront");
}
DI float rbf(float x) { return lo2f(pk2(x, 0.f)); }
DI void ln_scal(f2 s, f2 ss, f2& r, f2& mr, f2* pmean = nullptr, f2* pden = nullptr) {
  const f2 m = s * (1.f / D_);
  const f2 var = ss * (1.f / D_) - m * m;
  const f2 v = f2{fmaxf(var.x, 0.f), fmaxf(var.y, 0.f)} + 1e-5f;
  r = f2{__builtin_amdgcn_rsqf(v.x), __builtin_amdgcn_rsqf(v.y)};
  mr = -m * r;
  if (pmean) *pmean = m;
  if (pden) *pden = v * r;
}
template <int N, class Mid> DI void gelu_pack(const f2 (&x)[N], float r, float den, unsigned* o, Mid mid) {
  const float c0 = -2.f * 0.7978845608028654f * 1.4426950408889634f * r, c1 = c0 * 0.044715f * r * r;
  f2 a[N];
#pragma unroll
  for (int i = 0; i < N; ++i) a[i] = x[i] * x[i] * c1 + c0;
  mid(0);
  PIN();
#pragma unroll
  for (int i = 0; i < N; ++i) a[i] *= x[i];
  mid(1);
  PIN();
#pragma unroll
  for (int i = 0; i < N; ++i) a[i] = f2{__builtin_amdgcn_exp2f(a[i].x), __builtin_amdgcn_exp2f(a[i].y)};
  mid(2);
  PIN();
#pragma unroll
  for (int i = 0; i < N; ++i) a[i] = a[i] * den + den;
  PIN();
#pragma unroll
  for (int i = 0; i < N; ++i) a[i] = f2{__builtin_amdgcn_rcpf(a[i].x), __builtin_amdgcn_rcpf(a[i].y)};
  mid(3);
  PIN();
#pragma unroll
  for (int i = 0; i < N; ++i) a[i] *= x[i];
  PIN();
#pragma unroll
  for (int i = 0; i < N; ++i) o[i] = pk2(a[i].x, a[i].y);
}


static constexpr int RT_BYTES = 32 * 896;
template <bool WIDE> struct Job {
  static constexpr int NL = WIDE ? 7 : 5, RB = WIDE ? 896 : 640, PITCH = WIDE ? F_ * 2 : D_ * 2;
  v4u r[NL];
  unsigned dst;
  DI void load(int job, int w, const unsigned (&vo)[NL], const void* w1, const void* w2, const void* wx) {
    const bool big = WIDE || job >= 80;
    const int jj = WIDE ? job : (job >= 80 ? job - 80 : (job >= 40 ? job - 40 : job));
    const int gq = WIDE ? jj & 3 : jj & 1, ks = WIDE ? jj >> 2 : jj >> 1;
    const rsrc_t rs = mkrs(big ? wx : (job >= 40 ? w2 : w1), big ? D_ * F_ * 2 : D_ * D_ * 2);
    const unsigned src = (32 * ks + 8 * w) * PITCH + gq * RB;
    dst = (WIDE ? O_W3 : (job >= 80 ? O_W4 : (job >= 40 ? O_W2 : O_W1))) + ((gq * 4 + w) * (!WIDE && job >= 80 ? 56 : 20) + ks) * (NL * 1024);
#pragma unroll
    for (int i = 0; i < NL; ++i)
      r[i] = ld128(rs, (WIDE && gq == 3 && (vo[i] & 1)) ? OOB : (vo[i] & ~1u) + src, 0);
  }
  DI void put(int w, int lane, LAS char* L) {
#pragma unroll
    for (int i = 0; i < NL; ++i) *(LAS v4u*)(L + w * (8 * RB) + lane * 16 + i * 1024) = r[i];
  }
  DI void emit(int w, int lane, LAS char* L, rsrc_t ws) {
    const int g = lane >> 4, q = (lane & 15) >> 2, p = lane & 3;
    LAS char* row = L + (8 * g + q) * RB + w * (RB / 4);
    LAS char* pr = row + 32 * (w & 1) + 16 * p;
    LAS char* sg = row + ((w & 1) ? 0 : RB / 4 - 32) + 8 * p;
    v2u lo[NL], hi[NL];
#pragma unroll
    for (int f = 0; f < NL; ++f) {
      LAS char* c = f < NL - 1 ? pr + 64 * (f >> 1) + 8 * (f & 1) : sg;
      lo[f] = __builtin_bit_cast(v2u, __builtin_amdgcn_ds_read_tr16_b64_v4i16((LAS s4v*)c));
      hi[f] = __builtin_bit_cast(v2u, __builtin_amdgcn_ds_read_tr16_b64_v4i16((LAS s4v*)(c + 4 * RB)));
    }
#pragma unroll
    for (int f = 0; f < NL; ++f) hst128(v4u{lo[f].x, lo[f].y, hi[f].x, hi[f].y}, ws, lane * 16 + dst, f * 1024);
  }
};

template <class Extra, class Gap>
DI void gemm_ks(rsrc_t ws, unsigned abase, const unsigned (&vo)[4], const v4u* b, LAS char* L, int w, int lane, f4 (&acc)[5],
                Extra extra, Gap gap) {
  constexpr int KS = 5, WIN = 12, NA = 4 * KS, RING = WIN + 1;
  v4u a[RING];
  unsigned so[4];
#pragma unroll
  for (int i = 0; i < 4; ++i) so[i] = abase + ((w + 1 + i) & 3) * 20480;
  auto lda = [&](int n) { a[n % RING] = hld128(ws, vo[n / KS] + ((n % KS) & 3) * 1024, so[n / KS] + ((n % KS) >> 2) * 4096); };
#pragma unroll
  for (int n = 0; n < WIN; ++n) {
    lda(n);
    PIN();
  }
  f4 ac[2][5], p[15];
  unsigned lr = RED_OFF + lane * 16;
  KEEP(lr);
  sfor<0, NA>([&](auto ic) {
    constexpr int n = decltype(ic)::v, i = n / KS, k = n % KS;
    if (n == NA - 3) {
      WGBAR();
    }
#pragma unroll
    for (int t = 0; t < 5; ++t) {
      if (k == 0) mfma_av0(ac[i & 1][t], b[t], a[n % RING]);
      else mfma_av(ac[i & 1][t], b[k * 5 + t], a[n % RING]);
      if (t == 1) {
        if constexpr (n + WIN < NA) lda(n + WIN);
      }
      if (t == (n + WIN < NA ? 4 : 1) && n >= NA - 12) extra(n - (NA - 12));
      if (k == 0 && i > 0 && t > 0) *(LAS f4*)(L + (lr + ((w + i) & 3) * 15360) + ((i - 1) * 5 + t - 1) * 1024) = ac[(i - 1) & 1][t - 1];
      if (k == 1 && i > 0 && t == 0) *(LAS f4*)(L + (lr + ((w + i) & 3) * 15360) + ((i - 1) * 5 + 4) * 1024) = ac[(i - 1) & 1][4];
      if (n == NA - 3) {
        p[t] = *(LAS f4*)(L + (lr + w * 15360) + t * 1024);
        p[5 + t] = *(LAS f4*)(L + (lr + w * 15360) + (5 + t) * 1024);
      }
      if (n == NA - 2) p[10 + t] = *(LAS f4*)(L + (lr + w * 15360) + (10 + t) * 1024);
      if (n == NA - 1) {
        p[t][0] = add1(p[t][0], p[5 + t][0]);
        p[t][1] = add1(p[t][1], p[5 + t][1]);
        KEEP(p[t]);
      }
      gap(n, t);
      PIN();
    }
  });
#pragma unroll
  for (int t = 0; t < 5; ++t) {
    p[t][2] += p[5 + t][2];
    p[t][3] += p[5 + t][3];
    p[t] += p[10 + t];
    KEEP(p[t]);
  }
  PIN();
#pragma unroll
  for (int t = 0; t < 5; ++t) acc[t] = ac[1][t] + p[t];
}

#define YOUNG() do { asm volatile("; YOUNG" ::: "memory"); PIN(); } while (0)
#define WAITV(n) asm volatile("s_waitcnt vmcnt(" #n ") ; WAITV" ::: "memory")
__global__ __launch_bounds__(256, 1) void fused(const void*, const void*, const void*, const void*, const void*, const void*,
                                                const void*, const void*, const void*, const void*, const void*, const void*,
                                                void*, void*) {
  extern __shared__ __attribute__((aligned(16))) char smem[];
  LAS char* L = (LAS char*)smem;
  unsigned pf = 0;
  if (blockIdx.x >= 160) {
    unsigned long long pc;
    unsigned off = (((blockIdx.x >> 3) & 1) * 256 + threadIdx.x) * 128;
    asm volatile("s_getpc_b64 %1\n.Lfused_pc:\n\tv_min_u32 %2, .Lfused_end-.Lfused_pc, %2\n\tglobal_load_dword %0, %2, %1"
                 : "=&v"(pf), "=&s"(pc), "+v"(off));
  }
  PIN();
  typedef void* const __attribute__((address_space(4))) * karg_t;
  const karg_t ka = (karg_t)__builtin_amdgcn_kernarg_segment_ptr();
  void *gq = ka[0], *gk = ka[1], *gv = ka[2], *gsk = ka[3], *gssm = ka[4], *gD = ka[5], *gw1 = ka[6], *gw2 = ka[7], *gw3 = ka[8],
       *gw4 = ka[9], *gc = ka[10], *gwm = ka[11], *gout = ka[12], *gws = ka[13];
  asm volatile("" ::"s"(gq), "s"(gk), "s"(gv), "s"(gsk), "s"(gssm), "s"(gD), "s"(gw1), "s"(gw2), "s"(gw3), "s"(gw4),
               "s"(gc), "s"(gwm), "s"(gout), "s"(gws));
  const int tid = threadIdx.x, lane = tid & 63;
  const int w = __builtin_amdgcn_readfirstlane(tid >> 6);
  const int b = blockIdx.x, x = b & 7, j = b >> 3;
  const int tm = 4 * x + (j >> 3), tn = j & 7;
  const int rg = 2 * x + (j >> 4), cg = j & 15;
  const bool conv_wg = j < 20;
  const rsrc_t ws = mkrs(gws, O_END);
  const int g = lane >> 4, l15 = lane & 15;

  const int rt0 = 64 * tm + 8 * tn + 2 * w;
  const int rcm = 8 * lane, rco = 512 + 2 * lane;
  v4u qm[2];
  unsigned qo[2];
  auto load_q = [&]() {
    const rsrc_t rq = mkrs(gq, SD2);
#pragma unroll
    for (int t = 0; t < 2; ++t) {
      qm[t] = ld128(rq, rcm * 2, (rt0 + t) * D_ * 2);
      qo[t] = ld32(rq, rco * 2, (rt0 + t) * D_ * 2);
    }
  };
  v2u tag;
  auto make_tag = [&](v2u ep) {
    tag.x = __builtin_amdgcn_readfirstlane(ep.x) * 0x9E3779B1u + 0x7F4A7C15u;
    tag.y = (__builtin_amdgcn_readfirstlane(ep.y) ^ tag.x) * 0x85EBCA77u + 0x2545F491u;
    tag.x &= 0xffffff00u;
    tag.y |= 1u;
  };
  v4u b1[25], b2[25];
  const unsigned sb1 = O_W1 + (tn * 100 + w * 25) * 1024, sb2 = O_W2 + (tn * 100 + w * 25) * 1024;
  auto zero_h1 = [&](int i) {
    hst128(v4u{0u, 0u, 0u, 0u}, ws, (i < 2 || tid < 128) ? tid * 16 : OOB, O_H1 + b * 10240 + i * 4096);
  };
  v4u mm[5];
  unsigned mo[5];
  auto load_mod = [&](bool ok) {
    unsigned o1 = (ok ? 0u : OOB) | (lane * 16), o2 = (ok ? 0u : OOB) | (lane * 4 + 1024);
    asm volatile("" : "+v"(o1), "+v"(o2));
#pragma unroll
    for (int i = 0; i < 5; ++i) {
      mm[i] = hld128(ws, o1, O_MOD + i * D_ * 2);
      mo[i] = __builtin_amdgcn_raw_buffer_load_b32(ws, o2, O_MOD + i * D_ * 2, 16);
    }
  };
  f2 qv[10], n1[10];
  auto ln1 = [&]() {
#pragma unroll
    for (int i = 0; i < 4; ++i) {
      qv[2 * i] = f2{lo2f(qm[0][i]), lo2f(qm[1][i])};
      qv[2 * i + 1] = f2{hi2f(qm[0][i]), hi2f(qm[1][i])};
    }
    qv[8] = f2{lo2f(qo[0]), lo2f(qo[1])};
    qv[9] = f2{hi2f(qo[0]), hi2f(qo[1])};
    f2 s = qv[0], ss = qv[0] * qv[0];
#pragma unroll
    for (int i = 1; i < 10; ++i) { s += qv[i]; ss += qv[i] * qv[i]; }
    wave_sum4(s, ss);
    f2 r, mr;
    ln_scal(s, ss, r, mr);
#pragma unroll
    for (int i = 0; i < 10; ++i) n1[i] = qv[i] * r + mr;
  };
  unsigned z3off, im0, io0, roff, roff3, own_r, x1m, x1s, stoff, h1m, h1s, f1pub, f1wait, frpub, frwait, f2pub, f2wait, f3pub, f3wait;
  const unsigned own_m = ((4 * tm + w) * 20 + 5 * (tn >> 1) + 3 * (tn & 1)) * 1024;
  const unsigned own_s = ((4 * tm + w) * 20 + 5 * (tn >> 1) + 2) * 1024 + (tn & 1) * 512;
  auto offsets = [&]() {
    z3off = tid < 128 ? tid * 16 + 8192 : OOB;
    im0 = RED_OFF + 16384 + lane * 128 + (((2 * w) ^ (lane & 7)) << 4);
    io0 = RED_OFF + 16384 + (64 + (lane >> 2)) * 128 + (((2 * w) ^ ((lane >> 2) & 7)) << 4) + 4 * (lane & 3);
    roff = O_A1 + ((4 * tm + (tn >> 1)) * 20 + (tid >> 5)) * 1024 + (((tid ^ (tid >> 3)) & 7) + 8 * (tn & 1) + 16 * ((tid >> 3) & 3)) * 16;
    roff3 = tid < 128 ? roff : OOB;
    own_r = (l15 + 16 * (g >> 1)) * 16 + 8 * (g & 1);
    x1m = O_X1 + own_m + lane * 16;
    x1s = (g & 1) ? OOB + SD2 : O_X1 + own_s + (l15 + 16 * (g >> 1)) * 16;
    stoff = (g & 1) ? OOB : O_STAT + ((4 * tm + w) * 8 + tn) * 128 + l15 * 8 + (g >> 1) * 4;
    f3pub = lane == 0 ? O_F3 + rg * 512 + (w >> 1) * 256 + cg * 16 + (w & 1) * 8 : OOB;
    f3wait = lane < 8 ? O_F3 + rg * 512 + (tm & 1) * 256 + w * 64 + lane * 8 : OOB;
    const int sh = (tn >> 1) == 0 ? 1 : ((tn >> 1) == 1 ? -1 : ((tn >> 1) == 2 ? 2 : -2));
    const int t2 = 64 * tm + 16 * w + l15 + sh;
    const bool ok = t2 >= 0 && t2 < S_;
    const unsigned d = O_H1 + ((t2 >> 4) * 20 + 5 * (tn >> 1)) * 1024 + (t2 & 15) * 16;
    h1m = ok ? d + 3072 * (tn & 1) + 256 * g : OOB;
    h1s = (ok && !(g & 1)) ? d + 2048 + 512 * (tn & 1) + 256 * (g >> 1) : OOB;
    const int nb = tm + (sh > 0 ? 1 : -1);
    f1pub = OOB;
    if (lane == 0) f1pub = O_F1 + tm * 128 + tn * 8;
    if (lane == 1 && nb >= 0 && nb < 32) f1pub = O_F1 + nb * 128 + 64 + tn * 8;
    const bool up = ((lane >> 1) & 1) == 0;
    f1wait = (lane < 8 || (lane < 16 && (up ? tm > 0 : tm < 31))) ? O_F1 + tm * 128 + lane * 8 : OOB;
    frpub = lane == 0 ? O_FR + tm * 128 + tn * 8 : OOB;
    frwait = lane < 8 ? O_FR + tm * 128 + lane * 8 : OOB;
    f2pub = lane == 0 ? O_F2 + (tm >> 1) * 128 + ((tm & 1) * 8 + tn) * 8 : OOB;
    f2wait = lane < 16 ? O_F2 + rg * 128 + lane * 8 : OOB;
    asm volatile("" : "+v"(frpub), "+v"(frwait), "+v"(f2pub), "+v"(f2wait));
    asm volatile("" : "+v"(z3off), "+v"(im0), "+v"(io0), "+v"(roff), "+v"(roff3), "+v"(own_r), "+v"(f1pub), "+v"(f1wait));
    asm volatile("" : "+v"(x1m), "+v"(x1s), "+v"(stoff), "+v"(f3pub), "+v"(f3wait));
    asm volatile("" : "+v"(h1m), "+v"(h1s));
  };
  constexpr int NB2 = 19;
  bool wok = false, bok = false;
  if (conv_wg) {
    const int cgp = j < 16 ? x : 8 + (x >> 2), ci = j < 16 ? j : (x & 3) * 4 + (j - 16);
    const int pT0 = 128 * ci + 32 * w + 8 * ((lane >> 3) & 3) + 4 * (lane >> 5), pC0 = 64 * cgp + 8 * (lane & 7);
    const int ch = 64 * cgp + 4 * ci + w;
    v4u kw[7], vw[4], skw[4], kr[4];
    {
      const rsrc_t rk = mkrs(gk, SD2), rv = mkrs(gv, SD2), rs = mkrs(gsk, D_ * 8);
      const unsigned o = (unsigned)(pT0 * D_ + pC0) * 2;
#pragma unroll
      for (int i = 0; i < 7; ++i) kw[i] = __builtin_amdgcn_raw_buffer_load_b128(rk, o + (i - 3) * D_ * 2, 0, 0);
#pragma unroll
      for (int i = 0; i < 4; ++i) vw[i] = ld128(rv, o + i * D_ * 2, 0);
#pragma unroll
      for (int i = 0; i < 4; ++i) skw[i] = __builtin_amdgcn_raw_buffer_load_b128(rs, pC0 * 8 + i * 16, 0, 0);
      PIN();
      offsets();
      PIN();
      asm volatile("s_waitcnt vmcnt(4)" ::: "memory");
      const rsrc_t rK = mkrs((const char*)gssm + (size_t)ch * 4096, 4096);
#pragma unroll
      for (int i = 0; i < 4; ++i) kr[i] = ld128(rK, lane * 16 + i * 1024, 0);
    }
    const v2u ep = hld64(ws, lane == 0 ? O_EPOCH : O_FP + x * 256 + (j >= 16) * 128, 0);
    const unsigned dvb = __builtin_amdgcn_raw_buffer_load_b16(mkrs(gD, D_ * 2), ch * 2, 0, 0);
    PIN();
    {
      unsigned pe[8][2];
#pragma unroll
      for (int pr = 0; pr < 4; ++pr) {
        const f2 w0 = {lo2f(skw[pr].x), lo2f(skw[pr].z)}, w1 = {hi2f(skw[pr].x), hi2f(skw[pr].z)};
        const f2 w2 = {lo2f(skw[pr].y), lo2f(skw[pr].w)}, w3 = {hi2f(skw[pr].y), hi2f(skw[pr].w)};
        f2 kf[7], o[4];
#pragma unroll
        for (int i = 0; i < 7; ++i) kf[i] = up2(kw[i][pr]);
#pragma unroll
        for (int i = 0; i < 4; ++i) o[i] = (kf[i + 3] * w0 + kf[i + 2] * w1 + kf[i + 1] * w2 + kf[i] * w3) * up2(vw[i][pr]);
        pe[2 * pr][0] = pk2(o[3].x, o[2].x);
        pe[2 * pr][1] = pk2(o[1].x, o[0].x);
        pe[2 * pr + 1][0] = pk2(o[3].y, o[2].y);
        pe[2 * pr + 1][1] = pk2(o[1].y, o[0].y);
      }
      const unsigned so = (pC0 + 4 * (lane >> 5)) * KVP + (pT0 & ~7) * 2;
#pragma unroll
      for (int e = 0; e < 4; ++e) {
        const auto s0 = __builtin_amdgcn_permlane32_swap(pe[e][0], pe[e + 4][0], false, false);
        const auto s1 = __builtin_amdgcn_permlane32_swap(pe[e][1], pe[e + 4][1], false, false);
        hst128(v4u{s0[1], s1[1], s0[0], s1[0]}, ws, so, O_KVT + e * KVP);
      }
    }
    PIN();
    LAS char* T = L + w * WR;
    if (lane < 8) *(LAS v4u*)(T + lane * CS) = v4u{0u, 0u, 0u, 0u};
#pragma unroll
    for (int i = 0; i < 4; ++i) *(LAS v4u*)(T + 7 * CS + (i * 64 + lane + 1) * 16) = kr[i];
    asm volatile("s_waitcnt lgkmcnt(0)" ::: "memory");
    v4u kl[4];
#pragma unroll
    for (int i = 0; i < 4; ++i) kl[i] = *(LAS v4u*)(T + 7 * CS + (i * 64 + lane) * 16);
    auto tbuild = [&](int i) {
      const unsigned d[8] = {kl[i].x, kl[i].y, kl[i].z, kl[i].w, kr[i].x, kr[i].y, kr[i].z, kr[i].w};
      unsigned a[7];
#pragma unroll
      for (int e = 0; e < 7; ++e) a[e] = __builtin_amdgcn_alignbit(d[e + 1], d[e], 16);
#pragma unroll
      for (int m = 0; m < 7; ++m) {
        const int h = (m + 1) >> 1;
        const v4u o = (m & 1) ? v4u{d[h], d[h + 1], d[h + 2], d[h + 3]} : v4u{a[h], a[h + 1], a[h + 2], a[h + 3]};
        *(LAS v4u*)(T + m * CS + (i * 64 + lane + 1) * 16) = o;
      }
    };
    tbuild(0);
    make_tag(ep);
    DRAIN();
    asm volatile("s_barrier" ::: "memory");
    if (w == 0) {
      unsigned o = OOB;
      if (j < 16 && lane == 0) o = O_FP + x * 256 + ci * 8;
      if (j >= 16 && lane < 4) o = O_FP + ((x & 4) + lane) * 256 + 128 + ci * 8;
      hst64(tag, ws, o, 0);
    }
    tbuild(1);
    tbuild(2);
    tbuild(3);
    if (lane < 32) *(LAS v4u*)(T + UOFF + lane * 16) = v4u{0u, 0u, 0u, 0u};
    if (w == 0) poll64(ws, lane < 16 ? O_FP + x * 256 + (j >= 16) * 128 + lane * 8 : OOB, tag);
    WGBAR();
    const v4u wf = hld128(ws, O_FW + x * 1024 + lane * 16, 0);
    v4u ur[4];
#pragma unroll
    for (int i = 0; i < 4; ++i) ur[i] = hld128(ws, lane * 16 + i * 1024, O_KVT + ch * KVP);
    const LAS char* pa = T + (l15 & 7) * CS + (3 - g + (l15 >> 3)) * 16;
    const LAS char* pv = T + UOFF + 512 + (l15 - 1) * 32 + g * 16;
    v4u fa[2][8], fv[2][8];
#pragma unroll
    for (int a = 0; a < 8; ++a) fa[0][a] = *(const LAS v4u*)(pa + 8 * a * 64);
    f4 acc[8];
    v4u ad;
    {
      const int jd = 8 * g - 9 - l15;
#pragma unroll
      for (int e = 0; e < 4; ++e) ad[e] = jd == 2 * e ? dvb : (jd == 2 * e + 1 ? dvb << 16 : 0u);
      asm volatile("" : "+v"(ad));
    }
    PIN();
#pragma unroll
    for (int i = 0; i < 4; ++i) *(LAS v4u*)(T + UOFF + 512 + i * 1024 + lane * 16) = ur[i];
    asm volatile("s_waitcnt lgkmcnt(0)" ::: "memory");
#pragma unroll
    for (int a = 0; a < 8; ++a) fv[0][a] = *(const LAS v4u*)(pv + a * 512);
    wok = wflags_ok(wf, lane, 40, tag);
    __builtin_amdgcn_fence(__ATOMIC_ACQUIRE, "wavefront");
    const unsigned boff = wok ? lane * 16 : OOB;
#pragma unroll
    for (int q = 0; q < 8; ++q) mfma_v0(acc[q], ad, fv[0][q]);
    PIN();
#pragma unroll
    for (int g0 = 0; g0 < 8; ++g0) {
      int n = 0;
      if (g0 == 0) load_q();
#pragma unroll
      for (int a = 0; a < 8; ++a)
#pragma unroll
        for (int q = a; q < 8; ++q) {
          mfma_v(acc[q], fa[g0 & 1][a], fv[g0 & 1][q - a]);
          ++n;
          if ((n & 1) == 0) {
            const int e = n / 2 - 1;
            if (g0 < 7 && e < 16) {
              if (e < 8) fv[(g0 + 1) & 1][e] = *(const LAS v4u*)(pv + e * 512 - (g0 + 1) * 64);
              else fa[(g0 + 1) & 1][e - 8] = *(const LAS v4u*)(pa + (g0 + 1 + 8 * (e - 8)) * 64);
            }
            PIN();
          }
          if (n % 5 == 3) {
            const int i = g0 * 7 + n / 5 - 12;
            if (i < -9) zero_h1(i + 12);
            if (i >= 0 && i < 25) b1[i] = hld128(ws, boff + (i & 3) * 1024, sb1 + (i >> 2) * 4096);
            if (i >= 25 && i < 25 + NB2) b2[i - 25] = hld128(ws, boff + ((i - 25) & 3) * 1024, sb2 + ((i - 25) >> 2) * 4096);
            PIN();
          }
        }
    }
    const v4u bfl = hld128(ws, O_FB + x * 1024 + lane * 16, 0);
    MFENCE();
    PIN();
#pragma unroll
    for (int q = 0; q < 8; q += 2) {
      const auto s0 = __builtin_amdgcn_permlane16_swap(pk2(acc[q][0], acc[q][1]), pk2(acc[q + 1][0], acc[q + 1][1]), false, false);
      const auto s1 = __builtin_amdgcn_permlane16_swap(pk2(acc[q][2], acc[q][3]), pk2(acc[q + 1][2], acc[q + 1][3]), false, false);
      *(LAS v4u*)(T + 64 * w + (32 * (q + (g & 1)) + 2 * l15 + (g >> 1)) * 16) = v4u{s0[0], s1[0], s0[1], s1[1]};
    }
    WGBAR();
    {
      v4u zo[4];
#pragma unroll
      for (int i = 0; i < 4; ++i) zo[i] = *(LAS v4u*)(L + (lane & 3) * (WR + 64) + (64 * w + 16 * i + (lane >> 2)) * 16);
#pragma unroll
      for (int i = 0; i < 4; ++i) hst128(zo[i], ws, (lane >> 2) * ZP + (lane & 3) * 16, O_ZT + (64 * w + 16 * i) * ZP + (ch - w) * 16);
    }
    YOUNG();
    bok = wflags_ok(bfl, lane, 48, tag);
    __builtin_amdgcn_fence(__ATOMIC_ACQUIRE, "wavefront");
    load_mod(bok);
    PIN();
    ln1();
    PIN();
    WAITV(10);
  } else {
    const int hb = (j - 20) * 8 + x;
    unsigned vn[5];
#pragma unroll
    for (int i = 0; i < 5; ++i) vn[i] = (i * 64 + lane) * 16 + (((i * 64 + lane) * 1639u) >> 16) * 640;
    Job<false> j0, j1;
    j0.load(hb, w, vn, gw1, gw2, gw4);
    const v2u ep = hld64(ws, O_EPOCH, 0);
    const v2u cw = __builtin_amdgcn_raw_buffer_load_b64(mkrs(gc, D_ * 2), tid * 8, 0, 0);
    PIN();
    unsigned vw[7];
#pragma unroll
    for (int i = 0; i < 7; ++i) {
      const unsigned c = i * 64 + lane, row = (c * 1171u) >> 16;
      vw[i] = c * 16 + row * 2560 + (c - row * 56 >= 48);
      KEEP(vw[i]);
    }
    offsets();
    LAS float* sc = (LAS float*)(L + 4 * RT_BYTES);
    {
      const float c4[4] = {lo2f(cw.x), hi2f(cw.x), lo2f(cw.y), hi2f(cw.y)};
      f4 sv;
#pragma unroll
      for (int e = 0; e < 4; ++e) sv[e] = rbf(c4[e] * __builtin_amdgcn_rcpf(1.f + __builtin_amdgcn_exp2f(-1.4426950408889634f * c4[e])));
      *(LAS f4*)(sc + tid * 4) = sv;
    }
    j0.put(w, lane, L);
    make_tag(ep);
    WGBAR();
    j0.emit(w, lane, L, ws);
    if (j == 20 && w == 0) hst128(v4u{tag.x, tag.y, 0u, 0u}, ws, (lane >> 3) * 1024 + (lane & 7) * 16, O_CNT + x * 128);
    asm volatile("" ::"v"(pf) : "memory");
    auto first_pub = [&]() {
      WGBAR();
      if (w == 0) hst64(tag, ws, lane < 8 ? O_FW + lane * 1024 + hb * 8 : OOB, 0);
    };
    if (hb < 60) {
      const rsrc_t rm = mkrs(gwm, D_ * 6 * D_ * 2);
      v4u wm[20];
      YOUNG();
#pragma unroll
      for (int i = 0; i < 8; ++i) wm[i] = ld128(rm, (tid >> 3) * (6 * D_ * 2) + (tid & 7) * 16, hb * 128 + i * 32 * (6 * D_ * 2));
      PIN();
      WAITV(8);
      first_pub();
#pragma unroll
      for (int i = 8; i < 20; ++i) wm[i] = ld128(rm, (tid >> 3) * (6 * D_ * 2) + (tid & 7) * 16, hb * 128 + i * 32 * (6 * D_ * 2));
      j1.load(132 + hb, w, vn, gw1, gw2, gw4);
      load_q();
      float sl[20];
#pragma unroll
      for (int i = 0; i < 20; ++i) sl[i] = sc[(tid >> 3) + 32 * i];
      PIN();
      f2 a[4] = {f2{0.f, 0.f}, f2{0.f, 0.f}, f2{0.f, 0.f}, f2{0.f, 0.f}};
#pragma unroll
      for (int i = 0; i < 20; ++i)
#pragma unroll
        for (int e = 0; e < 4; ++e) a[e] += up2(wm[i][e]) * sl[i];
      LAS char* mred = L + 4 * RT_BYTES + 4096;
      *(LAS f4*)(mred + (tid >> 3) * 272 + (tid & 7) * 32) = f4{a[0].x, a[0].y, a[1].x, a[1].y};
      *(LAS f4*)(mred + (tid >> 3) * 272 + (tid & 7) * 32 + 16) = f4{a[2].x, a[2].y, a[3].x, a[3].y};
      WGBAR();
      {
        const int col = tid >> 2, part = tid & 3;
        float v[8];
#pragma unroll
        for (int r = 0; r < 8; ++r) v[r] = *(LAS float*)(mred + (part * 8 + r) * 272 + col * 4);
        float s = ((v[0] + v[1]) + (v[2] + v[3])) + ((v[4] + v[5]) + (v[6] + v[7]));
        s += dpp<0xB1>(s);
        s += dpp<0x4E>(s);
        s = rbf(s);
        if ((hb >= 10 && hb < 20) || (hb >= 40 && hb < 50)) s = rbf(1.f + s);
        if (part == 0) *((LAS unsigned short*)sc + 8 + col) = (unsigned short)(__builtin_bit_cast(unsigned, s) >> 16);
      }
      j1.put(w, lane, L);
      WGBAR();
      if (w == 0) {
        hst128(*(LAS v4u*)((LAS char*)sc + 16 + (lane & 7) * 16), ws, lane < 8 ? lane * 16 : OOB, O_MOD + hb * 128);
        DRAIN();
        hst64(tag, ws, lane < 8 ? O_FB + lane * 1024 + hb * 8 : OOB, 0);
      }
    } else {
      Job<true> ja, jb, jc;
      YOUNG();
      ja.load(hb - 60, w, vw, gw1, gw2, gw3);
      PIN();
      WAITV(7);
      first_pub();
      jb.load(hb - 24, w, vw, gw1, gw2, gw3);
      if (hb < 68) jc.load(hb + 12, w, vw, gw1, gw2, gw3);
      j1.load(36 + hb, w, vn, gw1, gw2, gw4);
      load_q();
      PIN();
      ja.put(w, lane, L + RT_BYTES);
      jb.put(w, lane, L + 2 * RT_BYTES);
      if (hb < 68) jc.put(w, lane, L + 3 * RT_BYTES);
      WGBAR();
      ja.emit(w, lane, L + RT_BYTES, ws);
      jb.emit(w, lane, L + 2 * RT_BYTES, ws);
      if (hb < 68) jc.emit(w, lane, L + 3 * RT_BYTES, ws);
      j1.put(w, lane, L);
      DRAIN();
      WGBAR();
      if (w == 0) hst64(tag, ws, lane < 8 ? O_FB + lane * 1024 + hb * 8 : OOB, 0);
    }
    pollw(ws, O_FW + x * 1024 + lane * 16, lane, 40, tag);
    j1.emit(w, lane, L, ws);
    zero_h1(0);
    zero_h1(1);
    zero_h1(2);
    if (hb >= 34 && hb < 60) hst128(v4u{0u, 0u, 0u, 0u}, ws, w == 0 ? lane * 16 : OOB, O_FR + (hb - 34) * 1024);
    YOUNG();
#pragma unroll
    for (int i = 0; i < 10; ++i) b1[i] = hld128(ws, lane * 16, sb1 + i * 1024);
    PIN();
    ln1();
    PIN();
    WAITV(10);
  }
  if (conv_wg && !wok) pollw(ws, O_FW + x * 1024 + lane * 16, lane, 40, tag);
  WGBAR();
  if (w == 0 && lane < 8)
    __hip_atomic_fetch_add((GAS unsigned*)((GAS char*)gws + O_CNT + (lane * 8 + x) * 128), 1u, __ATOMIC_RELAXED, __HIP_MEMORY_SCOPE_AGENT);
  if (!wok) {
    if (conv_wg) {
#pragma unroll
      for (int i = 0; i < 10; ++i) b1[i] = hld128(ws, lane * 16, sb1 + i * 1024);
    }
#pragma unroll
    for (int i = 10; i < 25; ++i) b1[i] = hld128(ws, lane * 16, sb1 + i * 1024);
#pragma unroll
    for (int i = 0; i < NB2; ++i) b2[i] = hld128(ws, lane * 16, sb2 + i * 1024);
  }
#pragma unroll
  for (int i = NB2; i < 25; ++i) b2[i] = hld128(ws, lane * 16, sb2 + i * 1024);
  v4u b3[35], b4[70];
  const unsigned sb3 = O_W3 + (cg * 140 + w * 35) * 1024, sb4 = O_W4 + (tn * 280 + w * 70) * 1024;
#define LB3(lo, hi) _Pragma("unroll") for (int i_ = lo; i_ < hi; ++i_) b3[i_] = hld128(ws, lane * 16 + (i_ & 3) * 1024, sb3 + (i_ >> 2) * 4096)
  auto ks4 = [](int s) { return s < 8 ? 7 * (s >> 2) + (s & 1) + 4 * ((s >> 1) & 1) : 7 * ((s - 8) / 3) + ((s - 8) % 3 == 2 ? 6 : 2 + (s - 8) % 3); };
  auto lb4 = [&](int i) {
    const int f = ks4(i / 5) * 5 + i % 5;
    b4[i] = hld128(ws, lane * 16 + (f & 3) * 1024, sb4 + (f >> 2) * 4096);
  };
#define LB4(lo, hi) _Pragma("unroll") for (int i_ = lo; i_ < hi; ++i_) lb4(i_)
  if (!bok) {
    pollw(ws, O_FB + x * 1024 + lane * 16, lane, 48, tag);
    load_mod(true);
  }
  constexpr int NB3C = 10;
  f2 gh[10];
  float cb1[10], sbv[10];
  auto md = [&](int v, int i) { const unsigned u = i < 8 ? mm[v][(i >> 1) & 3] : mo[v]; return (i & 1) ? hi2f(u) : lo2f(u); };
  auto use_mod = [&]() {
    LB3(0, NB3C);
#pragma unroll
    for (int i = 0; i < 10; ++i) {
      gh[i] = (n1[i] * md(1, i) + md(0, i)) * md(2, i);
      cb1[i] = md(4, i);
      sbv[i] = md(3, i);
      asm volatile("" : "+v"(gh[i]), "+v"(cb1[i]), "+v"(sbv[i]));
    }
  };
  use_mod();
  PIN();
  if (w == 0) {
    poll64(ws, lane < 8 ? O_CNT + (x * 8 + lane) * 128 : OOB, v2u{tag.x + 32, tag.y});
    if (b == 0) hst64(tag, ws, lane == 0 ? O_EPOCH : OOB, 0);
  }
  WGBAR();

  v4u hx[6];
  f4 csp[7];
  auto img3 = [&](int lo, int hi, int hc) {
    const v4u ones = {0x3f803f80u, 0x3f803f80u, 0x3f803f80u, 0x3f803f80u};
    unsigned la = w * 25600 + lane * 16, lb = (75 + w * 10) * 1024 + lane * 16;
    asm volatile("" : "+v"(la), "+v"(lb));
#pragma unroll
    for (int i = lo; i < 35; ++i) {
      if (i < hi) *(LAS v4u*)(L + (i < 25 ? la : lb) + i * 1024) = b3[i];
      if (i < hc) csp[i % 7] = mfma(b3[i], ones, i < 7 ? f4{0.f, 0.f, 0.f, 0.f} : csp[i % 7]);
      PIN();
    }
  };
  {
    v4u zc[3];
#pragma unroll
    for (int i = 0; i < 3; ++i) zc[i] = hld128(ws, i < 2 ? tid * 16 + i * 4096 : z3off, O_ZT + (8 * tm + tn) * ZP);
    PIN();
#pragma unroll
    for (int i = 0; i < 3; ++i)
#pragma unroll
      for (int e = 0; e < 4; ++e) *(LAS unsigned*)(L + RED_OFF + e * 3072 + (tid + 256 * i) * 4) = zc[i][e];
    WGBAR();
    const v4u z0 = *(LAS v4u*)(L + RED_OFF + w * 3072 + rcm * 4), z1 = *(LAS v4u*)(L + RED_OFF + w * 3072 + rcm * 4 + 16);
    const v2u z2 = *(LAS v2u*)(L + RED_OFF + w * 3072 + rco * 4);
    LB3(10, 19);
    f2 xv[10], hv[10], av[10], s, ss, r, mr;
#pragma unroll
    for (int i = 0; i < 10; ++i) {
      xv[i] = gh[i] * up2(i < 4 ? z0[i & 3] : (i < 8 ? z1[i & 3] : z2[i & 1])) + qv[i];
      s = i ? s + xv[i] : xv[i];
      ss = i ? ss + xv[i] * xv[i] : xv[i] * xv[i];
    }
    wave_sum4(s, ss);
    ln_scal(s, ss, r, mr);
#pragma unroll
    for (int i = 0; i < 10; ++i) {
      hv[i] = (xv[i] * r + mr) * cb1[i] + sbv[i];
      s = i ? s + hv[i] : hv[i];
      ss = i ? ss + hv[i] * hv[i] : hv[i] * hv[i];
    }
    wave_sum4(s, ss);
    ln_scal(s, ss, r, mr);
#pragma unroll
    for (int i = 0; i < 10; ++i) av[i] = hv[i] * r + mr;
    auto put = [&](const f2(&v)[10], int img) {
      *(LAS v4u*)(L + im0 + img * 10240) = v4u{pk2(v[0].x, v[1].x), pk2(v[2].x, v[3].x), pk2(v[4].x, v[5].x), pk2(v[6].x, v[7].x)};
      *(LAS v4u*)(L + (im0 ^ 16) + img * 10240) = v4u{pk2(v[0].y, v[1].y), pk2(v[2].y, v[3].y), pk2(v[4].y, v[5].y), pk2(v[6].y, v[7].y)};
      *(LAS unsigned*)(L + io0 + img * 10240) = pk2(v[8].x, v[9].x);
      *(LAS unsigned*)(L + (io0 ^ 16) + img * 10240) = pk2(v[8].y, v[9].y);
    };
    put(av, 0);
    WGBAR();
    {
      v4u oc[3];
#pragma unroll
      for (int i = 0; i < 3; ++i) oc[i] = *(LAS v4u*)(L + RED_OFF + 16384 + i * 4096 + tid * 16);
#pragma unroll
      for (int i = 0; i < 3; ++i) hst128(oc[i], ws, i < 2 ? roff : roff3, i * 8192);
    }
    PIN();
    put(hv, 1);
    put(xv, 2);
    DRAIN();
    WGBAR();
    if (w == 0) hst64(tag, ws, frpub, 0);
    LB3(19, 28);
    PIN();
#pragma unroll
    for (int i = 0; i < 6; ++i) hx[i] = *(LAS v4u*)(L + RED_OFF + 26624 + (i / 3) * 10240 + (i % 3) * 4096 + tid * 16);
    img3(0, 16, 16);
    if (w == 0) poll64(ws, frwait, tag);
    WGBAR();
  }

  {
    f4 acc[5];
    const unsigned vo[4] = {(unsigned)lane * 16, (unsigned)lane * 16, (unsigned)lane * 16, (unsigned)lane * 16};
    gemm_ks(ws, O_A1 + (4 * tm * 20 + 5 * w) * 1024, vo, b1, L, w, lane, acc,
            [&](int n) { if (n < 7) { LB3(28 + n, 29 + n); } else lb4(16 + n); },
            [&](int n, int t) {
              if (t == 3 && n >= 8 && n < 14) hst128(hx[n - 8], ws, ((n - 8) % 3) < 2 ? roff : roff3, (1 + (n - 8) / 3) * SD2 + ((n - 8) % 3) * 8192);
            });
    f2 v[10];
    unsigned o[10];
#pragma unroll
    for (int t = 0; t < 5; ++t) {
      v[2 * t] = f2{acc[t][0], acc[t][1]};
      v[2 * t + 1] = f2{acc[t][2], acc[t][3]};
    }
    gelu_pack<10>(v, 1.f, 1.f, o, [](int) {});
    const auto s0 = __builtin_amdgcn_permlane16_swap(o[8], o[8], false, false);
    const auto s1 = __builtin_amdgcn_permlane16_swap(o[9], o[9], false, false);
    hst128(v4u{o[0], o[1], o[2], o[3]}, ws, h1m, 0);
    hst128(v4u{o[4], o[5], o[6], o[7]}, ws, h1m + 1024, 0);
    hst128(v4u{s0[0], s1[0], s0[1], s1[1]}, ws, h1s, 0);
    DRAIN();
    WGBAR();
    if (w == 0) hst64(tag, ws, f1pub, 0);
    LB4(0, 9);
    PIN();
    img3(16, 24, 35);
    if (l15 == 0) {
#pragma unroll
      for (int t = 0; t < 7; ++t) *(LAS f4*)(L + (w * 25 + 24) * 1024 + (t * 4 + g) * 16) = csp[t];
    }
    if (w == 0) poll64(ws, f1wait, tag);
    WGBAR();
  }
  v4u xr[2];
  v2u xro;
  unsigned x1p[10];
  {
    v4u hr[2];
    v2u hro;
    f4 acc[5];
    f2 hf[10];
    const unsigned vo[4] = {(unsigned)lane * 16, (unsigned)lane * 16, (unsigned)lane * 16, (unsigned)lane * 16};
    gemm_ks(ws, O_H1 + (4 * tm * 20 + 5 * w) * 1024, vo, b2, L, w, lane, acc, [&](int n) {
      lb4(n < 5 ? 9 + n : 28 + n);
    }, [&](int n, int t) {
      if (t == 3 && n == 2) hr[0] = hld128(ws, lane * 16, O_HP + own_m);
      if (t == 3 && n == 4) hr[1] = hld128(ws, lane * 16 + 1024, O_HP + own_m);
      if (t == 3 && n == 6) hro = hld64(ws, own_r, O_HP + own_s);
      if (n == 18) {
        unsigned h0 = t < 4 ? hr[t >> 1][2 * (t & 1)] : hro.x;
        KEEP(h0);
        hf[2 * t] = up2(h0);
        KEEP(hf[2 * t]);
      }
    });
#pragma unroll
    for (int t = 0; t < 5; ++t) {
      const unsigned h1 = t < 4 ? hr[t >> 1][2 * (t & 1) + 1] : hro.y;
      const f2 p = f2{acc[t][0], acc[t][1]} + hf[2 * t], q = f2{acc[t][2], acc[t][3]} + up2(h1);
      x1p[2 * t] = pk2(p.x, p.y);
      x1p[2 * t + 1] = pk2(q.x, q.y);
    }
    const auto s0 = __builtin_amdgcn_permlane16_swap(x1p[8], x1p[8], false, false);
    const auto s1 = __builtin_amdgcn_permlane16_swap(x1p[9], x1p[9], false, false);
    hst128(v4u{x1p[0], x1p[1], x1p[2], x1p[3]}, ws, x1m, 0);
    hst128(v4u{x1p[4], x1p[5], x1p[6], x1p[7]}, ws, x1m + 1024, 0);
    hst128(v4u{s0[0], s1[0], s0[1], s1[1]}, ws, x1s, 0);
    float t1 = 0.f, t2 = 0.f;
#pragma unroll
    for (int i = 0; i < 10; ++i) {
      t1 = __builtin_amdgcn_fdot2_f32_bf16(__builtin_bit_cast(bf2, x1p[i]), __builtin_bit_cast(bf2, 0x3f803f80u), t1, false);
      t2 = __builtin_amdgcn_fdot2_f32_bf16(__builtin_bit_cast(bf2, x1p[i]), __builtin_bit_cast(bf2, x1p[i]), t2, false);
    }
    t1 = swap32_add(t1, t2);
    t1 = swap16_add(t1, t1);
    hst32(__builtin_bit_cast(unsigned, t1), ws, stoff, 0);
    f4 cst;
    {
      f4 cp[4];
#pragma unroll
      for (int v = 0; v < 4; ++v) cp[v] = *(LAS f4*)(L + (v * 25 + 24) * 1024 + lane * 16);
      cst = (cp[0] + cp[1]) + (cp[2] + cp[3]);
      KEEP(cst);
    }
    PIN();
    DRAIN();
    WGBAR();
    if (w == 0) hst64(tag, ws, f2pub, 0);
    LB4(14, 23);
    PIN();
    img3(24, 35, 0);
    if (w == 1) *(LAS f4*)(L + CSUM_OFF + lane * 16) = cst;
    if (w == 0) poll64(ws, f2wait, tag);
    WGBAR();
  }
  constexpr int WIN4 = 8, RING4 = WIN4 + 1, PUBK = 10, PUBN = 9, SMPN = 20, CHKN = 26;
  v4u a4[RING4];
  auto lda4 = [&](int n) {
    const int i = n < 32 ? n >> 3 : (n - 32) / 6, k = ks4(n < 32 ? n & 7 : 8 + (n - 32) % 6);
    a4[n % RING4] = hld128(ws, lane * 16 + (k & 3) * 1024, O_H + ((4 * tm + ((w + 1 + i) & 3)) * 56 + 14 * w + (k & ~3)) * 1024);
  };
  {
    constexpr int W3 = 6;
    v4u a[20][2];
    const unsigned sa = O_X1 + (8 * rg + 2 * w) * 20 * 1024;
    auto lda = [&](int k, int i) { a[k][i] = hld128(ws, lane * 16 + (k & 3) * 1024, sa + i * 20480 + (k >> 2) * 4096); };
#pragma unroll
    for (int k = 0; k < W3; ++k)
#pragma unroll
      for (int i = 0; i < 2; ++i) {
        lda(k, i);
        PIN();
      }
    f4 acc[2][7];
    v2u sv[2][2];
    f2 r, den, mean;
    auto bslot = [](int k, int t) { const int v = k / 5, i = (k % 5) * 7 + t; return (i < 25 ? v * 25 + i : 75 + v * 10 + i) * 1024; };
    const LAS char* Lb = L + lane * 16;
    const LAS char* Lb2 = L + lane * 16 + 65536;
    const LAS char* Lb3 = L + lane * 16 + 131072;
    auto rdb = [&](int k, int t) { const int o = bslot(k, t); return *(const LAS v4u*)(o < 65536 ? Lb + o : (o < 131072 ? Lb2 + (o - 65536) : Lb3 + (o - 131072))); };
    v4u bf[3][4];
#pragma unroll
    for (int t = 0; t < 8; ++t) {
      bf[t >> 2][t & 3] = rdb(t >> 2, t & 3);
      PIN();
    }
    sfor<0, 20>([&](auto ic) {
      constexpr int k = decltype(ic)::v;
      __builtin_amdgcn_s_waitcnt(k < 19 ? 0xC47F : 0xC37F);
#pragma unroll
      for (int t = 0; t < 4; ++t) {
        if (k == 0) mfma_va0(acc[0][t], bf[0][t], a[0][0]);
        else mfma_va(acc[0][t], bf[k % 3][t], a[k][0]);
        if (k < 18) bf[(k + 2) % 3][t] = rdb(k + 2, t);
        else if (t < 3) bf[(k + 2) % 3][t] = rdb(k - 18, 4 + t);
        PIN();
      }
#pragma unroll
      for (int t = 0; t < 4; ++t) {
        if (k == 0) mfma_va0(acc[1][t], bf[0][t], a[0][1]);
        else mfma_va(acc[1][t], bf[k % 3][t], a[k][1]);
        if (t < 2) {
          if constexpr (k + W3 < 20) lda(k + W3, t);
        }
        if (t >= (k + W3 < 20 ? 2 : 0) && t < (k + W3 < 20 ? 4 : 2)) {
          if constexpr (k == 14 || k == 15) sv[k - 14][t & 1] = hld64(ws, l15 * 8 + g * 256 + (t & 1) * 128, O_STAT + (8 * rg + 2 * w + k - 14) * 1024);
          else if constexpr (k >= 16) lb4(40 + 2 * (k - 16) + (t & 1));
        }
        PIN();
      }
    });
    const unsigned hs = O_H + ((8 * rg + 2 * w) * 56 + 7 * (cg >> 1) + 4 * (cg & 1)) * 1024 + lane * 16;
    unsigned lcs = CSUM_OFF + g * 16;
    KEEP(lcs);
#pragma unroll
    for (int t = 0; t < 4; ++t) csp[t] = *(LAS f4*)(L + lcs + t * 64);
    MFENCE();
    PIN();
    {
      asm volatile("" : "+v"(sv[0][0]), "+v"(sv[0][1]), "+v"(sv[1][0]), "+v"(sv[1][1]));
      const f2 p0 = __builtin_bit_cast(f2, sv[0][0]) + __builtin_bit_cast(f2, sv[0][1]);
      const f2 p1 = __builtin_bit_cast(f2, sv[1][0]) + __builtin_bit_cast(f2, sv[1][1]);
      f2 mr;
      ln_scal(f2{xrow_sum(p0.x), xrow_sum(p1.x)}, f2{xrow_sum(p0.y), xrow_sum(p1.y)}, r, mr, &mean, &den);
    }
#pragma unroll
    for (int i = 0; i < 2; ++i) {
      f2 v[8];
      unsigned o[8];
#pragma unroll
      for (int e = 0; e < 8; ++e) {
        const int t = e >> 1, h = 2 * (e & 1);
        v[e] = f2{acc[i][t][h], acc[i][t][h + 1]} - f2{csp[t][h], csp[t][h + 1]} * mean[i];
      }
      gelu_pack<8>(v, r[i], den[i], o, [&](int m) {
        if (i == 0) lb4(28 + m);
        else if (m == 0) lb4(32);
      });
      hst128(v4u{o[0], o[1], o[2], o[3]}, ws, hs, i * 57344);
      hst128(v4u{o[4], o[5], o[6], o[7]}, ws, hs, i * 57344 + 1024);
    }
    YOUNG();
    sfor<0, 20>([&](auto ic) {
      constexpr int k = decltype(ic)::v;
      __builtin_amdgcn_s_waitcnt(k < 19 ? 0xC37F : 0xC07F);
      if (k == PUBK) {
        WAITV(10);
        hst64(tag, ws, f3pub, 0);
      }
#pragma unroll
      for (int e = 0; e < 6; ++e) {
        const int i = e / 3, t = e % 3;
        if (k == 0) mfma_va0(acc[i][4 + t], bf[2][t], a[0][i]);
        else mfma_va(acc[i][4 + t], bf[(k + 2) % 3][t], a[k][i]);
        if (i == 0 && k < 18) bf[(k + 1) % 3][t] = rdb(k + 2, 4 + t);
        if (k >= 1 && k < 5 && e == 5) lb4(47 + k);
        if (k >= 1 && k < 19 && e == 3) lb4(51 + k);
        PIN();
      }
    });
    v2u fl;
#pragma unroll
    for (int t = 4; t < 7; ++t) csp[t] = *(LAS f4*)(L + lcs + t * 64);
    MFENCE();
    PIN();
    unsigned os[2][2];
#pragma unroll
    for (int i = 0; i < 2; ++i) {
      f2 v[6];
      unsigned o[6];
#pragma unroll
      for (int e = 0; e < 6; ++e) {
        const int t = 4 + (e >> 1), h = 2 * (e & 1);
        v[e] = f2{acc[i][t][h], acc[i][t][h + 1]} - f2{csp[t][h], csp[t][h + 1]} * mean[i];
      }
      gelu_pack<6>(v, r[i], den[i], o, [&](int m) {
        if (i == 0 && m == 0) fl = hld64(ws, f3wait, 0);
      });
      hst128(v4u{o[0], o[1], o[2], o[3]}, ws, hs, i * 57344 + 2048);
      os[i][0] = o[4];
      os[i][1] = o[5];
      if (i == 0) {
        PIN();
        KEEP(fl);
        if (!__all(f3wait >= OOB || (fl.x == tag.x && fl.y == tag.y))) poll64(ws, f3wait, tag);
        __builtin_amdgcn_fence(__ATOMIC_ACQUIRE, "wavefront");
#pragma unroll
        for (int n = 0; n < WIN4; ++n) {
          lda4(n);
          PIN();
        }
      }
    }
    {
      const auto s0 = __builtin_amdgcn_permlane16_swap(os[0][0], os[1][0], false, false);
      const auto s1 = __builtin_amdgcn_permlane16_swap(os[0][1], os[1][1], false, false);
      hst128(v4u{s0[0], s1[0], s0[1], s1[1]}, ws, (g & 1) * 57344 + (l15 + 16 * (g >> 1)) * 16,
             O_H + ((8 * rg + 2 * w) * 56 + 7 * (cg >> 1) + 3) * 1024 + (cg & 1) * 512);
    }
    YOUNG();
  }
  {
    v4u gb[2];
    v2u gbo, fl;
    f4 ac[4][5], p[15];
    float gf[20], cf[20];
    unsigned lq = lane;
    KEEP(lq);
    const unsigned lr = RED_OFF + lq * 16, gq4 = lq >> 4;
    unsigned dm = ((64 * tm + 16 * w + (lq & 15)) * D_ + 80 * tn) * 2, ds = dm + 8 * gq4 + ((tn & 1) ? 0 : 128);
    dm += 16 * gq4 + 32 * (tn & 1);
    unsigned xro_off = ((lq & 15) + 16 * (gq4 >> 1)) * 16 + 8 * (gq4 & 1);
    sfor<0, 56>([&](auto ic) {
      constexpr int n = decltype(ic)::v, i = n < 32 ? n >> 3 : (n - 32) / 6, sl = n < 32 ? n & 7 : 8 + (n - 32) % 6;
      if (n == 54) {
        WGBAR();
      }
      if (n == PUBN) {
        WAITV(7);
        hst64(tag, ws, f3pub + (O_F4 - O_F3), 0);
      }
      if (n == 23) asm volatile("" : "+v"(dm), "+v"(ds), "+v"(xro_off));
      if (n == 37) asm volatile("s_barrier" ::: "memory");
      if (n == CHKN) {
        KEEP(fl);
        if (!__all(f3wait >= OOB || (fl.x == tag.x && fl.y == tag.y))) poll64(ws, f3wait + (O_F4 - O_F3), tag);
        __builtin_amdgcn_fence(__ATOMIC_ACQUIRE, "wavefront");
#pragma unroll
        for (int m = 32; m < CHKN + WIN4; ++m) lda4(m);
        PIN();
      }
#pragma unroll
      for (int t = 0; t < 5; ++t) {
        const int f = sl * 5 + t;
        if (sl == 0) mfma_aa0(ac[i][t], b4[f], a4[n % RING4]);
        else if (f >= 22 && f < 48) mfma_va(ac[i][t], b4[f], a4[n % RING4]);
        else mfma_aa(ac[i][t], b4[f], a4[n % RING4]);
        if (t == 1) {
          if constexpr (n + WIN4 < 56) {
            if (n < 32 - WIN4 || n >= CHKN) lda4(n + WIN4);
          }
        }
        if (t == 3) {
          if (n == 30 || n == 31) gb[n & 1] = hld128(ws, gq4 * 16 + (n & 1) * 64, O_MOD + (5 * D_ + 80 * tn + 16 * (tn & 1)) * 2);
          if (n == 32 || n == 33) xr[n & 1] = hld128(ws, lq * 16 + (n & 1) * 1024, O_X + own_m);
          if (n == 34) gbo = hld64(ws, gq4 * 8, O_MOD + (5 * D_ + 80 * tn + ((tn & 1) ? 0 : 64)) * 2);
          if (n == 35) xro = hld64(ws, xro_off, O_X + own_s);
        }
        if constexpr (n >= 44 && n < 54) {
          constexpr int q = n - 44;
          const unsigned gw = q < 8 ? gb[(q >> 2) & 1][q & 3] : gbo[q & 1], xw = q < 8 ? xr[(q >> 2) & 1][q & 3] : xro[q & 1];
          float tmp;
          if (t == 1) asm volatile("v_lshlrev_b32 %0, 16, %2\n\tv_and_b32 %1, 0xffff0000, %2" : "=&v"(gf[2 * q]), "=v"(gf[2 * q + 1]) : "v"(gw));
          if (t == 2) asm volatile("v_lshlrev_b32 %0, 16, %2\n\tv_and_b32 %1, 0xffff0000, %2" : "=&v"(cf[2 * q]), "=v"(cf[2 * q + 1]) : "v"(xw));
          if (t == 3) asm volatile("v_lshlrev_b32 %1, 16, %2\n\tv_fmac_f32 %0, %1, %3" : "+v"(cf[2 * q]), "=&v"(tmp) : "v"(x1p[q]), "v"(gf[2 * q]));
          if (t == 4) asm volatile("v_and_b32 %1, 0xffff0000, %2\n\tv_fmac_f32 %0, %1, %3" : "+v"(cf[2 * q + 1]), "=&v"(tmp) : "v"(x1p[q]), "v"(gf[2 * q + 1]));
        }
        if (n == SMPN && t == 0) fl = hld64(ws, f3wait + (O_F4 - O_F3), 0);
        if (n >= 38 && sl < 11 && (t == 0 || t == 2) && (sl - 8) * 2 + (t == 2) > 0) {
          const int u = (sl - 8) * 2 + (t == 2) - 1;
          *(LAS f4*)(L + (lr + ((w + i) & 3) * 15360) + ((i - 1) * 5 + u) * 1024) = ac[i > 0 ? i - 1 : 0][u];
        }
        if (n == 54) {
          p[t] = *(LAS f4*)(L + (lr + w * 15360) + t * 1024);
          p[5 + t] = *(LAS f4*)(L + (lr + w * 15360) + (5 + t) * 1024);
        }
        if (n == 55) p[10 + t] = *(LAS f4*)(L + (lr + w * 15360) + (10 + t) * 1024);
        PIN();
      }
    });
    MFENCE();
    PIN();
    unsigned o[10];
#pragma unroll
    for (int t = 0; t < 5; ++t) {
      const f4 y = (ac[3][t] + p[t]) + (p[5 + t] + p[10 + t]);
      const f2 u = f2{y[0], y[1]} * f2{gf[4 * t], gf[4 * t + 1]} + f2{cf[4 * t], cf[4 * t + 1]};
      const f2 v = f2{y[2], y[3]} * f2{gf[4 * t + 2], gf[4 * t + 3]} + f2{cf[4 * t + 2], cf[4 * t + 3]};
      o[2 * t] = pk2(u.x, u.y);
      o[2 * t + 1] = pk2(v.x, v.y);
    }
    const rsrc_t ro = mkrs(gout, SD2);
    __builtin_amdgcn_raw_buffer_store_b128(v4u{o[0], o[1], o[2], o[3]}, ro, dm, 0, 0);
    __builtin_amdgcn_raw_buffer_store_b128(v4u{o[4], o[5], o[6], o[7]}, ro, dm + 64, 0, 0);
    __builtin_amdgcn_raw_buffer_store_b64(v2u{o[8], o[9]}, ro, ds, 0, 0);
  }
  asm volatile(".Lfused_end:" ::: "memory");
}

extern "C" void kernel_launch(void* const* d_in, const int* in_sizes, int n_in, void* d_out, int out_size,
                              void* d_ws, size_t ws_size, hipStream_t stream) {
  (void)hipFuncSetAttribute((const void*)fused, hipFuncAttributeMaxDynamicSharedMemorySize, LDS_BYTES);
  fused<<<256, 256, LDS_BYTES, stream>>>(d_in[0], d_in[1], d_in[2], d_in[3], d_in[4], d_in[5], d_in[7], d_in[8], d_in[9],
                                         d_in[10], d_in[11], d_in[12], d_out, d_ws);
}
```

```cpp
#include <hip/hip_runtime.h>
#include <stdint.h>

typedef unsigned v4u __attribute__((ext_vector_type(4)));
typedef unsigned v2u __attribute__((ext_vector_type(2)));
typedef float f4 __attribute__((ext_vector_type(4)));
typedef float f2 __attribute__((ext_vector_type(2)));
typedef short s4v __attribute__((ext_vector_type(4)));
typedef __bf16 bf2 __attribute__((ext_vector_type(2)));
typedef __bf16 bfx8 __attribute__((ext_vector_type(8)));
typedef __amdgpu_buffer_rsrc_t rsrc_t;
#define LAS __attribute__((address_space(3)))
#define GAS __attribute__((address_space(1)))
#define DI __device__ __forceinline__

static constexpr int S_ = 2048, D_ = 640, F_ = 1728;
static constexpr unsigned OOB = 0x80000000u;

static constexpr unsigned O_EPOCH = 0;
static constexpr unsigned O_FP = 1024;
static constexpr unsigned O_FR = O_FP + 16 * 128;
static constexpr unsigned O_F1 = O_FR + 32 * 128;
static constexpr unsigned O_F2 = O_F1 + 32 * 128;
static constexpr unsigned O_F3 = O_F2 + 16 * 128;
static constexpr unsigned O_F4 = O_F3 + 16 * 512;
static constexpr unsigned O_CNT = O_F4 + 16 * 512;
static constexpr unsigned O_FW = O_CNT + 64 * 128;
static constexpr unsigned O_FB = O_FW + 8192;
static constexpr unsigned O_MOD = 65536;
static constexpr unsigned O_STAT = O_MOD + 16384;
static constexpr unsigned O_KVT = O_STAT + 131072;
static constexpr unsigned SD2 = S_ * D_ * 2;
static constexpr unsigned KVP = 4096 + 128, ZP = 10240 + 128;
static constexpr unsigned O_ZT = O_KVT + D_ * KVP;
static constexpr unsigned O_A1 = O_ZT + 256 * ZP;
static constexpr unsigned O_HP = O_A1 + SD2;
static constexpr unsigned O_X = O_HP + SD2;
static constexpr unsigned O_H1 = O_X + SD2;
static constexpr unsigned O_X1 = O_H1 + SD2;
static constexpr unsigned O_H = O_X1 + SD2;
static constexpr unsigned O_W1 = O_H + 128 * 56 * 1024;
static constexpr unsigned O_W2 = O_W1 + 800 * 1024;
static constexpr unsigned O_W3 = O_W2 + 800 * 1024;
static constexpr unsigned O_W4 = O_W3 + 2240 * 1024;
static constexpr unsigned O_END = O_W4 + 2240 * 1024;

static constexpr int CS = 4160;
static constexpr int UOFF = 8 * CS;
static constexpr int WR = 8 * CS + 4608;
static constexpr int LDS_BYTES = 160 * 1024;
static constexpr int RED_OFF = 100 * 1024;
static constexpr int CSUM_OFF = 140 * 1024;
static constexpr unsigned SPIN_CAP = 200000;

template <int V> struct IC { static constexpr int v = V; };
template <int I, int N, class F> DI void sfor(F&& f) {
  if constexpr (I < N) {
    f(IC<I>{});
    sfor<I + 1, N>(f);
  }
}
DI float lo2f(unsigned w) { return __builtin_bit_cast(float, w << 16); }
DI float hi2f(unsigned w) { return __builtin_bit_cast(float, w & 0xffff0000u); }
DI f2 up2(unsigned w) { return f2{lo2f(w), hi2f(w)}; }
DI unsigned pk2(float a, float b) { return __builtin_bit_cast(unsigned, __builtin_convertvector(f2{a, b}, bf2)); }
DI f4 mfma(v4u a, v4u b, f4 c) {
  return __builtin_amdgcn_mfma_f32_16x16x32_bf16(__builtin_bit_cast(bfx8, a), __builtin_bit_cast(bfx8, b), c, 0, 0, 0);
}
DI void mfma_v(f4& c, v4u a, v4u b) { asm volatile("v_mfma_f32_16x16x32_bf16 %0, %1, %2, %0" : "+v"(c) : "v"(a), "v"(b)); }
DI void mfma_v0(f4& c, v4u a, v4u b) { asm volatile("v_mfma_f32_16x16x32_bf16 %0, %1, %2, 0" : "=&v"(c) : "v"(a), "v"(b)); }
DI void mfma_va(f4& c, v4u a, v4u b) { asm volatile("v_mfma_f32_16x16x32_bf16 %0, %1, %2, %0" : "+v"(c) : "v"(a), "a"(b)); }
DI void mfma_va0(f4& c, v4u a, v4u b) { asm volatile("v_mfma_f32_16x16x32_bf16 %0, %1, %2, 0" : "=&v"(c) : "v"(a), "a"(b)); }
DI void mfma_av(f4& c, v4u a, v4u b) { asm volatile("v_mfma_f32_16x16x32_bf16 %0, %1, %2, %0" : "+v"(c) : "a"(a), "v"(b)); }
DI void mfma_av0(f4& c, v4u a, v4u b) { asm volatile("v_mfma_f32_16x16x32_bf16 %0, %1, %2, 0" : "=&v"(c) : "a"(a), "v"(b)); }
DI void mfma_aa(f4& c, v4u a, v4u b) { asm volatile("v_mfma_f32_16x16x32_bf16 %0, %1, %2, %0" : "+v"(c) : "a"(a), "a"(b)); }
DI void mfma_aa0(f4& c, v4u a, v4u b) { asm volatile("v_mfma_f32_16x16x32_bf16 %0, %1, %2, 0" : "=&v"(c) : "a"(a), "a"(b)); }
#define KEEP(x) asm volatile("" : "+v"(x))
#define MFENCE() asm volatile("s_nop 7\n\ts_nop 1" ::: "memory")
DI float add1(float a, float b) { float r; asm("v_add_f32 %0, %1, %2" : "=v"(r) : "v"(a), "v"(b)); return r; }
DI rsrc_t mkrs(const void* p, unsigned bytes) { return __builtin_amdgcn_make_buffer_rsrc((void*)p, 0, bytes, 0x00020000); }
DI v4u ld128(rsrc_t r, unsigned v, unsigned s) { return __builtin_amdgcn_raw_buffer_load_b128(r, v, s, 2); }
DI unsigned ld32(rsrc_t r, unsigned v, unsigned s) { return __builtin_amdgcn_raw_buffer_load_b32(r, v, s, 2); }
DI v4u hld128(rsrc_t r, unsigned v, unsigned s) { return __builtin_amdgcn_raw_buffer_load_b128(r, v, s, 16); }
DI v2u hld64(rsrc_t r, unsigned v, unsigned s) { return __builtin_amdgcn_raw_buffer_load_b64(r, v, s, 16); }
DI void hst128(v4u x, rsrc_t r, unsigned v, unsigned s) { __builtin_amdgcn_raw_buffer_store_b128(x, r, v + s, 0, 16); }
DI void hst64(v2u x, rsrc_t r, unsigned v, unsigned s) { __builtin_amdgcn_raw_buffer_store_b64(x, r, v, s, 16); }
DI void hst32(unsigned x, rsrc_t r, unsigned v, unsigned s) { __builtin_amdgcn_raw_buffer_store_b32(x, r, v, s, 16); }
#define DRAIN() do { asm volatile("s_waitcnt vmcnt(0)" ::: "memory"); __builtin_amdgcn_s_waitcnt(0x0F70); } while (0)
#define WGBAR() asm volatile("s_waitcnt lgkmcnt(0)\n\ts_barrier" ::: "memory")
#define PIN() __builtin_amdgcn_sched_barrier(0)

template <int CTRL> DI float dpp(float x) {
  return __builtin_bit_cast(float, __builtin_amdgcn_mov_dpp(__builtin_bit_cast(int, x), CTRL, 0xf, 0xf, true));
}
DI float xrow_sum(float x) {
  auto s = __builtin_amdgcn_permlane16_swap(__builtin_bit_cast(unsigned, x), __builtin_bit_cast(unsigned, x), false, false);
  x = __builtin_bit_cast(float, (unsigned)s[0]) + __builtin_bit_cast(float, (unsigned)s[1]);
  auto t = __builtin_amdgcn_permlane32_swap(__builtin_bit_cast(unsigned, x), __builtin_bit_cast(unsigned, x), false, false);
  return __builtin_bit_cast(float, (unsigned)t[0]) + __builtin_bit_cast(float, (unsigned)t[1]);
}
DI float swap32_add(float a, float b) {
  auto t = __builtin_amdgcn_permlane32_swap(__builtin_bit_cast(unsigned, a), __builtin_bit_cast(unsigned, b), false, false);
  return __builtin_bit_cast(float, (unsigned)t[0]) + __builtin_bit_cast(float, (unsigned)t[1]);
}
DI float swap16_add(float a, float b) {
  auto t = __builtin_amdgcn_permlane16_swap(__builtin_bit_cast(unsigned, a), __builtin_bit_cast(unsigned, b), false, false);
  return __builtin_bit_cast(float, (unsigned)t[0]) + __builtin_bit_cast(float, (unsigned)t[1]);
}
DI float rdl(float v, int l) { return __builtin_bit_cast(float, __builtin_amdgcn_readlane(__builtin_bit_cast(int, v), l)); }
DI void wave_sum4(f2& s, f2& ss) {
  float v = swap16_add(swap32_add(s.x, s.y), swap32_add(ss.x, ss.y));
  v += dpp<0xB1>(v);
  v += dpp<0x4E>(v);
  v += dpp<0x141>(v);
  v += dpp<0x128>(v);
  s = f2{rdl(v, 0), rdl(v, 32)};
  ss = f2{rdl(v, 16), rdl(v, 48)};
}
DI void poll64(rsrc_t ws, unsigned voff, v2u want) {
  const bool idle = voff >= OOB;
  for (unsigned spin = 0; spin < SPIN_CAP; ++spin) {
    asm volatile("" ::: "memory");
    const v2u v = hld64(ws, voff, 0);
    if (__all(idle || (v.x == want.x && v.y == want.y))) break;
  }
  __builtin_amdgcn_fence(__ATOMIC_ACQUIRE, "wavefront");
}

DI bool wflags_ok(v4u a, int lane, int nl, v2u want) {
  return __all(lane >= nl || (a.x == want.x && a.y == want.y && a.z == want.x && a.w == want.y));
}
DI void pollw(rsrc_t ws, unsigned voff, int lane, int nl, v2u want) {
  for (unsigned spin = 0; spin < SPIN_CAP; ++spin) {
    asm volatile("" ::: "memory");
    if (wflags_ok(hld128(ws, voff, 0), lane, nl, want)) break;
    __builtin_amdgcn_s_sleep(4);
  }
  __builtin_amdgcn_fence(__ATOMIC_ACQUIRE, "wavefront");
}
DI float rbf(float x) { return lo2f(pk2(x, 0.f)); }
DI void ln_scal(f2 s, f2 ss, f2& r, f2& mr, f2* pmean = nullptr, f2* pden = nullptr) {
  const f2 m = s * (1.f / D_);
  const f2 var = ss * (1.f / D_) - m * m;
  const f2 v = f2{fmaxf(var.x, 0.f), fmaxf(var.y, 0.f)} + 1e-5f;
  r = f2{__builtin_amdgcn_rsqf(v.x), __builtin_amdgcn_rsqf(v.y)};
  mr = -m * r;
  if (pmean) *pmean = m;
  if (pden) *pden = v * r;
}
static constexpr float GC0 = -2.f * 0.7978845608028654f * 1.4426950408889634f, GC1 = GC0 * 0.044715f;
template <int N, class Mid> DI void gelu_pack(const f2 (&x)[N], float c0, float c1, float den, unsigned* o, Mid mid) {
  f2 a[N];
#define STAGE(expr) _Pragma("unroll") for (int i = 0; i < N; ++i) a[i] = expr; _Pragma("unroll") for (int i = 0; i < N; ++i) KEEP(a[i]); PIN()
  STAGE(x[i] * x[i]);
  mid(0);
  STAGE(a[i] * c1 + c0);
  STAGE(a[i] * x[i]);
  mid(1);
  STAGE((f2{__builtin_amdgcn_exp2f(a[i].x), __builtin_amdgcn_exp2f(a[i].y)}));
  mid(2);
  STAGE(a[i] * den + den);
  STAGE((f2{__builtin_amdgcn_rcpf(a[i].x), __builtin_amdgcn_rcpf(a[i].y)}));
  mid(3);
  STAGE(a[i] * x[i]);
#undef STAGE
#pragma unroll
  for (int i = 0; i < N; ++i) o[i] = pk2(a[i].x, a[i].y);
}


static constexpr int RT_BYTES = 32 * 896;
template <bool WIDE> struct Job {
  static constexpr int NL = WIDE ? 7 : 5, RB = WIDE ? 896 : 640, PITCH = WIDE ? F_ * 2 : D_ * 2;
  v4u r[NL];
  unsigned dst;
  DI void load(int job, int w, const unsigned (&vo)[NL], const void* w1, const void* w2, const void* wx) {
    const bool big = WIDE || job >= 80;
    const int jj = WIDE ? job : (job >= 80 ? job - 80 : (job >= 40 ? job - 40 : job));
    const int gq = WIDE ? jj & 3 : jj & 1, ks = WIDE ? jj >> 2 : jj >> 1;
    const rsrc_t rs = mkrs(big ? wx : (job >= 40 ? w2 : w1), big ? D_ * F_ * 2 : D_ * D_ * 2);
    const unsigned src = (32 * ks + 8 * w) * PITCH + gq * RB;
    dst = (WIDE ? O_W3 : (job >= 80 ? O_W4 : (job >= 40 ? O_W2 : O_W1))) + ((gq * 4 + w) * (!WIDE && job >= 80 ? 56 : 20) + ks) * (NL * 1024);
#pragma unroll
    for (int i = 0; i < NL; ++i)
      r[i] = ld128(rs, (WIDE && gq == 3 && (vo[i] & 1)) ? OOB : (vo[i] & ~1u) + src, 0);
  }
  DI void put(int w, int lane, LAS char* L) {
#pragma unroll
    for (int i = 0; i < NL; ++i) *(LAS v4u*)(L + w * (8 * RB) + lane * 16 + i * 1024) = r[i];
  }
  DI void emit(int w, int lane, LAS char* L, rsrc_t ws) {
    const int g = lane >> 4, q = (lane & 15) >> 2, p = lane & 3;
    LAS char* row = L + (8 * g + q) * RB + w * (RB / 4);
    LAS char* pr = row + 32 * (w & 1) + 16 * p;
    LAS char* sg = row + ((w & 1) ? 0 : RB / 4 - 32) + 8 * p;
    v2u lo[NL], hi[NL];
#pragma unroll
    for (int f = 0; f < NL; ++f) {
      LAS char* c = f < NL - 1 ? pr + 64 * (f >> 1) + 8 * (f & 1) : sg;
      lo[f] = __builtin_bit_cast(v2u, __builtin_amdgcn_ds_read_tr16_b64_v4i16((LAS s4v*)c));
      hi[f] = __builtin_bit_cast(v2u, __builtin_amdgcn_ds_read_tr16_b64_v4i16((LAS s4v*)(c + 4 * RB)));
    }
#pragma unroll
    for (int f = 0; f < NL; ++f) hst128(v4u{lo[f].x, lo[f].y, hi[f].x, hi[f].y}, ws, lane * 16 + dst, f * 1024);
  }
};

template <class Extra, class Gap>
DI void gemm_ks(rsrc_t ws, unsigned abase, const unsigned (&vo)[4], const v4u* b, LAS char* L, int w, int lane, f4 (&acc)[5],
                Extra extra, Gap gap) {
  constexpr int KS = 5, WIN = 12, NA = 4 * KS, RING = WIN + 1;
  v4u a[RING];
  unsigned so[4];
#pragma unroll
  for (int i = 0; i < 4; ++i) so[i] = abase + ((w + 1 + i) & 3) * 20480;
  auto lda = [&](int n) { a[n % RING] = hld128(ws, vo[n / KS] + ((n % KS) & 3) * 1024, so[n / KS] + ((n % KS) >> 2) * 4096); };
#pragma unroll
  for (int n = 0; n < WIN; ++n) {
    lda(n);
    PIN();
  }
  f4 ac[2][5], p[15];
  unsigned lr = RED_OFF + lane * 16;
  KEEP(lr);
  sfor<0, NA>([&](auto ic) {
    constexpr int n = decltype(ic)::v, i = n / KS, k = n % KS;
    if (n == NA - 3) {
      WGBAR();
    }
#pragma unroll
    for (int t = 0; t < 5; ++t) {
      if (k == 0) mfma_av0(ac[i & 1][t], b[t], a[n % RING]);
      else mfma_av(ac[i & 1][t], b[k * 5 + t], a[n % RING]);
      if (t == 1) {
        if constexpr (n + WIN < NA) lda(n + WIN);
      }
      if (t == (n + WIN < NA ? 4 : 1) && n >= NA - 12) extra(n - (NA - 12));
      if (k == 0 && i > 0 && t > 0) *(LAS f4*)(L + (lr + ((w + i) & 3) * 15360) + ((i - 1) * 5 + t - 1) * 1024) = ac[(i - 1) & 1][t - 1];
      if (k == 1 && i > 0 && t == 0) *(LAS f4*)(L + (lr + ((w + i) & 3) * 15360) + ((i - 1) * 5 + 4) * 1024) = ac[(i - 1) & 1][4];
      if (n == NA - 3) {
        p[t] = *(LAS f4*)(L + (lr + w * 15360) + t * 1024);
        p[5 + t] = *(LAS f4*)(L + (lr + w * 15360) + (5 + t) * 1024);
      }
      if (n == NA - 2) p[10 + t] = *(LAS f4*)(L + (lr + w * 15360) + (10 + t) * 1024);
      if (n == NA - 1) {
        p[t][0] = add1(p[t][0], p[5 + t][0]);
        p[t][1] = add1(p[t][1], p[5 + t][1]);
        KEEP(p[t]);
      }
      gap(n, t);
      PIN();
    }
  });
#pragma unroll
  for (int t = 0; t < 5; ++t) {
    p[t][2] += p[5 + t][2];
    p[t][3] += p[5 + t][3];
    p[t] += p[10 + t];
    KEEP(p[t]);
  }
  PIN();
#pragma unroll
  for (int t = 0; t < 5; ++t) acc[t] = ac[1][t] + p[t];
}

#define YOUNG() do { asm volatile("; YOUNG" ::: "memory"); PIN(); } while (0)
#define WAITV(n) asm volatile("s_waitcnt vmcnt(" #n ") ; WAITV" ::: "memory")
__global__ __launch_bounds__(256, 1) void fused(const void*, const void*, const void*, const void*, const void*, const void*,
                                                const void*, const void*, const void*, const void*, const void*, const void*,
                                                void*, void*) {
  extern __shared__ __attribute__((aligned(16))) char smem[];
  LAS char* L = (LAS char*)smem;
  unsigned pf = 0;
  if (blockIdx.x >= 160) {
    unsigned long long pc;
    unsigned off = (((blockIdx.x >> 3) & 1) * 256 + threadIdx.x) * 128;
    asm volatile("s_getpc_b64 %1\n.Lfused_pc:\n\tv_min_u32 %2, .Lfused_end-.Lfused_pc, %2\n\tglobal_load_dword %0, %2, %1"
                 : "=&v"(pf), "=&s"(pc), "+v"(off));
  }
  PIN();
  typedef void* const __attribute__((address_space(4))) * karg_t;
  const karg_t ka = (karg_t)__builtin_amdgcn_kernarg_segment_ptr();
  void *gq = ka[0], *gk = ka[1], *gv = ka[2], *gsk = ka[3], *gssm = ka[4], *gD = ka[5], *gw1 = ka[6], *gw2 = ka[7], *gw3 = ka[8],
       *gw4 = ka[9], *gc = ka[10], *gwm = ka[11], *gout = ka[12], *gws = ka[13];
  asm volatile("" ::"s"(gq), "s"(gk), "s"(gv), "s"(gsk), "s"(gssm), "s"(gD), "s"(gw1), "s"(gw2), "s"(gw3), "s"(gw4),
               "s"(gc), "s"(gwm), "s"(gout), "s"(gws));
  const int tid = threadIdx.x, lane = tid & 63;
  const int w = __builtin_amdgcn_readfirstlane(tid >> 6);
  const int b = blockIdx.x, x = b & 7, j = b >> 3;
  const int tm = 4 * x + (j >> 3), tn = j & 7;
  const int rg = 2 * x + (j >> 4), cg = j & 15;
  const bool conv_wg = j < 20;
  const rsrc_t ws = mkrs(gws, O_END);
  const int g = lane >> 4, l15 = lane & 15;

  const int rt0 = 64 * tm + 8 * tn + 2 * w;
  const int rcm = 8 * lane, rco = 512 + 2 * lane;
  v4u qm[2];
  unsigned qo[2];
  auto load_q = [&]() {
    const rsrc_t rq = mkrs(gq, SD2);
#pragma unroll
    for (int t = 0; t < 2; ++t) {
      qm[t] = ld128(rq, rcm * 2, (rt0 + t) * D_ * 2);
      qo[t] = ld32(rq, rco * 2, (rt0 + t) * D_ * 2);
    }
  };
  v2u tag;
  auto make_tag = [&](v2u ep) {
    tag.x = __builtin_amdgcn_readfirstlane(ep.x) * 0x9E3779B1u + 0x7F4A7C15u;
    tag.y = (__builtin_amdgcn_readfirstlane(ep.y) ^ tag.x) * 0x85EBCA77u + 0x2545F491u;
    tag.x &= 0xffffff00u;
    tag.y |= 1u;
  };
  v4u b1[25], b2[25];
  const unsigned sb1 = O_W1 + (tn * 100 + w * 25) * 1024, sb2 = O_W2 + (tn * 100 + w * 25) * 1024;
  auto zero_h1 = [&](int i) {
    hst128(v4u{0u, 0u, 0u, 0u}, ws, (i < 2 || tid < 128) ? tid * 16 : OOB, O_H1 + b * 10240 + i * 4096);
  };
  v4u mm[5];
  unsigned mo[5];
  auto load_mod = [&](bool ok) {
    unsigned o1 = (ok ? 0u : OOB) | (lane * 16), o2 = (ok ? 0u : OOB) | (lane * 4 + 1024);
    asm volatile("" : "+v"(o1), "+v"(o2));
#pragma unroll
    for (int i = 0; i < 5; ++i) {
      mm[i] = hld128(ws, o1, O_MOD + i * D_ * 2);
      mo[i] = __builtin_amdgcn_raw_buffer_load_b32(ws, o2, O_MOD + i * D_ * 2, 16);
    }
  };
  f2 qv[10], n1[10];
  auto ln1 = [&]() {
#pragma unroll
    for (int i = 0; i < 4; ++i) {
      qv[2 * i] = f2{lo2f(qm[0][i]), lo2f(qm[1][i])};
      qv[2 * i + 1] = f2{hi2f(qm[0][i]), hi2f(qm[1][i])};
    }
    qv[8] = f2{lo2f(qo[0]), lo2f(qo[1])};
    qv[9] = f2{hi2f(qo[0]), hi2f(qo[1])};
    f2 s = qv[0], ss = qv[0] * qv[0];
#pragma unroll
    for (int i = 1; i < 10; ++i) { s += qv[i]; ss += qv[i] * qv[i]; }
    wave_sum4(s, ss);
    f2 r, mr;
    ln_scal(s, ss, r, mr);
#pragma unroll
    for (int i = 0; i < 10; ++i) n1[i] = qv[i] * r + mr;
  };
  unsigned z3off, im0, io0, roff, roff3, own_r, x1m, x1s, stoff, h1m, h1s, f1pub, f1wait, frpub, frwait, f2pub, f2wait, f3pub, f3wait;
  const unsigned own_m = ((4 * tm + w) * 20 + 5 * (tn >> 1) + 3 * (tn & 1)) * 1024;
  const unsigned own_s = ((4 * tm + w) * 20 + 5 * (tn >> 1) + 2) * 1024 + (tn & 1) * 512;
  auto offsets = [&]() {
    z3off = tid < 128 ? tid * 16 + 8192 : OOB;
    im0 = RED_OFF + 16384 + lane * 128 + (((2 * w) ^ (lane & 7)) << 4);
    io0 = RED_OFF + 16384 + (64 + (lane >> 2)) * 128 + (((2 * w) ^ ((lane >> 2) & 7)) << 4) + 4 * (lane & 3);
    roff = O_A1 + ((4 * tm + (tn >> 1)) * 20 + (tid >> 5)) * 1024 + (((tid ^ (tid >> 3)) & 7) + 8 * (tn & 1) + 16 * ((tid >> 3) & 3)) * 16;
    roff3 = tid < 128 ? roff : OOB;
    own_r = (l15 + 16 * (g >> 1)) * 16 + 8 * (g & 1);
    x1m = O_X1 + own_m + lane * 16;
    x1s = (g & 1) ? OOB + SD2 : O_X1 + own_s + (l15 + 16 * (g >> 1)) * 16;
    stoff = (g & 1) ? OOB : O_STAT + ((4 * tm + w) * 8 + tn) * 128 + l15 * 8 + (g >> 1) * 4;
    f3pub = lane == 0 ? O_F3 + rg * 512 + (w >> 1) * 256 + cg * 16 + (w & 1) * 8 : OOB;
    f3wait = lane < 8 ? O_F3 + rg * 512 + (tm & 1) * 256 + w * 64 + lane * 8 : OOB;
    const int sh = (tn >> 1) == 0 ? 1 : ((tn >> 1) == 1 ? -1 : ((tn >> 1) == 2 ? 2 : -2));
    const int t2 = 64 * tm + 16 * w + l15 + sh;
    const bool ok = t2 >= 0 && t2 < S_;
    const unsigned d = O_H1 + ((t2 >> 4) * 20 + 5 * (tn >> 1)) * 1024 + (t2 & 15) * 16;
    h1m = ok ? d + 3072 * (tn & 1) + 256 * g : OOB;
    h1s = (ok && !(g & 1)) ? d + 2048 + 512 * (tn & 1) + 256 * (g >> 1) : OOB;
    const int nb = tm + (sh > 0 ? 1 : -1);
    f1pub = OOB;
    if (lane == 0) f1pub = O_F1 + tm * 128 + tn * 8;
    if (lane == 1 && nb >= 0 && nb < 32) f1pub = O_F1 + nb * 128 + 64 + tn * 8;
    const bool up = ((lane >> 1) & 1) == 0;
    f1wait = (lane < 8 || (lane < 16 && (up ? tm > 0 : tm < 31))) ? O_F1 + tm * 128 + lane * 8 : OOB;
    frpub = lane == 0 ? O_FR + tm * 128 + tn * 8 : OOB;
    frwait = lane < 8 ? O_FR + tm * 128 + lane * 8 : OOB;
    f2pub = lane == 0 ? O_F2 + (tm >> 1) * 128 + ((tm & 1) * 8 + tn) * 8 : OOB;
    f2wait = lane < 16 ? O_F2 + rg * 128 + lane * 8 : OOB;
    asm volatile("" : "+v"(frpub), "+v"(frwait), "+v"(f2pub), "+v"(f2wait));
    asm volatile("" : "+v"(z3off), "+v"(im0), "+v"(io0), "+v"(roff), "+v"(roff3), "+v"(own_r), "+v"(f1pub), "+v"(f1wait));
    asm volatile("" : "+v"(x1m), "+v"(x1s), "+v"(stoff), "+v"(f3pub), "+v"(f3wait));
    asm volatile("" : "+v"(h1m), "+v"(h1s));
  };
  constexpr int NB2 = 19;
  bool wok = false, bok = false;
  if (conv_wg) {
    const int cgp = j < 16 ? x : 8 + (x >> 2), ci = j < 16 ? j : (x & 3) * 4 + (j - 16);
    const int pT0 = 128 * ci + 32 * w + 8 * ((lane >> 3) & 3) + 4 * (lane >> 5), pC0 = 64 * cgp + 8 * (lane & 7);
    const int ch = 64 * cgp + 4 * ci + w;
    v4u kw[7], vw[4], skw[4], kr[4];
    {
      const rsrc_t rk = mkrs(gk, SD2), rv = mkrs(gv, SD2), rs = mkrs(gsk, D_ * 8);
      const unsigned o = (unsigned)(pT0 * D_ + pC0) * 2;
#pragma unroll
      for (int i = 0; i < 7; ++i) kw[i] = __builtin_amdgcn_raw_buffer_load_b128(rk, o + (i - 3) * D_ * 2, 0, 0);
#pragma unroll
      for (int i = 0; i < 4; ++i) vw[i] = ld128(rv, o + i * D_ * 2, 0);
#pragma unroll
      for (int i = 0; i < 4; ++i) skw[i] = __builtin_amdgcn_raw_buffer_load_b128(rs, pC0 * 8 + i * 16, 0, 0);
      PIN();
      offsets();
      PIN();
      asm volatile("s_waitcnt vmcnt(4)" ::: "memory");
      const rsrc_t rK = mkrs((const char*)gssm + (size_t)ch * 4096, 4096);
#pragma unroll
      for (int i = 0; i < 4; ++i) kr[i] = ld128(rK, lane * 16 + i * 1024, 0);
    }
    const v2u ep = hld64(ws, lane == 0 ? O_EPOCH : O_FP + x * 256 + (j >= 16) * 128, 0);
    const unsigned dvb = __builtin_amdgcn_raw_buffer_load_b16(mkrs(gD, D_ * 2), ch * 2, 0, 0);
    PIN();
    {
      unsigned pe[8][2];
#pragma unroll
      for (int pr = 0; pr < 4; ++pr) {
        const f2 w0 = {lo2f(skw[pr].x), lo2f(skw[pr].z)}, w1 = {hi2f(skw[pr].x), hi2f(skw[pr].z)};
        const f2 w2 = {lo2f(skw[pr].y), lo2f(skw[pr].w)}, w3 = {hi2f(skw[pr].y), hi2f(skw[pr].w)};
        f2 kf[7], o[4];
#pragma unroll
        for (int i = 0; i < 7; ++i) kf[i] = up2(kw[i][pr]);
#pragma unroll
        for (int i = 0; i < 4; ++i) o[i] = (kf[i + 3] * w0 + kf[i + 2] * w1 + kf[i + 1] * w2 + kf[i] * w3) * up2(vw[i][pr]);
        pe[2 * pr][0] = pk2(o[3].x, o[2].x);
        pe[2 * pr][1] = pk2(o[1].x, o[0].x);
        pe[2 * pr + 1][0] = pk2(o[3].y, o[2].y);
        pe[2 * pr + 1][1] = pk2(o[1].y, o[0].y);
      }
      const unsigned so = (pC0 + 4 * (lane >> 5)) * KVP + (pT0 & ~7) * 2;
#pragma unroll
      for (int e = 0; e < 4; ++e) {
        const auto s0 = __builtin_amdgcn_permlane32_swap(pe[e][0], pe[e + 4][0], false, false);
        const auto s1 = __builtin_amdgcn_permlane32_swap(pe[e][1], pe[e + 4][1], false, false);
        hst128(v4u{s0[1], s1[1], s0[0], s1[0]}, ws, so, O_KVT + e * KVP);
      }
    }
    PIN();
    LAS char* T = L + w * WR;
    if (lane < 8) *(LAS v4u*)(T + lane * CS) = v4u{0u, 0u, 0u, 0u};
#pragma unroll
    for (int i = 0; i < 4; ++i) *(LAS v4u*)(T + 7 * CS + (i * 64 + lane + 1) * 16) = kr[i];
    asm volatile("s_waitcnt lgkmcnt(0)" ::: "memory");
    v4u kl[4];
#pragma unroll
    for (int i = 0; i < 4; ++i) kl[i] = *(LAS v4u*)(T + 7 * CS + (i * 64 + lane) * 16);
    auto tbuild = [&](int i) {
      const unsigned d[8] = {kl[i].x, kl[i].y, kl[i].z, kl[i].w, kr[i].x, kr[i].y, kr[i].z, kr[i].w};
      unsigned a[7];
#pragma unroll
      for (int e = 0; e < 7; ++e) a[e] = __builtin_amdgcn_alignbit(d[e + 1], d[e], 16);
#pragma unroll
      for (int m = 0; m < 7; ++m) {
        const int h = (m + 1) >> 1;
        const v4u o = (m & 1) ? v4u{d[h], d[h + 1], d[h + 2], d[h + 3]} : v4u{a[h], a[h + 1], a[h + 2], a[h + 3]};
        *(LAS v4u*)(T + m * CS + (i * 64 + lane + 1) * 16) = o;
      }
    };
    tbuild(0);
    make_tag(ep);
    DRAIN();
    asm volatile("s_barrier" ::: "memory");
    if (w == 0) {
      unsigned o = OOB;
      if (j < 16 && lane == 0) o = O_FP + x * 256 + ci * 8;
      if (j >= 16 && lane < 4) o = O_FP + ((x & 4) + lane) * 256 + 128 + ci * 8;
      hst64(tag, ws, o, 0);
    }
    tbuild(1);
    tbuild(2);
    tbuild(3);
    if (lane < 32) *(LAS v4u*)(T + UOFF + lane * 16) = v4u{0u, 0u, 0u, 0u};
    const LAS char* pa = T + (l15 & 7) * CS + (3 - g + (l15 >> 3)) * 16;
    const LAS char* pv = T + UOFF + 512 + (l15 - 1) * 32 + g * 16;
    v4u fa[2][8], fv[2][8];
    asm volatile("s_waitcnt lgkmcnt(0)" ::: "memory");
#pragma unroll
    for (int a = 0; a < 8; ++a) fa[0][a] = *(const LAS v4u*)(pa + 8 * a * 64);
    f4 acc[8];
    v4u ad;
    {
      const int jd = 8 * g - 9 - l15;
#pragma unroll
      for (int e = 0; e < 4; ++e) ad[e] = jd == 2 * e ? dvb : (jd == 2 * e + 1 ? dvb << 16 : 0u);
      asm volatile("" : "+v"(ad));
    }
    const rsrc_t ru = mkrs((const char*)gws + O_KVT + ch * KVP, 4096);
    unsigned v0off = (l15 - 1) * 32 + g * 16, v1off = v0off + 512;
    asm volatile("" : "+v"(v0off), "+v"(v1off));
    if (w == 0) poll64(ws, lane < 16 ? O_FP + x * 256 + (j >= 16) * 128 + lane * 8 : OOB, tag);
    WGBAR();
#pragma unroll
    for (int a = 0; a < 8; ++a) {
      fv[0][a] = hld128(ru, a == 0 ? v0off : v1off + (a - 1) * 512, 0);
      PIN();
    }
    v4u ur[4];
#pragma unroll
    for (int i = 0; i < 4; ++i) ur[i] = hld128(ru, lane * 16 + i * 1024, 0);
    const v4u wf = hld128(ws, O_FW + x * 1024 + lane * 16, 0);
    PIN();
#pragma unroll
    for (int q = 0; q < 8; ++q) mfma_v0(acc[q], ad, fv[0][q]);
    PIN();
#pragma unroll
    for (int i = 0; i < 4; ++i) *(LAS v4u*)(T + UOFF + 512 + i * 1024 + lane * 16) = ur[i];
    unsigned boff = OOB;
    PIN();
#pragma unroll
    for (int g0 = 0; g0 < 8; ++g0) {
      int n = 0;
      if (g0 == 0) load_q();
      if (g0 == 1) {
        wok = wflags_ok(wf, lane, 40, tag);
        __builtin_amdgcn_fence(__ATOMIC_ACQUIRE, "wavefront");
        boff = wok ? lane * 16 : OOB;
      }
#pragma unroll
      for (int a = 0; a < 8; ++a)
#pragma unroll
        for (int q = a; q < 8; ++q) {
          mfma_v(acc[q], fa[g0 & 1][a], fv[g0 & 1][q - a]);
          ++n;
          if ((n & 1) == 0) {
            const int e = n / 2 - 1;
            if (g0 < 7 && e < 16) {
              if (e < 8) fv[(g0 + 1) & 1][e] = *(const LAS v4u*)(pv + e * 512 - (g0 + 1) * 64);
              else fa[(g0 + 1) & 1][e - 8] = *(const LAS v4u*)(pa + (g0 + 1 + 8 * (e - 8)) * 64);
            }
            PIN();
          }
          if (n % 5 == 3) {
            const int i = g0 * 7 + n / 5 - 12;
            if (i < -9) zero_h1(i + 12);
            if (i >= 0 && i < 25) b1[i] = hld128(ws, boff + (i & 3) * 1024, sb1 + (i >> 2) * 4096);
            if (i >= 25 && i < 25 + NB2) b2[i - 25] = hld128(ws, boff + ((i - 25) & 3) * 1024, sb2 + ((i - 25) >> 2) * 4096);
            PIN();
          }
        }
    }
    const v4u bfl = hld128(ws, O_FB + x * 1024 + lane * 16, 0);
    MFENCE();
    PIN();
#pragma unroll
    for (int q = 0; q < 8; q += 2) {
      const auto s0 = __builtin_amdgcn_permlane16_swap(pk2(acc[q][0], acc[q][1]), pk2(acc[q + 1][0], acc[q + 1][1]), false, false);
      const auto s1 = __builtin_amdgcn_permlane16_swap(pk2(acc[q][2], acc[q][3]), pk2(acc[q + 1][2], acc[q + 1][3]), false, false);
      *(LAS v4u*)(T + 64 * w + (32 * (q + (g & 1)) + 2 * l15 + (g >> 1)) * 16) = v4u{s0[0], s1[0], s0[1], s1[1]};
    }
    WGBAR();
    {
      v4u zo[4];
#pragma unroll
      for (int i = 0; i < 4; ++i) zo[i] = *(LAS v4u*)(L + (lane & 3) * (WR + 64) + (64 * w + 16 * i + (lane >> 2)) * 16);
#pragma unroll
      for (int i = 0; i < 4; ++i) hst128(zo[i], ws, (lane >> 2) * ZP + (lane & 3) * 16, O_ZT + (64 * w + 16 * i) * ZP + (ch - w) * 16);
    }
    YOUNG();
    bok = wflags_ok(bfl, lane, 48, tag);
    __builtin_amdgcn_fence(__ATOMIC_ACQUIRE, "wavefront");
    load_mod(bok);
    PIN();
    ln1();
    PIN();
    WAITV(10);
  } else {
    const int hb = (j - 20) * 8 + x;
    unsigned vn[5];
#pragma unroll
    for (int i = 0; i < 5; ++i) vn[i] = (i * 64 + lane) * 16 + (((i * 64 + lane) * 1639u) >> 16) * 640;
    Job<false> j0, j1;
    j0.load(hb, w, vn, gw1, gw2, gw4);
    const v2u ep = hld64(ws, O_EPOCH, 0);
    const v2u cw = __builtin_amdgcn_raw_buffer_load_b64(mkrs(gc, D_ * 2), tid * 8, 0, 0);
    PIN();
    unsigned vw[7];
#pragma unroll
    for (int i = 0; i < 7; ++i) {
      const unsigned c = i * 64 + lane, row = (c * 1171u) >> 16;
      vw[i] = c * 16 + row * 2560 + (c - row * 56 >= 48);
      KEEP(vw[i]);
    }
    offsets();
    LAS float* sc = (LAS float*)(L + 4 * RT_BYTES);
    {
      const float c4[4] = {lo2f(cw.x), hi2f(cw.x), lo2f(cw.y), hi2f(cw.y)};
      f4 sv;
#pragma unroll
      for (int e = 0; e < 4; ++e) sv[e] = rbf(c4[e] * __builtin_amdgcn_rcpf(1.f + __builtin_amdgcn_exp2f(-1.4426950408889634f * c4[e])));
      *(LAS f4*)(sc + tid * 4) = sv;
    }
    j0.put(w, lane, L);
    make_tag(ep);
    WGBAR();
    j0.emit(w, lane, L, ws);
    if (j == 20 && w == 0) hst128(v4u{tag.x, tag.y, 0u, 0u}, ws, (lane >> 3) * 1024 + (lane & 7) * 16, O_CNT + x * 128);
    asm volatile("" ::"v"(pf) : "memory");
    auto first_pub = [&]() {
      WGBAR();
      if (w == 0) hst64(tag, ws, lane < 8 ? O_FW + lane * 1024 + hb * 8 : OOB, 0);
    };
    if (hb < 60) {
      const rsrc_t rm = mkrs(gwm, D_ * 6 * D_ * 2);
      v4u wm[20];
      YOUNG();
#pragma unroll
      for (int i = 0; i < 8; ++i) wm[i] = ld128(rm, (tid >> 3) * (6 * D_ * 2) + (tid & 7) * 16, hb * 128 + i * 32 * (6 * D_ * 2));
      PIN();
      WAITV(8);
      first_pub();
#pragma unroll
      for (int i = 8; i < 20; ++i) wm[i] = ld128(rm, (tid >> 3) * (6 * D_ * 2) + (tid & 7) * 16, hb * 128 + i * 32 * (6 * D_ * 2));
      j1.load(132 + hb, w, vn, gw1, gw2, gw4);
      load_q();
      float sl[20];
#pragma unroll
      for (int i = 0; i < 20; ++i) sl[i] = sc[(tid >> 3) + 32 * i];
      PIN();
      f2 a[4] = {f2{0.f, 0.f}, f2{0.f, 0.f}, f2{0.f, 0.f}, f2{0.f, 0.f}};
#pragma unroll
      for (int i = 0; i < 20; ++i)
#pragma unroll
        for (int e = 0; e < 4; ++e) a[e] += up2(wm[i][e]) * sl[i];
      LAS char* mred = L + 4 * RT_BYTES + 4096;
      *(LAS f4*)(mred + (tid >> 3) * 272 + (tid & 7) * 32) = f4{a[0].x, a[0].y, a[1].x, a[1].y};
      *(LAS f4*)(mred + (tid >> 3) * 272 + (tid & 7) * 32 + 16) = f4{a[2].x, a[2].y, a[3].x, a[3].y};
      WGBAR();
      {
        const int col = tid >> 2, part = tid & 3;
        float v[8];
#pragma unroll
        for (int r = 0; r < 8; ++r) v[r] = *(LAS float*)(mred + (part * 8 + r) * 272 + col * 4);
        float s = ((v[0] + v[1]) + (v[2] + v[3])) + ((v[4] + v[5]) + (v[6] + v[7]));
        s += dpp<0xB1>(s);
        s += dpp<0x4E>(s);
        s = rbf(s);
        if ((hb >= 10 && hb < 20) || (hb >= 40 && hb < 50)) s = rbf(1.f + s);
        if (part == 0) *((LAS unsigned short*)sc + 8 + col) = (unsigned short)(__builtin_bit_cast(unsigned, s) >> 16);
      }
      j1.put(w, lane, L);
      WGBAR();
      if (w == 0) {
        hst128(*(LAS v4u*)((LAS char*)sc + 16 + (lane & 7) * 16), ws, lane < 8 ? lane * 16 : OOB, O_MOD + hb * 128);
        DRAIN();
        hst64(tag, ws, lane < 8 ? O_FB + lane * 1024 + hb * 8 : OOB, 0);
      }
    } else {
      Job<true> ja, jb, jc;
      YOUNG();
      ja.load(hb - 60, w, vw, gw1, gw2, gw3);
      PIN();
      WAITV(7);
      first_pub();
      jb.load(hb - 24, w, vw, gw1, gw2, gw3);
      if (hb < 68) jc.load(hb + 12, w, vw, gw1, gw2, gw3);
      j1.load(36 + hb, w, vn, gw1, gw2, gw4);
      load_q();
      PIN();
      ja.put(w, lane, L + RT_BYTES);
      jb.put(w, lane, L + 2 * RT_BYTES);
      if (hb < 68) jc.put(w, lane, L + 3 * RT_BYTES);
      WGBAR();
      ja.emit(w, lane, L + RT_BYTES, ws);
      jb.emit(w, lane, L + 2 * RT_BYTES, ws);
      if (hb < 68) jc.emit(w, lane, L + 3 * RT_BYTES, ws);
      j1.put(w, lane, L);
      DRAIN();
      WGBAR();
      if (w == 0) hst64(tag, ws, lane < 8 ? O_FB + lane * 1024 + hb * 8 : OOB, 0);
    }
    pollw(ws, O_FW + x * 1024 + lane * 16, lane, 40, tag);
    j1.emit(w, lane, L, ws);
    zero_h1(0);
    zero_h1(1);
    zero_h1(2);
    if (hb >= 34 && hb < 60) hst128(v4u{0u, 0u, 0u, 0u}, ws, w == 0 ? lane * 16 : OOB, O_FR + (hb - 34) * 1024);
    YOUNG();
#pragma unroll
    for (int i = 0; i < 10; ++i) b1[i] = hld128(ws, lane * 16, sb1 + i * 1024);
    PIN();
    ln1();
    PIN();
    WAITV(10);
  }
  if (conv_wg && !wok) pollw(ws, O_FW + x * 1024 + lane * 16, lane, 40, tag);
  WGBAR();
  if (w == 0 && lane < 8)
    __hip_atomic_fetch_add((GAS unsigned*)((GAS char*)gws + O_CNT + (lane * 8 + x) * 128), 1u, __ATOMIC_RELAXED, __HIP_MEMORY_SCOPE_AGENT);
  if (!wok) {
    if (conv_wg) {
#pragma unroll
      for (int i = 0; i < 10; ++i) b1[i] = hld128(ws, lane * 16, sb1 + i * 1024);
    }
#pragma unroll
    for (int i = 10; i < 25; ++i) b1[i] = hld128(ws, lane * 16, sb1 + i * 1024);
#pragma unroll
    for (int i = 0; i < NB2; ++i) b2[i] = hld128(ws, lane * 16, sb2 + i * 1024);
  }
#pragma unroll
  for (int i = NB2; i < 25; ++i) b2[i] = hld128(ws, lane * 16, sb2 + i * 1024);
  v4u b3[35], b4[70];
  const unsigned sb3 = O_W3 + (cg * 140 + w * 35) * 1024, sb4 = O_W4 + (tn * 280 + w * 70) * 1024;
#define LB3(lo, hi) _Pragma("unroll") for (int i_ = lo; i_ < hi; ++i_) b3[i_] = hld128(ws, lane * 16 + (i_ & 3) * 1024, sb3 + (i_ >> 2) * 4096)
  auto ks4 = [](int s) { return s < 8 ? 7 * (s >> 2) + (s & 1) + 4 * ((s >> 1) & 1) : 7 * ((s - 8) / 3) + ((s - 8) % 3 == 2 ? 6 : 2 + (s - 8) % 3); };
  auto lb4 = [&](int i) {
    const int f = ks4(i / 5) * 5 + i % 5;
    b4[i] = hld128(ws, lane * 16 + (f & 3) * 1024, sb4 + (f >> 2) * 4096);
  };
#define LB4(lo, hi) _Pragma("unroll") for (int i_ = lo; i_ < hi; ++i_) lb4(i_)
  if (!bok) {
    pollw(ws, O_FB + x * 1024 + lane * 16, lane, 48, tag);
    load_mod(true);
  }
  constexpr int NB3C = 10;
  f2 gh[10];
  float cb1[10], sbv[10];
  auto md = [&](int v, int i) { const unsigned u = i < 8 ? mm[v][(i >> 1) & 3] : mo[v]; return (i & 1) ? hi2f(u) : lo2f(u); };
  auto use_mod = [&]() {
    LB3(0, NB3C);
#pragma unroll
    for (int i = 0; i < 10; ++i) {
      gh[i] = (n1[i] * md(1, i) + md(0, i)) * md(2, i);
      cb1[i] = md(4, i);
      sbv[i] = md(3, i);
      asm volatile("" : "+v"(gh[i]), "+v"(cb1[i]), "+v"(sbv[i]));
    }
  };
  use_mod();
  PIN();
  if (w == 0) {
    poll64(ws, lane < 8 ? O_CNT + (x * 8 + lane) * 128 : OOB, v2u{tag.x + 32, tag.y});
    if (b == 0) hst64(tag, ws, lane == 0 ? O_EPOCH : OOB, 0);
  }
  WGBAR();

  v4u hx[6];
  f4 csp[7];
  auto img3 = [&](int lo, int hi, int hc) {
    const v4u ones = {0x3f803f80u, 0x3f803f80u, 0x3f803f80u, 0x3f803f80u};
    unsigned la = w * 25600 + lane * 16, lb = (75 + w * 10) * 1024 + lane * 16;
    asm volatile("" : "+v"(la), "+v"(lb));
#pragma unroll
    for (int i = lo; i < 35; ++i) {
      if (i < hi) *(LAS v4u*)(L + (i < 25 ? la : lb) + i * 1024) = b3[i];
      if (i < hc) csp[i % 7] = mfma(b3[i], ones, i < 7 ? f4{0.f, 0.f, 0.f, 0.f} : csp[i % 7]);
      PIN();
    }
  };
  {
    v4u zc[3];
#pragma unroll
    for (int i = 0; i < 3; ++i) zc[i] = hld128(ws, i < 2 ? tid * 16 + i * 4096 : z3off, O_ZT + (8 * tm + tn) * ZP);
    PIN();
#pragma unroll
    for (int i = 0; i < 3; ++i)
#pragma unroll
      for (int e = 0; e < 4; ++e) *(LAS unsigned*)(L + RED_OFF + e * 3072 + (tid + 256 * i) * 4) = zc[i][e];
    WGBAR();
    const v4u z0 = *(LAS v4u*)(L + RED_OFF + w * 3072 + rcm * 4), z1 = *(LAS v4u*)(L + RED_OFF + w * 3072 + rcm * 4 + 16);
    const v2u z2 = *(LAS v2u*)(L + RED_OFF + w * 3072 + rco * 4);
    LB3(10, 19);
    f2 xv[10], hv[10], av[10], s, ss, r, mr;
#pragma unroll
    for (int i = 0; i < 10; ++i) {
      xv[i] = gh[i] * up2(i < 4 ? z0[i & 3] : (i < 8 ? z1[i & 3] : z2[i & 1])) + qv[i];
      s = i ? s + xv[i] : xv[i];
      ss = i ? ss + xv[i] * xv[i] : xv[i] * xv[i];
    }
    wave_sum4(s, ss);
    ln_scal(s, ss, r, mr);
#pragma unroll
    for (int i = 0; i < 10; ++i) {
      hv[i] = (xv[i] * r + mr) * cb1[i] + sbv[i];
      s = i ? s + hv[i] : hv[i];
      ss = i ? ss + hv[i] * hv[i] : hv[i] * hv[i];
    }
    wave_sum4(s, ss);
    ln_scal(s, ss, r, mr);
#pragma unroll
    for (int i = 0; i < 10; ++i) av[i] = hv[i] * r + mr;
    auto put = [&](const f2(&v)[10], int img) {
      *(LAS v4u*)(L + im0 + img * 10240) = v4u{pk2(v[0].x, v[1].x), pk2(v[2].x, v[3].x), pk2(v[4].x, v[5].x), pk2(v[6].x, v[7].x)};
      *(LAS v4u*)(L + (im0 ^ 16) + img * 10240) = v4u{pk2(v[0].y, v[1].y), pk2(v[2].y, v[3].y), pk2(v[4].y, v[5].y), pk2(v[6].y, v[7].y)};
      *(LAS unsigned*)(L + io0 + img * 10240) = pk2(v[8].x, v[9].x);
      *(LAS unsigned*)(L + (io0 ^ 16) + img * 10240) = pk2(v[8].y, v[9].y);
    };
    put(av, 0);
    WGBAR();
    {
      v4u oc[3];
#pragma unroll
      for (int i = 0; i < 3; ++i) oc[i] = *(LAS v4u*)(L + RED_OFF + 16384 + i * 4096 + tid * 16);
#pragma unroll
      for (int i = 0; i < 3; ++i) hst128(oc[i], ws, i < 2 ? roff : roff3, i * 8192);
    }
    PIN();
    put(hv, 1);
    put(xv, 2);
    DRAIN();
    WGBAR();
    if (w == 0) hst64(tag, ws, frpub, 0);
    LB3(19, 28);
    PIN();
#pragma unroll
    for (int i = 0; i < 6; ++i) hx[i] = *(LAS v4u*)(L + RED_OFF + 26624 + (i / 3) * 10240 + (i % 3) * 4096 + tid * 16);
    img3(0, 16, 16);
    if (w == 0) poll64(ws, frwait, tag);
    WGBAR();
  }

  {
    f4 acc[5];
    const unsigned vo[4] = {(unsigned)lane * 16, (unsigned)lane * 16, (unsigned)lane * 16, (unsigned)lane * 16};
    gemm_ks(ws, O_A1 + (4 * tm * 20 + 5 * w) * 1024, vo, b1, L, w, lane, acc,
            [&](int n) { if (n < 7) { LB3(28 + n, 29 + n); } else lb4(16 + n); },
            [&](int n, int t) {
              if (t == 3 && n >= 8 && n < 14) hst128(hx[n - 8], ws, ((n - 8) % 3) < 2 ? roff : roff3, (1 + (n - 8) / 3) * SD2 + ((n - 8) % 3) * 8192);
            });
    f2 v[10];
    unsigned o[10];
#pragma unroll
    for (int t = 0; t < 5; ++t) {
      v[2 * t] = f2{acc[t][0], acc[t][1]};
      v[2 * t + 1] = f2{acc[t][2], acc[t][3]};
    }
    gelu_pack<10>(v, GC0, GC1, 1.f, o, [](int) {});
    const auto s0 = __builtin_amdgcn_permlane16_swap(o[8], o[8], false, false);
    const auto s1 = __builtin_amdgcn_permlane16_swap(o[9], o[9], false, false);
    hst128(v4u{o[0], o[1], o[2], o[3]}, ws, h1m, 0);
    hst128(v4u{o[4], o[5], o[6], o[7]}, ws, h1m + 1024, 0);
    hst128(v4u{s0[0], s1[0], s0[1], s1[1]}, ws, h1s, 0);
    DRAIN();
    WGBAR();
    if (w == 0) hst64(tag, ws, f1pub, 0);
    LB4(0, 9);
    PIN();
    img3(16, 24, 35);
    if (l15 == 0) {
#pragma unroll
      for (int t = 0; t < 7; ++t) *(LAS f4*)(L + (w * 25 + 24) * 1024 + (t * 4 + g) * 16) = csp[t];
    }
    if (w == 0) poll64(ws, f1wait, tag);
    WGBAR();
  }
  v4u xr[2];
  v2u xro;
  unsigned x1p[10];
  {
    v4u hr[2];
    v2u hro;
    f4 acc[5];
    f2 hf[10];
    const unsigned vo[4] = {(unsigned)lane * 16, (unsigned)lane * 16, (unsigned)lane * 16, (unsigned)lane * 16};
    gemm_ks(ws, O_H1 + (4 * tm * 20 + 5 * w) * 1024, vo, b2, L, w, lane, acc, [&](int n) {
      lb4(n < 5 ? 9 + n : 28 + n);
    }, [&](int n, int t) {
      if (t == 3 && n == 2) hr[0] = hld128(ws, lane * 16, O_HP + own_m);
      if (t == 3 && n == 4) hr[1] = hld128(ws, lane * 16 + 1024, O_HP + own_m);
      if (t == 3 && n == 6) hro = hld64(ws, own_r, O_HP + own_s);
      if (n == 18) {
        unsigned h0 = t < 4 ? hr[t >> 1][2 * (t & 1)] : hro.x;
        KEEP(h0);
        hf[2 * t] = up2(h0);
        KEEP(hf[2 * t]);
      }
    });
#pragma unroll
    for (int t = 0; t < 5; ++t) {
      const unsigned h1 = t < 4 ? hr[t >> 1][2 * (t & 1) + 1] : hro.y;
      const f2 p = f2{acc[t][0], acc[t][1]} + hf[2 * t], q = f2{acc[t][2], acc[t][3]} + up2(h1);
      x1p[2 * t] = pk2(p.x, p.y);
      x1p[2 * t + 1] = pk2(q.x, q.y);
    }
    const auto s0 = __builtin_amdgcn_permlane16_swap(x1p[8], x1p[8], false, false);
    const auto s1 = __builtin_amdgcn_permlane16_swap(x1p[9], x1p[9], false, false);
    hst128(v4u{x1p[0], x1p[1], x1p[2], x1p[3]}, ws, x1m, 0);
    hst128(v4u{x1p[4], x1p[5], x1p[6], x1p[7]}, ws, x1m + 1024, 0);
    hst128(v4u{s0[0], s1[0], s0[1], s1[1]}, ws, x1s, 0);
    float t1 = 0.f, t2 = 0.f;
#pragma unroll
    for (int i = 0; i < 10; ++i) {
      t1 = __builtin_amdgcn_fdot2_f32_bf16(__builtin_bit_cast(bf2, x1p[i]), __builtin_bit_cast(bf2, 0x3f803f80u), t1, false);
      t2 = __builtin_amdgcn_fdot2_f32_bf16(__builtin_bit_cast(bf2, x1p[i]), __builtin_bit_cast(bf2, x1p[i]), t2, false);
    }
    t1 = swap32_add(t1, t2);
    t1 = swap16_add(t1, t1);
    hst32(__builtin_bit_cast(unsigned, t1), ws, stoff, 0);
    f4 cst;
    {
      f4 cp[4];
#pragma unroll
      for (int v = 0; v < 4; ++v) cp[v] = *(LAS f4*)(L + (v * 25 + 24) * 1024 + lane * 16);
      cst = (cp[0] + cp[1]) + (cp[2] + cp[3]);
      KEEP(cst);
    }
    PIN();
    DRAIN();
    WGBAR();
    if (w == 0) hst64(tag, ws, f2pub, 0);
    LB4(14, 23);
    PIN();
    img3(24, 35, 0);
    if (w == 1) *(LAS f4*)(L + CSUM_OFF + lane * 16) = cst;
    if (w == 0) poll64(ws, f2wait, tag);
    WGBAR();
  }
  constexpr int WIN4 = 8, RING4 = WIN4 + 1, PUBK = 10, PUBN = 9, SMPN = 20, CHKN = 26;
  v4u a4[RING4];
  auto lda4 = [&](int n) {
    const int i = n < 32 ? n >> 3 : (n - 32) / 6, k = ks4(n < 32 ? n & 7 : 8 + (n - 32) % 6);
    a4[n % RING4] = hld128(ws, lane * 16 + (k & 3) * 1024, O_H + ((4 * tm + ((w + 1 + i) & 3)) * 56 + 14 * w + (k & ~3)) * 1024);
  };
  {
    constexpr int W3 = 6;
    v4u a[20][2];
    v2u sv[2][2];
    const unsigned sa = O_X1 + (8 * rg + 2 * w) * 20 * 1024;
    auto lda = [&](int k, int i) { a[k][i] = hld128(ws, lane * 16 + (k & 3) * 1024, sa + i * 20480 + (k >> 2) * 4096); };
#pragma unroll
    for (int k = 0; k < W3; ++k)
#pragma unroll
      for (int i = 0; i < 2; ++i) {
        lda(k, i);
        if (k == 1) {
          sv[i][0] = hld64(ws, l15 * 8 + g * 256, O_STAT + (8 * rg + 2 * w + i) * 1024);
          sv[i][1] = hld64(ws, l15 * 8 + g * 256 + 128, O_STAT + (8 * rg + 2 * w + i) * 1024);
        }
        PIN();
      }
    f4 acc[2][7];
    f2 gc0, gc1, den, mean;
    unsigned hs, hs1, lcs;
    auto bslot = [](int k, int t) { const int v = k / 5, i = (k % 5) * 7 + t; return (i < 25 ? v * 25 + i : 75 + v * 10 + i) * 1024; };
    const LAS char* Lb = L + lane * 16;
    const LAS char* Lb2 = L + lane * 16 + 65536;
    const LAS char* Lb3 = L + lane * 16 + 131072;
    auto rdb = [&](int k, int t) { const int o = bslot(k, t); return *(const LAS v4u*)(o < 65536 ? Lb + o : (o < 131072 ? Lb2 + (o - 65536) : Lb3 + (o - 131072))); };
    v4u bf[3][4];
#pragma unroll
    for (int t = 0; t < 8; ++t) {
      bf[t >> 2][t & 3] = rdb(t >> 2, t & 3);
      PIN();
    }
    sfor<0, 20>([&](auto ic) {
      constexpr int k = decltype(ic)::v;
      __builtin_amdgcn_s_waitcnt(k < 19 ? 0xC47F : 0xC37F);
#pragma unroll
      for (int t = 0; t < 4; ++t) {
        if (k == 0) mfma_va0(acc[0][t], bf[0][t], a[0][0]);
        else mfma_va(acc[0][t], bf[k % 3][t], a[k][0]);
        if (k < 18) bf[(k + 2) % 3][t] = rdb(k + 2, t);
        else if (t < 3) bf[(k + 2) % 3][t] = rdb(k - 18, 4 + t);
        PIN();
      }
#pragma unroll
      for (int t = 0; t < 4; ++t) {
        if (k == 0) mfma_va0(acc[1][t], bf[0][t], a[0][1]);
        else mfma_va(acc[1][t], bf[k % 3][t], a[k][1]);
        if (t < 2) {
          if constexpr (k + W3 < 20) lda(k + W3, t);
        }
        if (t >= (k + W3 < 20 ? 2 : 0) && t < (k + W3 < 20 ? 4 : 2)) {
          if constexpr (k >= 14 && k < 18) lb4(40 + 2 * (k - 14) + (t & 1));
        }
        if (k == 4 && t == 3) {
          asm volatile("" : "+v"(sv[0][0]), "+v"(sv[0][1]), "+v"(sv[1][0]), "+v"(sv[1][1]));
          const f2 p0 = __builtin_bit_cast(f2, sv[0][0]) + __builtin_bit_cast(f2, sv[0][1]);
          const f2 p1 = __builtin_bit_cast(f2, sv[1][0]) + __builtin_bit_cast(f2, sv[1][1]);
          f2 r, mr;
          ln_scal(f2{xrow_sum(p0.x), xrow_sum(p1.x)}, f2{xrow_sum(p0.y), xrow_sum(p1.y)}, r, mr, &mean, &den);
          gc0 = r * GC0;
          gc1 = r * r * r * GC1;
          hs = O_H + ((8 * rg + 2 * w) * 56 + 7 * (cg >> 1) + 4 * (cg & 1)) * 1024 + lane * 16;
          hs1 = O_H + ((8 * rg + 2 * w) * 56 + 7 * (cg >> 1) + 3) * 1024 + (cg & 1) * 512 + (g & 1) * 57344 + (l15 + 16 * (g >> 1)) * 16;
          lcs = CSUM_OFF + g * 16;
          asm volatile("" : "+v"(gc0), "+v"(gc1), "+v"(mean), "+v"(den), "+v"(hs), "+v"(hs1), "+v"(lcs));
        }
        if (k == 19) csp[t] = *(LAS f4*)(L + lcs + t * 64);
        PIN();
      }
    });
    MFENCE();
    PIN();
#pragma unroll
    for (int i = 0; i < 2; ++i) {
      f2 v[8];
      unsigned o[8];
#pragma unroll
      for (int e = 0; e < 8; ++e) {
        const int t = e >> 1, h = 2 * (e & 1);
        v[e] = f2{acc[i][t][h], acc[i][t][h + 1]} - f2{csp[t][h], csp[t][h + 1]} * mean[i];
      }
      gelu_pack<8>(v, gc0[i], gc1[i], den[i], o, [&](int m) {
        if (i == 0) lb4(28 + m);
        else if (m == 0) lb4(32);
      });
      hst128(v4u{o[0], o[1], o[2], o[3]}, ws, hs, i * 57344);
      hst128(v4u{o[4], o[5], o[6], o[7]}, ws, hs, i * 57344 + 1024);
    }
    YOUNG();
    sfor<0, 20>([&](auto ic) {
      constexpr int k = decltype(ic)::v;
      __builtin_amdgcn_s_waitcnt(k < 19 ? 0xC37F : 0xC07F);
      if (k == PUBK) {
        WAITV(10);
        hst64(tag, ws, f3pub, 0);
      }
#pragma unroll
      for (int e = 0; e < 6; ++e) {
        const int i = e / 3, t = e % 3;
        if (k == 0) mfma_va0(acc[i][4 + t], bf[2][t], a[0][i]);
        else mfma_va(acc[i][4 + t], bf[(k + 2) % 3][t], a[k][i]);
        if (i == 0 && k < 18) bf[(k + 1) % 3][t] = rdb(k + 2, 4 + t);
        if (k == 19 && i == 0) csp[4 + t] = *(LAS f4*)(L + lcs + (4 + t) * 64);
        if (k >= 1 && k < 5 && e == 5) lb4(47 + k);
        if (k >= 1 && k < 19 && e == 3) lb4(51 + k);
        PIN();
      }
    });
    v2u fl;
    MFENCE();
    PIN();
    unsigned os[2][2];
#pragma unroll
    for (int i = 0; i < 2; ++i) {
      f2 v[6];
      unsigned o[6];
#pragma unroll
      for (int e = 0; e < 6; ++e) {
        const int t = 4 + (e >> 1), h = 2 * (e & 1);
        v[e] = f2{acc[i][t][h], acc[i][t][h + 1]} - f2{csp[t][h], csp[t][h + 1]} * mean[i];
      }
      gelu_pack<6>(v, gc0[i], gc1[i], den[i], o, [&](int m) {
        if (i == 0 && m == 0) fl = hld64(ws, f3wait, 0);
      });
      hst128(v4u{o[0], o[1], o[2], o[3]}, ws, hs, i * 57344 + 2048);
      os[i][0] = o[4];
      os[i][1] = o[5];
      if (i == 0) {
        PIN();
        KEEP(fl);
        if (!__all(f3wait >= OOB || (fl.x == tag.x && fl.y == tag.y))) poll64(ws, f3wait, tag);
        __builtin_amdgcn_fence(__ATOMIC_ACQUIRE, "wavefront");
#pragma unroll
        for (int n = 0; n < WIN4; ++n) {
          lda4(n);
          PIN();
        }
      }
    }
    {
      const auto s0 = __builtin_amdgcn_permlane16_swap(os[0][0], os[1][0], false, false);
      const auto s1 = __builtin_amdgcn_permlane16_swap(os[0][1], os[1][1], false, false);
      hst128(v4u{s0[0], s1[0], s0[1], s1[1]}, ws, hs1, 0);
    }
    YOUNG();
  }
  {
    v4u gb[2];
    v2u gbo, fl;
    f4 ac[4][5], p[15];
    float gf[20], cf[20];
    unsigned lq = lane;
    KEEP(lq);
    const unsigned lr = RED_OFF + lq * 16, gq4 = lq >> 4;
    unsigned dm = ((64 * tm + 16 * w + (lq & 15)) * D_ + 80 * tn) * 2, ds = dm + 8 * gq4 + ((tn & 1) ? 0 : 128);
    dm += 16 * gq4 + 32 * (tn & 1);
    unsigned xro_off = ((lq & 15) + 16 * (gq4 >> 1)) * 16 + 8 * (gq4 & 1);
    sfor<0, 56>([&](auto ic) {
      constexpr int n = decltype(ic)::v, i = n < 32 ? n >> 3 : (n - 32) / 6, sl = n < 32 ? n & 7 : 8 + (n - 32) % 6;
      if (n == 54) {
        WGBAR();
      }
      if (n == PUBN) {
        WAITV(7);
        hst64(tag, ws, f3pub + (O_F4 - O_F3), 0);
      }
      if (n == 23) asm volatile("" : "+v"(dm), "+v"(ds), "+v"(xro_off));
      if (n == 37) asm volatile("s_barrier" ::: "memory");
      if (n == CHKN) {
        KEEP(fl);
        if (!__all(f3wait >= OOB || (fl.x == tag.x && fl.y == tag.y))) poll64(ws, f3wait + (O_F4 - O_F3), tag);
        __builtin_amdgcn_fence(__ATOMIC_ACQUIRE, "wavefront");
#pragma unroll
        for (int m = 32; m < CHKN + WIN4; ++m) lda4(m);
        PIN();
      }
#pragma unroll
      for (int t = 0; t < 5; ++t) {
        const int f = sl * 5 + t;
        if (sl == 0) mfma_aa0(ac[i][t], b4[f], a4[n % RING4]);
        else if (f >= 22 && f < 48) mfma_va(ac[i][t], b4[f], a4[n % RING4]);
        else mfma_aa(ac[i][t], b4[f], a4[n % RING4]);
        if (t == 1) {
          if constexpr (n + WIN4 < 56) {
            if (n < 32 - WIN4 || n >= CHKN) lda4(n + WIN4);
          }
        }
        if (t == 3) {
          if (n == 30 || n == 31) gb[n & 1] = hld128(ws, gq4 * 16 + (n & 1) * 64, O_MOD + (5 * D_ + 80 * tn + 16 * (tn & 1)) * 2);
          if (n == 32 || n == 33) xr[n & 1] = hld128(ws, lq * 16 + (n & 1) * 1024, O_X + own_m);
          if (n == 34) gbo = hld64(ws, gq4 * 8, O_MOD + (5 * D_ + 80 * tn + ((tn & 1) ? 0 : 64)) * 2);
          if (n == 35) xro = hld64(ws, xro_off, O_X + own_s);
        }
        if constexpr (n >= 44 && n < 54) {
          constexpr int q = n - 44;
          const unsigned gw = q < 8 ? gb[(q >> 2) & 1][q & 3] : gbo[q & 1], xw = q < 8 ? xr[(q >> 2) & 1][q & 3] : xro[q & 1];
          float tmp;
          if (t == 1) asm volatile("v_lshlrev_b32 %0, 16, %2\n\tv_and_b32 %1, 0xffff0000, %2" : "=&v"(gf[2 * q]), "=v"(gf[2 * q + 1]) : "v"(gw));
          if (t == 2) asm volatile("v_lshlrev_b32 %0, 16, %2\n\tv_and_b32 %1, 0xffff0000, %2" : "=&v"(cf[2 * q]), "=v"(cf[2 * q + 1]) : "v"(xw));
          if (t == 3) asm volatile("v_lshlrev_b32 %1, 16, %2\n\tv_fmac_f32 %0, %1, %3" : "+v"(cf[2 * q]), "=&v"(tmp) : "v"(x1p[q]), "v"(gf[2 * q]));
          if (t == 4) asm volatile("v_and_b32 %1, 0xffff0000, %2\n\tv_fmac_f32 %0, %1, %3" : "+v"(cf[2 * q + 1]), "=&v"(tmp) : "v"(x1p[q]), "v"(gf[2 * q + 1]));
        }
        if (n == SMPN && t == 0) fl = hld64(ws, f3wait + (O_F4 - O_F3), 0);
        if (n >= 38 && sl < 11 && (t == 0 || t == 2) && (sl - 8) * 2 + (t == 2) > 0) {
          const int u = (sl - 8) * 2 + (t == 2) - 1;
          *(LAS f4*)(L + (lr + ((w + i) & 3) * 15360) + ((i - 1) * 5 + u) * 1024) = ac[i > 0 ? i - 1 : 0][u];
        }
        if (n == 54) {
          p[t] = *(LAS f4*)(L + (lr + w * 15360) + t * 1024);
          p[5 + t] = *(LAS f4*)(L + (lr + w * 15360) + (5 + t) * 1024);
        }
        if (n == 55) p[10 + t] = *(LAS f4*)(L + (lr + w * 15360) + (10 + t) * 1024);
        PIN();
      }
    });
    MFENCE();
    PIN();
    unsigned o[10];
#pragma unroll
    for (int t = 0; t < 5; ++t) {
      const f4 y = (ac[3][t] + p[t]) + (p[5 + t] + p[10 + t]);
      const f2 u = f2{y[0], y[1]} * f2{gf[4 * t], gf[4 * t + 1]} + f2{cf[4 * t], cf[4 * t + 1]};
      const f2 v = f2{y[2], y[3]} * f2{gf[4 * t + 2], gf[4 * t + 3]} + f2{cf[4 * t + 2], cf[4 * t + 3]};
      o[2 * t] = pk2(u.x, u.y);
      o[2 * t + 1] = pk2(v.x, v.y);
    }
    const rsrc_t ro = mkrs(gout, SD2);
    __builtin_amdgcn_raw_buffer_store_b128(v4u{o[0], o[1], o[2], o[3]}, ro, dm, 0, 0);
    __builtin_amdgcn_raw_buffer_store_b128(v4u{o[4], o[5], o[6], o[7]}, ro, dm + 64, 0, 0);
    __builtin_amdgcn_raw_buffer_store_b64(v2u{o[8], o[9]}, ro, ds, 0, 0);
  }
  asm volatile(".Lfused_end:" ::: "memory");
}

extern "C" void kernel_launch(void* const* d_in, const int* in_sizes, int n_in, void* d_out, int out_size,
                              void* d_ws, size_t ws_size, hipStream_t stream) {
  (void)hipFuncSetAttribute((const void*)fused, hipFuncAttributeMaxDynamicSharedMemorySize, LDS_BYTES);
  fused<<<256, 256, LDS_BYTES, stream>>>(d_in[0], d_in[1], d_in[2], d_in[3], d_in[4], d_in[5], d_in[7], d_in[8], d_in[9],
                                         d_in[10], d_in[11], d_in[12], d_out, d_ws);
}
```

```cpp
#include <hip/hip_runtime.h>
#include <stdint.h>

typedef unsigned v4u __attribute__((ext_vector_type(4)));
typedef unsigned v2u __attribute__((ext_vector_type(2)));
typedef float f4 __attribute__((ext_vector_type(4)));
typedef float f2 __attribute__((ext_vector_type(2)));
typedef short s4v __attribute__((ext_vector_type(4)));
typedef __bf16 bf2 __attribute__((ext_vector_type(2)));
typedef __bf16 bfx8 __attribute__((ext_vector_type(8)));
typedef __amdgpu_buffer_rsrc_t rsrc_t;
#define LAS __attribute__((address_space(3)))
#define GAS __attribute__((address_space(1)))
#define DI __device__ __forceinline__

static constexpr int S_ = 2048, D_ = 640, F_ = 1728;
static constexpr unsigned OOB = 0x80000000u;

static constexpr unsigned O_EPOCH = 0;
static constexpr unsigned O_FP = 1024;
static constexpr unsigned O_FR = O_FP + 16 * 128;
static constexpr unsigned O_F1 = O_FR + 32 * 128;
static constexpr unsigned O_F2 = O_F1 + 32 * 128;
static constexpr unsigned O_F3 = O_F2 + 16 * 128;
static constexpr unsigned O_F4 = O_F3 + 16 * 512;
static constexpr unsigned O_CNT = O_F4 + 16 * 512;
static constexpr unsigned O_FW = O_CNT + 64 * 128;
static constexpr unsigned O_FB = O_FW + 8192;
static constexpr unsigned O_MOD = 65536;
static constexpr unsigned O_STAT = O_MOD + 16384;
static constexpr unsigned O_KVT = O_STAT + 131072;
static constexpr unsigned SD2 = S_ * D_ * 2;
static constexpr unsigned KVP = 4096 + 128, ZP = 20480 + 128;
static constexpr unsigned O_ZT = O_KVT + D_ * KVP;
static constexpr unsigned O_A1 = O_ZT + 128 * ZP;
static constexpr unsigned O_HP = O_A1 + SD2;
static constexpr unsigned O_X = O_HP + SD2;
static constexpr unsigned O_H1 = O_X + SD2;
static constexpr unsigned O_X1 = O_H1 + SD2;
static constexpr unsigned O_H = O_X1 + SD2;
static constexpr unsigned O_W1 = O_H + 128 * 56 * 1024;
static constexpr unsigned O_W2 = O_W1 + 800 * 1024;
static constexpr unsigned O_W3 = O_W2 + 800 * 1024;
static constexpr unsigned O_W4 = O_W3 + 2240 * 1024;
static constexpr unsigned O_END = O_W4 + 2240 * 1024;

static constexpr int CS = 4160;
static constexpr int UOFF = 8 * CS;
static constexpr int WR = 8 * CS + 4608;
static constexpr int LDS_BYTES = 160 * 1024;
static constexpr int RED_OFF = 100 * 1024;
static constexpr int CSUM_OFF = 140 * 1024;
static constexpr unsigned SPIN_CAP = 200000;

template <int V> struct IC { static constexpr int v = V; };
template <int I, int N, class F> DI void sfor(F&& f) {
  if constexpr (I < N) {
    f(IC<I>{});
    sfor<I + 1, N>(f);
  }
}
DI float lo2f(unsigned w) { return __builtin_bit_cast(float, w << 16); }
DI float hi2f(unsigned w) { return __builtin_bit_cast(float, w & 0xffff0000u); }
DI f2 up2(unsigned w) { return f2{lo2f(w), hi2f(w)}; }
DI unsigned pk2(float a, float b) { return __builtin_bit_cast(unsigned, __builtin_convertvector(f2{a, b}, bf2)); }
DI f4 mfma(v4u a, v4u b, f4 c) {
  return __builtin_amdgcn_mfma_f32_16x16x32_bf16(__builtin_bit_cast(bfx8, a), __builtin_bit_cast(bfx8, b), c, 0, 0, 0);
}
DI void mfma_v(f4& c, v4u a, v4u b) { asm volatile("v_mfma_f32_16x16x32_bf16 %0, %1, %2, %0" : "+v"(c) : "v"(a), "v"(b)); }
DI void mfma_v0(f4& c, v4u a, v4u b) { asm volatile("v_mfma_f32_16x16x32_bf16 %0, %1, %2, 0" : "=&v"(c) : "v"(a), "v"(b)); }
DI void mfma_va(f4& c, v4u a, v4u b) { asm volatile("v_mfma_f32_16x16x32_bf16 %0, %1, %2, %0" : "+v"(c) : "v"(a), "a"(b)); }
DI void mfma_va0(f4& c, v4u a, v4u b) { asm volatile("v_mfma_f32_16x16x32_bf16 %0, %1, %2, 0" : "=&v"(c) : "v"(a), "a"(b)); }
DI void mfma_av(f4& c, v4u a, v4u b) { asm volatile("v_mfma_f32_16x16x32_bf16 %0, %1, %2, %0" : "+v"(c) : "a"(a), "v"(b)); }
DI void mfma_av0(f4& c, v4u a, v4u b) { asm volatile("v_mfma_f32_16x16x32_bf16 %0, %1, %2, 0" : "=&v"(c) : "a"(a), "v"(b)); }
DI void mfma_aa(f4& c, v4u a, v4u b) { asm volatile("v_mfma_f32_16x16x32_bf16 %0, %1, %2, %0" : "+v"(c) : "a"(a), "a"(b)); }
DI void mfma_aa0(f4& c, v4u a, v4u b) { asm volatile("v_mfma_f32_16x16x32_bf16 %0, %1, %2, 0" : "=&v"(c) : "a"(a), "a"(b)); }
#define KEEP(x) asm volatile("" : "+v"(x))
#define MFENCE() asm volatile("s_nop 7\n\ts_nop 1" ::: "memory")
DI float add1(float a, float b) { float r; asm("v_add_f32 %0, %1, %2" : "=v"(r) : "v"(a), "v"(b)); return r; }
DI rsrc_t mkrs(const void* p, unsigned bytes) { return __builtin_amdgcn_make_buffer_rsrc((void*)p, 0, bytes, 0x00020000); }
DI v4u ld128(rsrc_t r, unsigned v, unsigned s) { return __builtin_amdgcn_raw_buffer_load_b128(r, v, s, 2); }
DI unsigned ld32(rsrc_t r, unsigned v, unsigned s) { return __builtin_amdgcn_raw_buffer_load_b32(r, v, s, 2); }
DI v4u hld128(rsrc_t r, unsigned v, unsigned s) { return __builtin_amdgcn_raw_buffer_load_b128(r, v, s, 16); }
DI v2u hld64(rsrc_t r, unsigned v, unsigned s) { return __builtin_amdgcn_raw_buffer_load_b64(r, v, s, 16); }
DI void hst128(v4u x, rsrc_t r, unsigned v, unsigned s) { __builtin_amdgcn_raw_buffer_store_b128(x, r, v + s, 0, 16); }
DI void hst64(v2u x, rsrc_t r, unsigned v, unsigned s) { __builtin_amdgcn_raw_buffer_store_b64(x, r, v, s, 16); }
DI void hst32(unsigned x, rsrc_t r, unsigned v, unsigned s) { __builtin_amdgcn_raw_buffer_store_b32(x, r, v, s, 16); }
#define DRAIN() do { asm volatile("s_waitcnt vmcnt(0)" ::: "memory"); __builtin_amdgcn_s_waitcnt(0x0F70); } while (0)
#define WGBAR() asm volatile("s_waitcnt lgkmcnt(0)\n\ts_barrier" ::: "memory")
#define PIN() __builtin_amdgcn_sched_barrier(0)

template <int CTRL> DI float dpp(float x) {
  return __builtin_bit_cast(float, __builtin_amdgcn_mov_dpp(__builtin_bit_cast(int, x), CTRL, 0xf, 0xf, true));
}
DI float xrow_sum(float x) {
  auto s = __builtin_amdgcn_permlane16_swap(__builtin_bit_cast(unsigned, x), __builtin_bit_cast(unsigned, x), false, false);
  x = __builtin_bit_cast(float, (unsigned)s[0]) + __builtin_bit_cast(float, (unsigned)s[1]);
  auto t = __builtin_amdgcn_permlane32_swap(__builtin_bit_cast(unsigned, x), __builtin_bit_cast(unsigned, x), false, false);
  return __builtin_bit_cast(float, (unsigned)t[0]) + __builtin_bit_cast(float, (unsigned)t[1]);
}
DI float swap32_add(float a, float b) {
  auto t = __builtin_amdgcn_permlane32_swap(__builtin_bit_cast(unsigned, a), __builtin_bit_cast(unsigned, b), false, false);
  return __builtin_bit_cast(float, (unsigned)t[0]) + __builtin_bit_cast(float, (unsigned)t[1]);
}
DI float swap16_add(float a, float b) {
  auto t = __builtin_amdgcn_permlane16_swap(__builtin_bit_cast(unsigned, a), __builtin_bit_cast(unsigned, b), false, false);
  return __builtin_bit_cast(float, (unsigned)t[0]) + __builtin_bit_cast(float, (unsigned)t[1]);
}
DI float rdl(float v, int l) { return __builtin_bit_cast(float, __builtin_amdgcn_readlane(__builtin_bit_cast(int, v), l)); }
DI void wave_sum4(f2& s, f2& ss) {
  float v = swap16_add(swap32_add(s.x, s.y), swap32_add(ss.x, ss.y));
  v += dpp<0xB1>(v);
  v += dpp<0x4E>(v);
  v += dpp<0x141>(v);
  v += dpp<0x128>(v);
  s = f2{rdl(v, 0), rdl(v, 32)};
  ss = f2{rdl(v, 16), rdl(v, 48)};
}
DI void poll64(rsrc_t ws, unsigned voff, v2u want) {
  const bool idle = voff >= OOB;
  for (unsigned spin = 0; spin < SPIN_CAP; ++spin) {
    asm volatile("" ::: "memory");
    const v2u v = hld64(ws, voff, 0);
    if (__all(idle || (v.x == want.x && v.y == want.y))) break;
  }
  __builtin_amdgcn_fence(__ATOMIC_ACQUIRE, "wavefront");
}

DI bool wflags_ok(v4u a, int lane, int nl, v2u want) {
  return __all(lane >= nl || (a.x == want.x && a.y == want.y && a.z == want.x && a.w == want.y));
}
DI void pollw(rsrc_t ws, unsigned voff, int lane, int nl, v2u want) {
  for (unsigned spin = 0; spin < SPIN_CAP; ++spin) {
    asm volatile("" ::: "memory");
    if (wflags_ok(hld128(ws, voff, 0), lane, nl, want)) break;
    __builtin_amdgcn_s_sleep(4);
  }
  __builtin_amdgcn_fence(__ATOMIC_ACQUIRE, "wavefront");
}
DI float rbf(float x) { return lo2f(pk2(x, 0.f)); }
DI void ln_scal(f2 s, f2 ss, f2& r, f2& mr, f2* pmean = nullptr, f2* pden = nullptr) {
  const f2 m = s * (1.f / D_);
  const f2 var = ss * (1.f / D_) - m * m;
  const f2 v = f2{fmaxf(var.x, 0.f), fmaxf(var.y, 0.f)} + 1e-5f;
  r = f2{__builtin_amdgcn_rsqf(v.x), __builtin_amdgcn_rsqf(v.y)};
  mr = -m * r;
  if (pmean) *pmean = m;
  if (pden) *pden = v * r;
}
static constexpr float GC0 = -2.f * 0.7978845608028654f * 1.4426950408889634f, GC1 = GC0 * 0.044715f;
template <int N, class Mid> DI void gelu_pack(const f2 (&x)[N], float c0, float c1, float den, unsigned* o, Mid mid) {
  f2 a[N];
#define STAGE(expr) _Pragma("unroll") for (int i = 0; i < N; ++i) a[i] = expr; _Pragma("unroll") for (int i = 0; i < N; ++i) KEEP(a[i]); PIN()
  STAGE(x[i] * x[i]);
  mid(0);
  STAGE(a[i] * c1 + c0);
  STAGE(a[i] * x[i]);
  mid(1);
  STAGE((f2{__builtin_amdgcn_exp2f(a[i].x), __builtin_amdgcn_exp2f(a[i].y)}));
  mid(2);
  STAGE(a[i] * den + den);
  STAGE((f2{__builtin_amdgcn_rcpf(a[i].x), __builtin_amdgcn_rcpf(a[i].y)}));
  mid(3);
  STAGE(a[i] * x[i]);
#undef STAGE
#pragma unroll
  for (int i = 0; i < N; ++i) o[i] = pk2(a[i].x, a[i].y);
}


static constexpr int RT_BYTES = 32 * 896;
template <bool WIDE> struct Job {
  static constexpr int NL = WIDE ? 7 : 5, RB = WIDE ? 896 : 640, PITCH = WIDE ? F_ * 2 : D_ * 2;
  v4u r[NL];
  unsigned dst;
  DI void load(int job, int w, const unsigned (&vo)[NL], const void* w1, const void* w2, const void* wx) {
    const bool big = WIDE || job >= 80;
    const int jj = WIDE ? job : (job >= 80 ? job - 80 : (job >= 40 ? job - 40 : job));
    const int gq = WIDE ? jj & 3 : jj & 1, ks = WIDE ? jj >> 2 : jj >> 1;
    const rsrc_t rs = mkrs(big ? wx : (job >= 40 ? w2 : w1), big ? D_ * F_ * 2 : D_ * D_ * 2);
    const unsigned src = (32 * ks + 8 * w) * PITCH + gq * RB;
    dst = (WIDE ? O_W3 : (job >= 80 ? O_W4 : (job >= 40 ? O_W2 : O_W1))) + ((gq * 4 + w) * (!WIDE && job >= 80 ? 56 : 20) + ks) * (NL * 1024);
#pragma unroll
    for (int i = 0; i < NL; ++i)
      r[i] = ld128(rs, (WIDE && gq == 3 && (vo[i] & 1)) ? OOB : (vo[i] & ~1u) + src, 0);
  }
  DI void put(int w, int lane, LAS char* L) {
#pragma unroll
    for (int i = 0; i < NL; ++i) *(LAS v4u*)(L + w * (8 * RB) + lane * 16 + i * 1024) = r[i];
  }
  DI void emit(int w, int lane, LAS char* L, rsrc_t ws) {
    const int g = lane >> 4, q = (lane & 15) >> 2, p = lane & 3;
    LAS char* row = L + (8 * g + q) * RB + w * (RB / 4);
    LAS char* pr = row + 32 * (w & 1) + 16 * p;
    LAS char* sg = row + ((w & 1) ? 0 : RB / 4 - 32) + 8 * p;
    v2u lo[NL], hi[NL];
#pragma unroll
    for (int f = 0; f < NL; ++f) {
      LAS char* c = f < NL - 1 ? pr + 64 * (f >> 1) + 8 * (f & 1) : sg;
      lo[f] = __builtin_bit_cast(v2u, __builtin_amdgcn_ds_read_tr16_b64_v4i16((LAS s4v*)c));
      hi[f] = __builtin_bit_cast(v2u, __builtin_amdgcn_ds_read_tr16_b64_v4i16((LAS s4v*)(c + 4 * RB)));
    }
#pragma unroll
    for (int f = 0; f < NL; ++f) hst128(v4u{lo[f].x, lo[f].y, hi[f].x, hi[f].y}, ws, lane * 16 + dst, f * 1024);
  }
};

template <class Extra, class Gap>
DI void gemm_ks(rsrc_t ws, unsigned abase, const unsigned (&vo)[4], const v4u* b, LAS char* L, int w, int lane, f4 (&acc)[5],
                Extra extra, Gap gap) {
  constexpr int KS = 5, WIN = 12, NA = 4 * KS, RING = WIN + 1;
  v4u a[RING];
  unsigned so[4];
#pragma unroll
  for (int i = 0; i < 4; ++i) so[i] = abase + ((w + 1 + i) & 3) * 20480;
  auto lda = [&](int n) { a[n % RING] = hld128(ws, vo[n / KS] + ((n % KS) & 3) * 1024, so[n / KS] + ((n % KS) >> 2) * 4096); };
#pragma unroll
  for (int n = 0; n < WIN; ++n) {
    lda(n);
    PIN();
  }
  f4 ac[2][5], p[15];
  unsigned lr = RED_OFF + lane * 16;
  KEEP(lr);
  sfor<0, NA>([&](auto ic) {
    constexpr int n = decltype(ic)::v, i = n / KS, k = n % KS;
    if (n == NA - 3) {
      WGBAR();
    }
#pragma unroll
    for (int t = 0; t < 5; ++t) {
      if (k == 0) mfma_av0(ac[i & 1][t], b[t], a[n % RING]);
      else mfma_av(ac[i & 1][t], b[k * 5 + t], a[n % RING]);
      if (t == 1) {
        if constexpr (n + WIN < NA) lda(n + WIN);
      }
      if (t == (n + WIN < NA ? 4 : 1) && n >= NA - 12) extra(n - (NA - 12));
      if (k == 0 && i > 0 && t > 0) *(LAS f4*)(L + (lr + ((w + i) & 3) * 15360) + ((i - 1) * 5 + t - 1) * 1024) = ac[(i - 1) & 1][t - 1];
      if (k == 1 && i > 0 && t == 0) *(LAS f4*)(L + (lr + ((w + i) & 3) * 15360) + ((i - 1) * 5 + 4) * 1024) = ac[(i - 1) & 1][4];
      if (n == NA - 3) {
        p[t] = *(LAS f4*)(L + (lr + w * 15360) + t * 1024);
        p[5 + t] = *(LAS f4*)(L + (lr + w * 15360) + (5 + t) * 1024);
      }
      if (n == NA - 2) p[10 + t] = *(LAS f4*)(L + (lr + w * 15360) + (10 + t) * 1024);
      if (n == NA - 1) {
        p[t][0] = add1(p[t][0], p[5 + t][0]);
        p[t][1] = add1(p[t][1], p[5 + t][1]);
        KEEP(p[t]);
      }
      gap(n, t);
      PIN();
    }
  });
#pragma unroll
  for (int t = 0; t < 5; ++t) {
    p[t][2] += p[5 + t][2];
    p[t][3] += p[5 + t][3];
    p[t] += p[10 + t];
    KEEP(p[t]);
  }
  PIN();
#pragma unroll
  for (int t = 0; t < 5; ++t) acc[t] = ac[1][t] + p[t];
}

#define YOUNG() do { asm volatile("; YOUNG" ::: "memory"); PIN(); } while (0)
#define WAITV(n) asm volatile("s_waitcnt vmcnt(" #n ") ; WAITV" ::: "memory")
__global__ __launch_bounds__(256, 1) void fused(const void*, const void*, const void*, const void*, const void*, const void*,
                                                const void*, const void*, const void*, const void*, const void*, const void*,
                                                void*, void*) {
  extern __shared__ __attribute__((aligned(16))) char smem[];
  LAS char* L = (LAS char*)smem;
  unsigned pf = 0;
  if (blockIdx.x >= 160) {
    unsigned long long pc;
    unsigned off = (((blockIdx.x >> 3) & 1) * 256 + threadIdx.x) * 128;
    asm volatile("s_getpc_b64 %1\n.Lfused_pc:\n\tv_min_u32 %2, .Lfused_end-.Lfused_pc, %2\n\tglobal_load_dword %0, %2, %1"
                 : "=&v"(pf), "=&s"(pc), "+v"(off));
  }
  PIN();
  typedef void* const __attribute__((address_space(4))) * karg_t;
  const karg_t ka = (karg_t)__builtin_amdgcn_kernarg_segment_ptr();
  void *gq = ka[0], *gk = ka[1], *gv = ka[2], *gsk = ka[3], *gssm = ka[4], *gD = ka[5], *gw1 = ka[6], *gw2 = ka[7], *gw3 = ka[8],
       *gw4 = ka[9], *gc = ka[10], *gwm = ka[11], *gout = ka[12], *gws = ka[13];
  asm volatile("" ::"s"(gq), "s"(gk), "s"(gv), "s"(gsk), "s"(gssm), "s"(gD), "s"(gw1), "s"(gw2), "s"(gw3), "s"(gw4),
               "s"(gc), "s"(gwm), "s"(gout), "s"(gws));
  const int tid = threadIdx.x, lane = tid & 63;
  const int w = __builtin_amdgcn_readfirstlane(tid >> 6);
  const int b = blockIdx.x, x = b & 7, j = b >> 3;
  const int tm = 4 * x + (j >> 3), tn = j & 7;
  const int rg = 2 * x + (j >> 4), cg = j & 15;
  const bool conv_wg = j < 20;
  const rsrc_t ws = mkrs(gws, O_END);
  const int g = lane >> 4, l15 = lane & 15;

  const int rt0 = 64 * tm + 8 * tn + 2 * w;
  const int rcm = 8 * lane, rco = 512 + 2 * lane;
  v4u qm[2];
  unsigned qo[2];
  auto load_q = [&]() {
    const rsrc_t rq = mkrs(gq, SD2);
#pragma unroll
    for (int t = 0; t < 2; ++t) {
      qm[t] = ld128(rq, rcm * 2, (rt0 + t) * D_ * 2);
      qo[t] = ld32(rq, rco * 2, (rt0 + t) * D_ * 2);
    }
  };
  v2u tag;
  auto make_tag = [&](v2u ep) {
    tag.x = __builtin_amdgcn_readfirstlane(ep.x) * 0x9E3779B1u + 0x7F4A7C15u;
    tag.y = (__builtin_amdgcn_readfirstlane(ep.y) ^ tag.x) * 0x85EBCA77u + 0x2545F491u;
    tag.x &= 0xffffff00u;
    tag.y |= 1u;
  };
  v4u b1[25], b2[25];
  const unsigned sb1 = O_W1 + (tn * 100 + w * 25) * 1024, sb2 = O_W2 + (tn * 100 + w * 25) * 1024;
  auto zero_h1 = [&](int i) {
    hst128(v4u{0u, 0u, 0u, 0u}, ws, (i < 2 || tid < 128) ? tid * 16 : OOB, O_H1 + b * 10240 + i * 4096);
  };
  v4u mm[5];
  unsigned mo[5];
  auto load_mod = [&](bool ok) {
    unsigned o1 = (ok ? 0u : OOB) | (lane * 16), o2 = (ok ? 0u : OOB) | (lane * 4 + 1024);
    asm volatile("" : "+v"(o1), "+v"(o2));
#pragma unroll
    for (int i = 0; i < 5; ++i) {
      mm[i] = hld128(ws, o1, O_MOD + i * D_ * 2);
      mo[i] = __builtin_amdgcn_raw_buffer_load_b32(ws, o2, O_MOD + i * D_ * 2, 16);
    }
  };
  f2 qv[10], n1[10];
  auto ln1 = [&]() {
#pragma unroll
    for (int i = 0; i < 4; ++i) {
      qv[2 * i] = f2{lo2f(qm[0][i]), lo2f(qm[1][i])};
      qv[2 * i + 1] = f2{hi2f(qm[0][i]), hi2f(qm[1][i])};
    }
    qv[8] = f2{lo2f(qo[0]), lo2f(qo[1])};
    qv[9] = f2{hi2f(qo[0]), hi2f(qo[1])};
    f2 s = qv[0], ss = qv[0] * qv[0];
#pragma unroll
    for (int i = 1; i < 10; ++i) { s += qv[i]; ss += qv[i] * qv[i]; }
    wave_sum4(s, ss);
    f2 r, mr;
    ln_scal(s, ss, r, mr);
#pragma unroll
    for (int i = 0; i < 10; ++i) n1[i] = qv[i] * r + mr;
  };
  unsigned z3off, im0, io0, roff, roff3, own_r, x1m, x1s, stoff, h1m, h1s, f1pub, f1wait, frpub, frwait, f2pub, f2wait, f3pub, f3wait;
  const unsigned own_m = ((4 * tm + w) * 20 + 5 * (tn >> 1) + 3 * (tn & 1)) * 1024;
  const unsigned own_s = ((4 * tm + w) * 20 + 5 * (tn >> 1) + 2) * 1024 + (tn & 1) * 512;
  auto offsets = [&]() {
    z3off = tid < 128 ? tid * 32 + 16384 : OOB;
    im0 = RED_OFF + 16384 + lane * 128 + (((2 * w) ^ (lane & 7)) << 4);
    io0 = RED_OFF + 16384 + (64 + (lane >> 2)) * 128 + (((2 * w) ^ ((lane >> 2) & 7)) << 4) + 4 * (lane & 3);
    roff = O_A1 + ((4 * tm + (tn >> 1)) * 20 + (tid >> 5)) * 1024 + (((tid ^ (tid >> 3)) & 7) + 8 * (tn & 1) + 16 * ((tid >> 3) & 3)) * 16;
    roff3 = tid < 128 ? roff : OOB;
    own_r = (l15 + 16 * (g >> 1)) * 16 + 8 * (g & 1);
    x1m = O_X1 + own_m + lane * 16;
    x1s = (g & 1) ? OOB + SD2 : O_X1 + own_s + (l15 + 16 * (g >> 1)) * 16;
    stoff = (g & 1) ? OOB : O_STAT + ((4 * tm + w) * 8 + tn) * 128 + l15 * 8 + (g >> 1) * 4;
    f3pub = lane == 0 ? O_F3 + rg * 512 + (w >> 1) * 256 + cg * 16 + (w & 1) * 8 : OOB;
    f3wait = lane < 8 ? O_F3 + rg * 512 + (tm & 1) * 256 + w * 64 + lane * 8 : OOB;
    const int sh = (tn >> 1) == 0 ? 1 : ((tn >> 1) == 1 ? -1 : ((tn >> 1) == 2 ? 2 : -2));
    const int t2 = 64 * tm + 16 * w + l15 + sh;
    const bool ok = t2 >= 0 && t2 < S_;
    const unsigned d = O_H1 + ((t2 >> 4) * 20 + 5 * (tn >> 1)) * 1024 + (t2 & 15) * 16;
    h1m = ok ? d + 3072 * (tn & 1) + 256 * g : OOB;
    h1s = (ok && !(g & 1)) ? d + 2048 + 512 * (tn & 1) + 256 * (g >> 1) : OOB;
    const int nb = tm + (sh > 0 ? 1 : -1);
    f1pub = OOB;
    if (lane == 0) f1pub = O_F1 + tm * 128 + tn * 8;
    if (lane == 1 && nb >= 0 && nb < 32) f1pub = O_F1 + nb * 128 + 64 + tn * 8;
    const bool up = ((lane >> 1) & 1) == 0;
    f1wait = (lane < 8 || (lane < 16 && (up ? tm > 0 : tm < 31))) ? O_F1 + tm * 128 + lane * 8 : OOB;
    frpub = lane == 0 ? O_FR + tm * 128 + tn * 8 : OOB;
    frwait = lane < 8 ? O_FR + tm * 128 + lane * 8 : OOB;
    f2pub = lane == 0 ? O_F2 + (tm >> 1) * 128 + ((tm & 1) * 8 + tn) * 8 : OOB;
    f2wait = lane < 16 ? O_F2 + rg * 128 + lane * 8 : OOB;
    asm volatile("" : "+v"(frpub), "+v"(frwait), "+v"(f2pub), "+v"(f2wait));
    asm volatile("" : "+v"(z3off), "+v"(im0), "+v"(io0), "+v"(roff), "+v"(roff3), "+v"(own_r), "+v"(f1pub), "+v"(f1wait));
    asm volatile("" : "+v"(x1m), "+v"(x1s), "+v"(stoff), "+v"(f3pub), "+v"(f3wait));
    asm volatile("" : "+v"(h1m), "+v"(h1s));
  };
  constexpr int NB2 = 19;
  bool wok = false, bok = false;
  if (conv_wg) {
    const int cgp = j < 16 ? x : 8 + (x >> 2), ci = j < 16 ? j : (x & 3) * 4 + (j - 16);
    const int pT0 = 128 * ci + 32 * w + 8 * ((lane >> 3) & 3) + 4 * (lane >> 5), pC0 = 64 * cgp + 8 * (lane & 7);
    const int ch = 64 * cgp + 4 * ci + w;
    v4u kw[7], vw[4], skw[4], kr[4];
    {
      const rsrc_t rk = mkrs(gk, SD2), rv = mkrs(gv, SD2), rs = mkrs(gsk, D_ * 8);
      const unsigned o = (unsigned)(pT0 * D_ + pC0) * 2;
#pragma unroll
      for (int i = 0; i < 7; ++i) kw[i] = __builtin_amdgcn_raw_buffer_load_b128(rk, o + (i - 3) * D_ * 2, 0, 0);
#pragma unroll
      for (int i = 0; i < 4; ++i) vw[i] = ld128(rv, o + i * D_ * 2, 0);
#pragma unroll
      for (int i = 0; i < 4; ++i) skw[i] = __builtin_amdgcn_raw_buffer_load_b128(rs, pC0 * 8 + i * 16, 0, 0);
      PIN();
      offsets();
      PIN();
      asm volatile("s_waitcnt vmcnt(4)" ::: "memory");
      const rsrc_t rK = mkrs((const char*)gssm + (size_t)ch * 4096, 4096);
#pragma unroll
      for (int i = 0; i < 4; ++i) kr[i] = ld128(rK, lane * 16 + i * 1024, 0);
    }
    const v2u ep = hld64(ws, lane == 0 ? O_EPOCH : O_FP + x * 256 + (j >= 16) * 128, 0);
    const unsigned dvb = __builtin_amdgcn_raw_buffer_load_b16(mkrs(gD, D_ * 2), ch * 2, 0, 0);
    PIN();
    {
      unsigned pe[8][2];
#pragma unroll
      for (int pr = 0; pr < 4; ++pr) {
        const f2 w0 = {lo2f(skw[pr].x), lo2f(skw[pr].z)}, w1 = {hi2f(skw[pr].x), hi2f(skw[pr].z)};
        const f2 w2 = {lo2f(skw[pr].y), lo2f(skw[pr].w)}, w3 = {hi2f(skw[pr].y), hi2f(skw[pr].w)};
        f2 kf[7], o[4];
#pragma unroll
        for (int i = 0; i < 7; ++i) kf[i] = up2(kw[i][pr]);
#pragma unroll
        for (int i = 0; i < 4; ++i) o[i] = (kf[i + 3] * w0 + kf[i + 2] * w1 + kf[i + 1] * w2 + kf[i] * w3) * up2(vw[i][pr]);
        pe[2 * pr][0] = pk2(o[3].x, o[2].x);
        pe[2 * pr][1] = pk2(o[1].x, o[0].x);
        pe[2 * pr + 1][0] = pk2(o[3].y, o[2].y);
        pe[2 * pr + 1][1] = pk2(o[1].y, o[0].y);
      }
      const unsigned so = (pC0 + 4 * (lane >> 5)) * KVP + (pT0 & ~7) * 2;
#pragma unroll
      for (int e = 0; e < 4; ++e) {
        const auto s0 = __builtin_amdgcn_permlane32_swap(pe[e][0], pe[e + 4][0], false, false);
        const auto s1 = __builtin_amdgcn_permlane32_swap(pe[e][1], pe[e + 4][1], false, false);
        hst128(v4u{s0[1], s1[1], s0[0], s1[0]}, ws, so, O_KVT + e * KVP);
      }
    }
    PIN();
    LAS char* T = L + w * WR;
    if (lane < 8) *(LAS v4u*)(T + lane * CS) = v4u{0u, 0u, 0u, 0u};
#pragma unroll
    for (int i = 0; i < 4; ++i) *(LAS v4u*)(T + 7 * CS + (i * 64 + lane + 1) * 16) = kr[i];
    asm volatile("s_waitcnt lgkmcnt(0)" ::: "memory");
    v4u kl[4];
#pragma unroll
    for (int i = 0; i < 4; ++i) kl[i] = *(LAS v4u*)(T + 7 * CS + (i * 64 + lane) * 16);
    auto tbuild = [&](int i) {
      const unsigned d[8] = {kl[i].x, kl[i].y, kl[i].z, kl[i].w, kr[i].x, kr[i].y, kr[i].z, kr[i].w};
      unsigned a[7];
#pragma unroll
      for (int e = 0; e < 7; ++e) a[e] = __builtin_amdgcn_alignbit(d[e + 1], d[e], 16);
#pragma unroll
      for (int m = 0; m < 7; ++m) {
        const int h = (m + 1) >> 1;
        const v4u o = (m & 1) ? v4u{d[h], d[h + 1], d[h + 2], d[h + 3]} : v4u{a[h], a[h + 1], a[h + 2], a[h + 3]};
        *(LAS v4u*)(T + m * CS + (i * 64 + lane + 1) * 16) = o;
      }
    };
    tbuild(0);
    make_tag(ep);
    DRAIN();
    asm volatile("s_barrier" ::: "memory");
    if (w == 0) {
      unsigned o = OOB;
      if (j < 16 && lane == 0) o = O_FP + x * 256 + ci * 8;
      if (j >= 16 && lane < 4) o = O_FP + ((x & 4) + lane) * 256 + 128 + ci * 8;
      hst64(tag, ws, o, 0);
    }
    tbuild(1);
    tbuild(2);
    tbuild(3);
    if (lane < 32) *(LAS v4u*)(T + UOFF + lane * 16) = v4u{0u, 0u, 0u, 0u};
    const LAS char* pa = T + (l15 & 7) * CS + (3 - g + (l15 >> 3)) * 16;
    const LAS char* pv = T + UOFF + 512 + (l15 - 1) * 32 + g * 16;
    v4u fa[2][8], fv[2][8];
    asm volatile("s_waitcnt lgkmcnt(0)" ::: "memory");
#pragma unroll
    for (int a = 0; a < 8; ++a) fa[0][a] = *(const LAS v4u*)(pa + 8 * a * 64);
    f4 acc[8];
    v4u ad;
    {
      const int jd = 8 * g - 9 - l15;
#pragma unroll
      for (int e = 0; e < 4; ++e) ad[e] = jd == 2 * e ? dvb : (jd == 2 * e + 1 ? dvb << 16 : 0u);
      asm volatile("" : "+v"(ad));
    }
    const rsrc_t ru = mkrs((const char*)gws + O_KVT + ch * KVP, 4096);
    unsigned v0off = (l15 - 1) * 32 + g * 16, v1off = v0off + 512;
    asm volatile("" : "+v"(v0off), "+v"(v1off));
    if (w == 0) poll64(ws, lane < 16 ? O_FP + x * 256 + (j >= 16) * 128 + lane * 8 : OOB, tag);
    WGBAR();
#pragma unroll
    for (int a = 0; a < 8; ++a) {
      fv[0][a] = hld128(ru, a == 0 ? v0off : v1off + (a - 1) * 512, 0);
      PIN();
    }
    v4u ur[4];
#pragma unroll
    for (int i = 0; i < 4; ++i) ur[i] = hld128(ru, lane * 16 + i * 1024, 0);
    const v4u wf = hld128(ws, O_FW + x * 1024 + lane * 16, 0);
    PIN();
#pragma unroll
    for (int q = 0; q < 8; ++q) mfma_v0(acc[q], ad, fv[0][q]);
    PIN();
#pragma unroll
    for (int i = 0; i < 4; ++i) *(LAS v4u*)(T + UOFF + 512 + i * 1024 + lane * 16) = ur[i];
    unsigned boff = OOB;
    PIN();
#pragma unroll
    for (int g0 = 0; g0 < 8; ++g0) {
      int n = 0;
      if (g0 == 0) load_q();
      if (g0 == 1) {
        wok = wflags_ok(wf, lane, 40, tag);
        __builtin_amdgcn_fence(__ATOMIC_ACQUIRE, "wavefront");
        boff = wok ? lane * 16 : OOB;
      }
#pragma unroll
      for (int a = 0; a < 8; ++a)
#pragma unroll
        for (int q = a; q < 8; ++q) {
          mfma_v(acc[q], fa[g0 & 1][a], fv[g0 & 1][q - a]);
          ++n;
          if ((n & 1) == 0) {
            const int e = n / 2 - 1;
            if (g0 < 7 && e < 16) {
              if (e < 8) fv[(g0 + 1) & 1][e] = *(const LAS v4u*)(pv + e * 512 - (g0 + 1) * 64);
              else fa[(g0 + 1) & 1][e - 8] = *(const LAS v4u*)(pa + (g0 + 1 + 8 * (e - 8)) * 64);
            }
            PIN();
          }
          if (n % 5 == 3) {
            const int i = g0 * 7 + n / 5 - 12;
            if (i < -9) zero_h1(i + 12);
            if (i >= 0 && i < 25) b1[i] = hld128(ws, boff + (i & 3) * 1024, sb1 + (i >> 2) * 4096);
            if (i >= 25 && i < 25 + NB2) b2[i - 25] = hld128(ws, boff + ((i - 25) & 3) * 1024, sb2 + ((i - 25) >> 2) * 4096);
            PIN();
          }
        }
    }
    const v4u bfl = hld128(ws, O_FB + x * 1024 + lane * 16, 0);
    MFENCE();
    PIN();
#pragma unroll
    for (int q = 0; q < 8; q += 2) {
      const auto s0 = __builtin_amdgcn_permlane16_swap(pk2(acc[q][0], acc[q][1]), pk2(acc[q + 1][0], acc[q + 1][1]), false, false);
      const auto s1 = __builtin_amdgcn_permlane16_swap(pk2(acc[q][2], acc[q][3]), pk2(acc[q + 1][2], acc[q + 1][3]), false, false);
      *(LAS v4u*)(T + 64 * w + (32 * (q + (g & 1)) + 2 * l15 + (g >> 1)) * 16) = v4u{s0[0], s1[0], s0[1], s1[1]};
    }
    WGBAR();
    {
      v4u zo[4];
#pragma unroll
      for (int i = 0; i < 4; ++i) zo[i] = *(LAS v4u*)(L + ((lane >> 1) & 3) * (WR + 64) + (64 * w + 16 * i + 2 * (lane >> 3) + (lane & 1)) * 16);
#pragma unroll
      for (int i = 0; i < 4; ++i) hst128(zo[i], ws, (lane >> 3) * ZP + (lane & 7) * 16, O_ZT + (32 * w + 8 * i) * ZP + (ch - w) * 32);
    }
    YOUNG();
    bok = wflags_ok(bfl, lane, 48, tag);
    __builtin_amdgcn_fence(__ATOMIC_ACQUIRE, "wavefront");
    load_mod(bok);
    PIN();
    ln1();
    PIN();
    WAITV(10);
  } else {
    const int hb = (j - 20) * 8 + x;
    unsigned vn[5];
#pragma unroll
    for (int i = 0; i < 5; ++i) vn[i] = (i * 64 + lane) * 16 + (((i * 64 + lane) * 1639u) >> 16) * 640;
    Job<false> j0, j1;
    j0.load(hb, w, vn, gw1, gw2, gw4);
    const v2u ep = hld64(ws, O_EPOCH, 0);
    const v2u cw = __builtin_amdgcn_raw_buffer_load_b64(mkrs(gc, D_ * 2), tid * 8, 0, 0);
    PIN();
    unsigned vw[7];
#pragma unroll
    for (int i = 0; i < 7; ++i) {
      const unsigned c = i * 64 + lane, row = (c * 1171u) >> 16;
      vw[i] = c * 16 + row * 2560 + (c - row * 56 >= 48);
      KEEP(vw[i]);
    }
    offsets();
    LAS float* sc = (LAS float*)(L + 4 * RT_BYTES);
    {
      const float c4[4] = {lo2f(cw.x), hi2f(cw.x), lo2f(cw.y), hi2f(cw.y)};
      f4 sv;
#pragma unroll
      for (int e = 0; e < 4; ++e) sv[e] = rbf(c4[e] * __builtin_amdgcn_rcpf(1.f + __builtin_amdgcn_exp2f(-1.4426950408889634f * c4[e])));
      *(LAS f4*)(sc + tid * 4) = sv;
    }
    j0.put(w, lane, L);
    make_tag(ep);
    WGBAR();
    j0.emit(w, lane, L, ws);
    if (j == 20 && w == 0) hst128(v4u{tag.x, tag.y, 0u, 0u}, ws, (lane >> 3) * 1024 + (lane & 7) * 16, O_CNT + x * 128);
    asm volatile("" ::"v"(pf) : "memory");
    auto first_pub = [&]() {
      WGBAR();
      if (w == 0) hst64(tag, ws, lane < 8 ? O_FW + lane * 1024 + hb * 8 : OOB, 0);
    };
    if (hb < 60) {
      const rsrc_t rm = mkrs(gwm, D_ * 6 * D_ * 2);
      v4u wm[20];
      YOUNG();
#pragma unroll
      for (int i = 0; i < 8; ++i) wm[i] = ld128(rm, (tid >> 3) * (6 * D_ * 2) + (tid & 7) * 16, hb * 128 + i * 32 * (6 * D_ * 2));
      PIN();
      WAITV(8);
      first_pub();
#pragma unroll
      for (int i = 8; i < 20; ++i) wm[i] = ld128(rm, (tid >> 3) * (6 * D_ * 2) + (tid & 7) * 16, hb * 128 + i * 32 * (6 * D_ * 2));
      j1.load(132 + hb, w, vn, gw1, gw2, gw4);
      load_q();
      float sl[20];
#pragma unroll
      for (int i = 0; i < 20; ++i) sl[i] = sc[(tid >> 3) + 32 * i];
      PIN();
      f2 a[4] = {f2{0.f, 0.f}, f2{0.f, 0.f}, f2{0.f, 0.f}, f2{0.f, 0.f}};
#pragma unroll
      for (int i = 0; i < 20; ++i)
#pragma unroll
        for (int e = 0; e < 4; ++e) a[e] += up2(wm[i][e]) * sl[i];
      LAS char* mred = L + 4 * RT_BYTES + 4096;
      *(LAS f4*)(mred + (tid >> 3) * 272 + (tid & 7) * 32) = f4{a[0].x, a[0].y, a[1].x, a[1].y};
      *(LAS f4*)(mred + (tid >> 3) * 272 + (tid & 7) * 32 + 16) = f4{a[2].x, a[2].y, a[3].x, a[3].y};
      WGBAR();
      {
        const int col = tid >> 2, part = tid & 3;
        float v[8];
#pragma unroll
        for (int r = 0; r < 8; ++r) v[r] = *(LAS float*)(mred + (part * 8 + r) * 272 + col * 4);
        float s = ((v[0] + v[1]) + (v[2] + v[3])) + ((v[4] + v[5]) + (v[6] + v[7]));
        s += dpp<0xB1>(s);
        s += dpp<0x4E>(s);
        s = rbf(s);
        if ((hb >= 10 && hb < 20) || (hb >= 40 && hb < 50)) s = rbf(1.f + s);
        if (part == 0) *((LAS unsigned short*)sc + 8 + col) = (unsigned short)(__builtin_bit_cast(unsigned, s) >> 16);
      }
      j1.put(w, lane, L);
      WGBAR();
      if (w == 0) {
        hst128(*(LAS v4u*)((LAS char*)sc + 16 + (lane & 7) * 16), ws, lane < 8 ? lane * 16 : OOB, O_MOD + hb * 128);
        DRAIN();
        hst64(tag, ws, lane < 8 ? O_FB + lane * 1024 + hb * 8 : OOB, 0);
      }
    } else {
      Job<true> ja, jb, jc;
      YOUNG();
      ja.load(hb - 60, w, vw, gw1, gw2, gw3);
      PIN();
      WAITV(7);
      first_pub();
      jb.load(hb - 24, w, vw, gw1, gw2, gw3);
      if (hb < 68) jc.load(hb + 12, w, vw, gw1, gw2, gw3);
      j1.load(36 + hb, w, vn, gw1, gw2, gw4);
      load_q();
      PIN();
      ja.put(w, lane, L + RT_BYTES);
      jb.put(w, lane, L + 2 * RT_BYTES);
      if (hb < 68) jc.put(w, lane, L + 3 * RT_BYTES);
      WGBAR();
      ja.emit(w, lane, L + RT_BYTES, ws);
      jb.emit(w, lane, L + 2 * RT_BYTES, ws);
      if (hb < 68) jc.emit(w, lane, L + 3 * RT_BYTES, ws);
      j1.put(w, lane, L);
      DRAIN();
      WGBAR();
      if (w == 0) hst64(tag, ws, lane < 8 ? O_FB + lane * 1024 + hb * 8 : OOB, 0);
    }
    pollw(ws, O_FW + x * 1024 + lane * 16, lane, 40, tag);
    j1.emit(w, lane, L, ws);
    zero_h1(0);
    zero_h1(1);
    zero_h1(2);
    if (hb >= 34 && hb < 60) hst128(v4u{0u, 0u, 0u, 0u}, ws, w == 0 ? lane * 16 : OOB, O_FR + (hb - 34) * 1024);
    YOUNG();
#pragma unroll
    for (int i = 0; i < 10; ++i) b1[i] = hld128(ws, lane * 16, sb1 + i * 1024);
    PIN();
    ln1();
    PIN();
    WAITV(10);
  }
  if (conv_wg && !wok) pollw(ws, O_FW + x * 1024 + lane * 16, lane, 40, tag);
  WGBAR();
  if (w == 0 && lane < 8)
    __hip_atomic_fetch_add((GAS unsigned*)((GAS char*)gws + O_CNT + (lane * 8 + x) * 128), 1u, __ATOMIC_RELAXED, __HIP_MEMORY_SCOPE_AGENT);
  if (!wok) {
    if (conv_wg) {
#pragma unroll
      for (int i = 0; i < 10; ++i) b1[i] = hld128(ws, lane * 16, sb1 + i * 1024);
    }
#pragma unroll
    for (int i = 10; i < 25; ++i) b1[i] = hld128(ws, lane * 16, sb1 + i * 1024);
#pragma unroll
    for (int i = 0; i < NB2; ++i) b2[i] = hld128(ws, lane * 16, sb2 + i * 1024);
  }
#pragma unroll
  for (int i = NB2; i < 25; ++i) b2[i] = hld128(ws, lane * 16, sb2 + i * 1024);
  v4u b3[35], b4[70];
  const unsigned sb3 = O_W3 + (cg * 140 + w * 35) * 1024, sb4 = O_W4 + (tn * 280 + w * 70) * 1024;
#define LB3(lo, hi) _Pragma("unroll") for (int i_ = lo; i_ < hi; ++i_) b3[i_] = hld128(ws, lane * 16 + (i_ & 3) * 1024, sb3 + (i_ >> 2) * 4096)
  auto ks4 = [](int s) { return s < 8 ? 7 * (s >> 2) + (s & 1) + 4 * ((s >> 1) & 1) : 7 * ((s - 8) / 3) + ((s - 8) % 3 == 2 ? 6 : 2 + (s - 8) % 3); };
  auto lb4 = [&](int i) {
    const int f = ks4(i / 5) * 5 + i % 5;
    b4[i] = hld128(ws, lane * 16 + (f & 3) * 1024, sb4 + (f >> 2) * 4096);
  };
#define LB4(lo, hi) _Pragma("unroll") for (int i_ = lo; i_ < hi; ++i_) lb4(i_)
  if (!bok) {
    pollw(ws, O_FB + x * 1024 + lane * 16, lane, 48, tag);
    load_mod(true);
  }
  constexpr int NB3C = 10;
  f2 gh[10];
  float cb1[10], sbv[10];
  auto md = [&](int v, int i) { const unsigned u = i < 8 ? mm[v][(i >> 1) & 3] : mo[v]; return (i & 1) ? hi2f(u) : lo2f(u); };
  auto use_mod = [&]() {
    LB3(0, NB3C);
#pragma unroll
    for (int i = 0; i < 10; ++i) {
      gh[i] = (n1[i] * md(1, i) + md(0, i)) * md(2, i);
      cb1[i] = md(4, i);
      sbv[i] = md(3, i);
      asm volatile("" : "+v"(gh[i]), "+v"(cb1[i]), "+v"(sbv[i]));
    }
  };
  use_mod();
  PIN();
  if (w == 0) {
    poll64(ws, lane < 8 ? O_CNT + (x * 8 + lane) * 128 : OOB, v2u{tag.x + 32, tag.y});
    if (b == 0) hst64(tag, ws, lane == 0 ? O_EPOCH : OOB, 0);
  }
  WGBAR();

  v4u hx[6];
  f4 csp[7];
  auto img3 = [&](int lo, int hi, int hc) {
    const v4u ones = {0x3f803f80u, 0x3f803f80u, 0x3f803f80u, 0x3f803f80u};
    unsigned la = w * 25600 + lane * 16, lb = (75 + w * 10) * 1024 + lane * 16;
    asm volatile("" : "+v"(la), "+v"(lb));
#pragma unroll
    for (int i = lo; i < 35; ++i) {
      if (i < hi) *(LAS v4u*)(L + (i < 25 ? la : lb) + i * 1024) = b3[i];
      if (i < hc) csp[i % 7] = mfma(b3[i], ones, i < 7 ? f4{0.f, 0.f, 0.f, 0.f} : csp[i % 7]);
      PIN();
    }
  };
  {
    v4u zc[3];
#pragma unroll
    for (int i = 0; i < 3; ++i) zc[i] = hld128(ws, i < 2 ? tid * 32 + i * 8192 : z3off, O_ZT + (4 * tm + (tn >> 1)) * ZP + (tn & 1) * 16);
    PIN();
#pragma unroll
    for (int i = 0; i < 3; ++i)
#pragma unroll
      for (int e = 0; e < 4; ++e) *(LAS unsigned*)(L + RED_OFF + e * 3072 + (tid + 256 * i) * 4) = zc[i][e];
    WGBAR();
    const v4u z0 = *(LAS v4u*)(L + RED_OFF + w * 3072 + rcm * 4), z1 = *(LAS v4u*)(L + RED_OFF + w * 3072 + rcm * 4 + 16);
    const v2u z2 = *(LAS v2u*)(L + RED_OFF + w * 3072 + rco * 4);
    LB3(10, 19);
    f2 xv[10], hv[10], av[10], s, ss, r, mr;
#pragma unroll
    for (int i = 0; i < 10; ++i) {
      xv[i] = gh[i] * up2(i < 4 ? z0[i & 3] : (i < 8 ? z1[i & 3] : z2[i & 1])) + qv[i];
      s = i ? s + xv[i] : xv[i];
      ss = i ? ss + xv[i] * xv[i] : xv[i] * xv[i];
    }
    wave_sum4(s, ss);
    ln_scal(s, ss, r, mr);
#pragma unroll
    for (int i = 0; i < 10; ++i) {
      hv[i] = (xv[i] * r + mr) * cb1[i] + sbv[i];
      s = i ? s + hv[i] : hv[i];
      ss = i ? ss + hv[i] * hv[i] : hv[i] * hv[i];
    }
    wave_sum4(s, ss);
    ln_scal(s, ss, r, mr);
#pragma unroll
    for (int i = 0; i < 10; ++i) av[i] = hv[i] * r + mr;
    auto put = [&](const f2(&v)[10], int img) {
      *(LAS v4u*)(L + im0 + img * 10240) = v4u{pk2(v[0].x, v[1].x), pk2(v[2].x, v[3].x), pk2(v[4].x, v[5].x), pk2(v[6].x, v[7].x)};
      *(LAS v4u*)(L + (im0 ^ 16) + img * 10240) = v4u{pk2(v[0].y, v[1].y), pk2(v[2].y, v[3].y), pk2(v[4].y, v[5].y), pk2(v[6].y, v[7].y)};
      *(LAS unsigned*)(L + io0 + img * 10240) = pk2(v[8].x, v[9].x);
      *(LAS unsigned*)(L + (io0 ^ 16) + img * 10240) = pk2(v[8].y, v[9].y);
    };
    put(av, 0);
    WGBAR();
    {
      v4u oc[3];
#pragma unroll
      for (int i = 0; i < 3; ++i) oc[i] = *(LAS v4u*)(L + RED_OFF + 16384 + i * 4096 + tid * 16);
#pragma unroll
      for (int i = 0; i < 3; ++i) hst128(oc[i], ws, i < 2 ? roff : roff3, i * 8192);
    }
    PIN();
    put(hv, 1);
    put(xv, 2);
    DRAIN();
    WGBAR();
    if (w == 0) hst64(tag, ws, frpub, 0);
    LB3(19, 28);
    PIN();
#pragma unroll
    for (int i = 0; i < 6; ++i) hx[i] = *(LAS v4u*)(L + RED_OFF + 26624 + (i / 3) * 10240 + (i % 3) * 4096 + tid * 16);
    img3(0, 16, 16);
    if (w == 0) poll64(ws, frwait, tag);
    WGBAR();
  }

  {
    f4 acc[5];
    const unsigned vo[4] = {(unsigned)lane * 16, (unsigned)lane * 16, (unsigned)lane * 16, (unsigned)lane * 16};
    gemm_ks(ws, O_A1 + (4 * tm * 20 + 5 * w) * 1024, vo, b1, L, w, lane, acc,
            [&](int n) { if (n < 7) { LB3(28 + n, 29 + n); } else lb4(16 + n); },
            [&](int n, int t) {
              if (t == 3 && n >= 8 && n < 14) hst128(hx[n - 8], ws, ((n - 8) % 3) < 2 ? roff : roff3, (1 + (n - 8) / 3) * SD2 + ((n - 8) % 3) * 8192);
            });
    f2 v[10];
    unsigned o[10];
#pragma unroll
    for (int t = 0; t < 5; ++t) {
      v[2 * t] = f2{acc[t][0], acc[t][1]};
      v[2 * t + 1] = f2{acc[t][2], acc[t][3]};
    }
    gelu_pack<10>(v, GC0, GC1, 1.f, o, [](int) {});
    const auto s0 = __builtin_amdgcn_permlane16_swap(o[8], o[8], false, false);
    const auto s1 = __builtin_amdgcn_permlane16_swap(o[9], o[9], false, false);
    hst128(v4u{o[0], o[1], o[2], o[3]}, ws, h1m, 0);
    hst128(v4u{o[4], o[5], o[6], o[7]}, ws, h1m + 1024, 0);
    hst128(v4u{s0[0], s1[0], s0[1], s1[1]}, ws, h1s, 0);
    DRAIN();
    WGBAR();
    if (w == 0) hst64(tag, ws, f1pub, 0);
    LB4(0, 9);
    PIN();
    img3(16, 24, 35);
    if (l15 == 0) {
#pragma unroll
      for (int t = 0; t < 7; ++t) *(LAS f4*)(L + (w * 25 + 24) * 1024 + (t * 4 + g) * 16) = csp[t];
    }
    if (w == 0) poll64(ws, f1wait, tag);
    WGBAR();
  }
  v4u xr[2];
  v2u xro;
  unsigned x1p[10];
  {
    v4u hr[2];
    v2u hro;
    f4 acc[5];
    f2 hf[10];
    const unsigned vo[4] = {(unsigned)lane * 16, (unsigned)lane * 16, (unsigned)lane * 16, (unsigned)lane * 16};
    gemm_ks(ws, O_H1 + (4 * tm * 20 + 5 * w) * 1024, vo, b2, L, w, lane, acc, [&](int n) {
      lb4(n < 5 ? 9 + n : 28 + n);
    }, [&](int n, int t) {
      if (t == 3 && n == 2) hr[0] = hld128(ws, lane * 16, O_HP + own_m);
      if (t == 3 && n == 4) hr[1] = hld128(ws, lane * 16 + 1024, O_HP + own_m);
      if (t == 3 && n == 6) hro = hld64(ws, own_r, O_HP + own_s);
      if (n == 18) {
        unsigned h0 = t < 4 ? hr[t >> 1][2 * (t & 1)] : hro.x;
        KEEP(h0);
        hf[2 * t] = up2(h0);
        KEEP(hf[2 * t]);
      }
    });
#pragma unroll
    for (int t = 0; t < 5; ++t) {
      const unsigned h1 = t < 4 ? hr[t >> 1][2 * (t & 1) + 1] : hro.y;
      const f2 p = f2{acc[t][0], acc[t][1]} + hf[2 * t], q = f2{acc[t][2], acc[t][3]} + up2(h1);
      x1p[2 * t] = pk2(p.x, p.y);
      x1p[2 * t + 1] = pk2(q.x, q.y);
    }
    const auto s0 = __builtin_amdgcn_permlane16_swap(x1p[8], x1p[8], false, false);
    const auto s1 = __builtin_amdgcn_permlane16_swap(x1p[9], x1p[9], false, false);
    hst128(v4u{x1p[0], x1p[1], x1p[2], x1p[3]}, ws, x1m, 0);
    hst128(v4u{x1p[4], x1p[5], x1p[6], x1p[7]}, ws, x1m + 1024, 0);
    hst128(v4u{s0[0], s1[0], s0[1], s1[1]}, ws, x1s, 0);
    float t1 = 0.f, t2 = 0.f;
#pragma unroll
    for (int i = 0; i < 10; ++i) {
      t1 = __builtin_amdgcn_fdot2_f32_bf16(__builtin_bit_cast(bf2, x1p[i]), __builtin_bit_cast(bf2, 0x3f803f80u), t1, false);
      t2 = __builtin_amdgcn_fdot2_f32_bf16(__builtin_bit_cast(bf2, x1p[i]), __builtin_bit_cast(bf2, x1p[i]), t2, false);
    }
    t1 = swap32_add(t1, t2);
    t1 = swap16_add(t1, t1);
    hst32(__builtin_bit_cast(unsigned, t1), ws, stoff, 0);
    f4 cst;
    {
      f4 cp[4];
#pragma unroll
      for (int v = 0; v < 4; ++v) cp[v] = *(LAS f4*)(L + (v * 25 + 24) * 1024 + lane * 16);
      cst = (cp[0] + cp[1]) + (cp[2] + cp[3]);
      KEEP(cst);
    }
    PIN();
    DRAIN();
    WGBAR();
    if (w == 0) hst64(tag, ws, f2pub, 0);
    LB4(14, 23);
    PIN();
    img3(24, 35, 0);
    if (w == 1) *(LAS f4*)(L + CSUM_OFF + lane * 16) = cst;
    if (w == 0) poll64(ws, f2wait, tag);
    WGBAR();
  }
  constexpr int WIN4 = 8, RING4 = WIN4 + 1, PUBK = 10, PUBN = 9, SMPN = 20, CHKN = 26;
  v4u a4[RING4];
  auto lda4 = [&](int n) {
    const int i = n < 32 ? n >> 3 : (n - 32) / 6, k = ks4(n < 32 ? n & 7 : 8 + (n - 32) % 6);
    a4[n % RING4] = hld128(ws, lane * 16 + (k & 3) * 1024, O_H + ((4 * tm + ((w + 1 + i) & 3)) * 56 + 14 * w + (k & ~3)) * 1024);
  };
  {
    constexpr int W3 = 6;
    v4u a[20][2];
    v2u sv[2][2];
    const unsigned sa = O_X1 + (8 * rg + 2 * w) * 20 * 1024;
    auto lda = [&](int k, int i) { a[k][i] = hld128(ws, lane * 16 + (k & 3) * 1024, sa + i * 20480 + (k >> 2) * 4096); };
#pragma unroll
    for (int k = 0; k < W3; ++k)
#pragma unroll
      for (int i = 0; i < 2; ++i) {
        lda(k, i);
        if (k == 1) {
          sv[i][0] = hld64(ws, l15 * 8 + g * 256, O_STAT + (8 * rg + 2 * w + i) * 1024);
          sv[i][1] = hld64(ws, l15 * 8 + g * 256 + 128, O_STAT + (8 * rg + 2 * w + i) * 1024);
        }
        PIN();
      }
    f4 acc[2][7];
    f2 gc0, gc1, den, mean;
    unsigned hs, hs1, lcs;
    auto bslot = [](int k, int t) { const int v = k / 5, i = (k % 5) * 7 + t; return (i < 25 ? v * 25 + i : 75 + v * 10 + i) * 1024; };
    const LAS char* Lb = L + lane * 16;
    const LAS char* Lb2 = L + lane * 16 + 65536;
    const LAS char* Lb3 = L + lane * 16 + 131072;
    auto rdb = [&](int k, int t) { const int o = bslot(k, t); return *(const LAS v4u*)(o < 65536 ? Lb + o : (o < 131072 ? Lb2 + (o - 65536) : Lb3 + (o - 131072))); };
    v4u bf[3][4];
#pragma unroll
    for (int t = 0; t < 8; ++t) {
      bf[t >> 2][t & 3] = rdb(t >> 2, t & 3);
      PIN();
    }
    sfor<0, 20>([&](auto ic) {
      constexpr int k = decltype(ic)::v;
      __builtin_amdgcn_s_waitcnt(k < 19 ? 0xC47F : 0xC37F);
#pragma unroll
      for (int t = 0; t < 4; ++t) {
        if (k == 0) mfma_va0(acc[0][t], bf[0][t], a[0][0]);
        else mfma_va(acc[0][t], bf[k % 3][t], a[k][0]);
        if (k < 18) bf[(k + 2) % 3][t] = rdb(k + 2, t);
        else if (t < 3) bf[(k + 2) % 3][t] = rdb(k - 18, 4 + t);
        PIN();
      }
#pragma unroll
      for (int t = 0; t < 4; ++t) {
        if (k == 0) mfma_va0(acc[1][t], bf[0][t], a[0][1]);
        else mfma_va(acc[1][t], bf[k % 3][t], a[k][1]);
        if (t < 2) {
          if constexpr (k + W3 < 20) lda(k + W3, t);
        }
        if (t >= (k + W3 < 20 ? 2 : 0) && t < (k + W3 < 20 ? 4 : 2)) {
          if constexpr (k >= 14 && k < 18) lb4(40 + 2 * (k - 14) + (t & 1));
        }
        if (k == 4 && t == 3) {
          asm volatile("" : "+v"(sv[0][0]), "+v"(sv[0][1]), "+v"(sv[1][0]), "+v"(sv[1][1]));
          const f2 p0 = __builtin_bit_cast(f2, sv[0][0]) + __builtin_bit_cast(f2, sv[0][1]);
          const f2 p1 = __builtin_bit_cast(f2, sv[1][0]) + __builtin_bit_cast(f2, sv[1][1]);
          f2 r, mr;
          ln_scal(f2{xrow_sum(p0.x), xrow_sum(p1.x)}, f2{xrow_sum(p0.y), xrow_sum(p1.y)}, r, mr, &mean, &den);
          gc0 = r * GC0;
          gc1 = r * r * r * GC1;
          hs = O_H + ((8 * rg + 2 * w) * 56 + 7 * (cg >> 1) + 4 * (cg & 1)) * 1024 + lane * 16;
          hs1 = O_H + ((8 * rg + 2 * w) * 56 + 7 * (cg >> 1) + 3) * 1024 + (cg & 1) * 512 + (g & 1) * 57344 + (l15 + 16 * (g >> 1)) * 16;
          lcs = CSUM_OFF + g * 16;
          asm volatile("" : "+v"(gc0), "+v"(gc1), "+v"(mean), "+v"(den), "+v"(hs), "+v"(hs1), "+v"(lcs));
        }
        if (k == 19) csp[t] = *(LAS f4*)(L + lcs + t * 64);
        PIN();
      }
    });
    MFENCE();
    PIN();
#pragma unroll
    for (int i = 0; i < 2; ++i) {
      f2 v[8];
      unsigned o[8];
#pragma unroll
      for (int e = 0; e < 8; ++e) {
        const int t = e >> 1, h = 2 * (e & 1);
        v[e] = f2{acc[i][t][h], acc[i][t][h + 1]} - f2{csp[t][h], csp[t][h + 1]} * mean[i];
      }
      gelu_pack<8>(v, gc0[i], gc1[i], den[i], o, [&](int m) {
        if (i == 0) lb4(28 + m);
        else if (m == 0) lb4(32);
      });
      hst128(v4u{o[0], o[1], o[2], o[3]}, ws, hs, i * 57344);
      hst128(v4u{o[4], o[5], o[6], o[7]}, ws, hs, i * 57344 + 1024);
    }
    YOUNG();
    sfor<0, 20>([&](auto ic) {
      constexpr int k = decltype(ic)::v;
      __builtin_amdgcn_s_waitcnt(k < 19 ? 0xC37F : 0xC07F);
      if (k == PUBK) {
        WAITV(10);
        hst64(tag, ws, f3pub, 0);
      }
#pragma unroll
      for (int e = 0; e < 6; ++e) {
        const int i = e / 3, t = e % 3;
        if (k == 0) mfma_va0(acc[i][4 + t], bf[2][t], a[0][i]);
        else mfma_va(acc[i][4 + t], bf[(k + 2) % 3][t], a[k][i]);
        if (i == 0 && k < 18) bf[(k + 1) % 3][t] = rdb(k + 2, 4 + t);
        if (k == 19 && i == 0) csp[4 + t] = *(LAS f4*)(L + lcs + (4 + t) * 64);
        if (k >= 1 && k < 5 && e == 5) lb4(47 + k);
        if (k >= 1 && k < 19 && e == 3) lb4(51 + k);
        PIN();
      }
    });
    v2u fl;
    MFENCE();
    PIN();
    unsigned os[2][2];
#pragma unroll
    for (int i = 0; i < 2; ++i) {
      f2 v[6];
      unsigned o[6];
#pragma unroll
      for (int e = 0; e < 6; ++e) {
        const int t = 4 + (e >> 1), h = 2 * (e & 1);
        v[e] = f2{acc[i][t][h], acc[i][t][h + 1]} - f2{csp[t][h], csp[t][h + 1]} * mean[i];
      }
      gelu_pack<6>(v, gc0[i], gc1[i], den[i], o, [&](int m) {
        if (i == 0 && m == 0) fl = hld64(ws, f3wait, 0);
      });
      hst128(v4u{o[0], o[1], o[2], o[3]}, ws, hs, i * 57344 + 2048);
      os[i][0] = o[4];
      os[i][1] = o[5];
      if (i == 0) {
        PIN();
        KEEP(fl);
        if (!__all(f3wait >= OOB || (fl.x == tag.x && fl.y == tag.y))) poll64(ws, f3wait, tag);
        __builtin_amdgcn_fence(__ATOMIC_ACQUIRE, "wavefront");
#pragma unroll
        for (int n = 0; n < WIN4; ++n) {
          lda4(n);
          PIN();
        }
      }
    }
    {
      const auto s0 = __builtin_amdgcn_permlane16_swap(os[0][0], os[1][0], false, false);
      const auto s1 = __builtin_amdgcn_permlane16_swap(os[0][1], os[1][1], false, false);
      hst128(v4u{s0[0], s1[0], s0[1], s1[1]}, ws, hs1, 0);
    }
    YOUNG();
  }
  {
    v4u gb[2];
    v2u gbo, fl;
    f4 ac[4][5], p[15];
    float gf[20], cf[20];
    unsigned lq = lane;
    KEEP(lq);
    const unsigned lr = RED_OFF + lq * 16, gq4 = lq >> 4;
    unsigned dm = ((64 * tm + 16 * w + (lq & 15)) * D_ + 80 * tn) * 2, ds = dm + 8 * gq4 + ((tn & 1) ? 0 : 128);
    dm += 16 * gq4 + 32 * (tn & 1);
    unsigned xro_off = ((lq & 15) + 16 * (gq4 >> 1)) * 16 + 8 * (gq4 & 1);
    sfor<0, 56>([&](auto ic) {
      constexpr int n = decltype(ic)::v, i = n < 32 ? n >> 3 : (n - 32) / 6, sl = n < 32 ? n & 7 : 8 + (n - 32) % 6;
      if (n == 54) {
        WGBAR();
      }
      if (n == PUBN) {
        WAITV(7);
        hst64(tag, ws, f3pub + (O_F4 - O_F3), 0);
      }
      if (n == 23) asm volatile("" : "+v"(dm), "+v"(ds), "+v"(xro_off));
      if (n == 37) asm volatile("s_barrier" ::: "memory");
      if (n == CHKN) {
        KEEP(fl);
        if (!__all(f3wait >= OOB || (fl.x == tag.x && fl.y == tag.y))) poll64(ws, f3wait + (O_F4 - O_F3), tag);
        __builtin_amdgcn_fence(__ATOMIC_ACQUIRE, "wavefront");
#pragma unroll
        for (int m = 32; m < CHKN + WIN4; ++m) lda4(m);
        PIN();
      }
#pragma unroll
      for (int t = 0; t < 5; ++t) {
        const int f = sl * 5 + t;
        if (sl == 0) mfma_aa0(ac[i][t], b4[f], a4[n % RING4]);
        else if (f >= 22 && f < 48) mfma_va(ac[i][t], b4[f], a4[n % RING4]);
        else mfma_aa(ac[i][t], b4[f], a4[n % RING4]);
        if (t == 1) {
          if constexpr (n + WIN4 < 56) {
            if (n < 32 - WIN4 || n >= CHKN) lda4(n + WIN4);
          }
        }
        if (t == 3) {
          if (n == 30 || n == 31) gb[n & 1] = hld128(ws, gq4 * 16 + (n & 1) * 64, O_MOD + (5 * D_ + 80 * tn + 16 * (tn & 1)) * 2);
          if (n == 32 || n == 33) xr[n & 1] = hld128(ws, lq * 16 + (n & 1) * 1024, O_X + own_m);
          if (n == 34) gbo = hld64(ws, gq4 * 8, O_MOD + (5 * D_ + 80 * tn + ((tn & 1) ? 0 : 64)) * 2);
          if (n == 35) xro = hld64(ws, xro_off, O_X + own_s);
        }
        if constexpr (n >= 44 && n < 54) {
          constexpr int q = n - 44;
          const unsigned gw = q < 8 ? gb[(q >> 2) & 1][q & 3] : gbo[q & 1], xw = q < 8 ? xr[(q >> 2) & 1][q & 3] : xro[q & 1];
          float tmp;
          if (t == 1) asm volatile("v_lshlrev_b32 %0, 16, %2\n\tv_and_b32 %1, 0xffff0000, %2" : "=&v"(gf[2 * q]), "=v"(gf[2 * q + 1]) : "v"(gw));
          if (t == 2) asm volatile("v_lshlrev_b32 %0, 16, %2\n\tv_and_b32 %1, 0xffff0000, %2" : "=&v"(cf[2 * q]), "=v"(cf[2 * q + 1]) : "v"(xw));
          if (t == 3) asm volatile("v_lshlrev_b32 %1, 16, %2\n\tv_fmac_f32 %0, %1, %3" : "+v"(cf[2 * q]), "=&v"(tmp) : "v"(x1p[q]), "v"(gf[2 * q]));
          if (t == 4) asm volatile("v_and_b32 %1, 0xffff0000, %2\n\tv_fmac_f32 %0, %1, %3" : "+v"(cf[2 * q + 1]), "=&v"(tmp) : "v"(x1p[q]), "v"(gf[2 * q + 1]));
        }
        if (n == SMPN && t == 0) fl = hld64(ws, f3wait + (O_F4 - O_F3), 0);
        if (n >= 38 && sl < 11 && (t == 0 || t == 2) && (sl - 8) * 2 + (t == 2) > 0) {
          const int u = (sl - 8) * 2 + (t == 2) - 1;
          *(LAS f4*)(L + (lr + ((w + i) & 3) * 15360) + ((i - 1) * 5 + u) * 1024) = ac[i > 0 ? i - 1 : 0][u];
        }
        if (n == 54) {
          p[t] = *(LAS f4*)(L + (lr + w * 15360) + t * 1024);
          p[5 + t] = *(LAS f4*)(L + (lr + w * 15360) + (5 + t) * 1024);
        }
        if (n == 55) p[10 + t] = *(LAS f4*)(L + (lr + w * 15360) + (10 + t) * 1024);
        PIN();
      }
    });
    MFENCE();
    PIN();
    unsigned o[10];
#pragma unroll
    for (int t = 0; t < 5; ++t) {
      const f4 y = (ac[3][t] + p[t]) + (p[5 + t] + p[10 + t]);
      const f2 u = f2{y[0], y[1]} * f2{gf[4 * t], gf[4 * t + 1]} + f2{cf[4 * t], cf[4 * t + 1]};
      const f2 v = f2{y[2], y[3]} * f2{gf[4 * t + 2], gf[4 * t + 3]} + f2{cf[4 * t + 2], cf[4 * t + 3]};
      o[2 * t] = pk2(u.x, u.y);
      o[2 * t + 1] = pk2(v.x, v.y);
    }
    const rsrc_t ro = mkrs(gout, SD2);
    __builtin_amdgcn_raw_buffer_store_b128(v4u{o[0], o[1], o[2], o[3]}, ro, dm, 0, 0);
    __builtin_amdgcn_raw_buffer_store_b128(v4u{o[4], o[5], o[6], o[7]}, ro, dm + 64, 0, 0);
    __builtin_amdgcn_raw_buffer_store_b64(v2u{o[8], o[9]}, ro, ds, 0, 0);
  }
  asm volatile(".Lfused_end:" ::: "memory");
}

extern "C" void kernel_launch(void* const* d_in, const int* in_sizes, int n_in, void* d_out, int out_size,
                              void* d_ws, size_t ws_size, hipStream_t stream) {
  (void)hipFuncSetAttribute((const void*)fused, hipFuncAttributeMaxDynamicSharedMemorySize, LDS_BYTES);
  fused<<<256, 256, LDS_BYTES, stream>>>(d_in[0], d_in[1], d_in[2], d_in[3], d_in[4], d_in[5], d_in[7], d_in[8], d_in[9],
                                         d_in[10], d_in[11], d_in[12], d_out, d_ws);
}
```

```cpp
#include <hip/hip_runtime.h>
#include <stdint.h>

typedef unsigned v4u __attribute__((ext_vector_type(4)));
typedef unsigned v2u __attribute__((ext_vector_type(2)));
typedef float f4 __attribute__((ext_vector_type(4)));
typedef float f2 __attribute__((ext_vector_type(2)));
typedef short s4v __attribute__((ext_vector_type(4)));
typedef __bf16 bf2 __attribute__((ext_vector_type(2)));
typedef __bf16 bfx8 __attribute__((ext_vector_type(8)));
typedef __amdgpu_buffer_rsrc_t rsrc_t;
#define LAS __attribute__((address_space(3)))
#define GAS __attribute__((address_space(1)))
#define DI __device__ __forceinline__

static constexpr int S_ = 2048, D_ = 640, F_ = 1728;
static constexpr unsigned OOB = 0x80000000u;

static constexpr unsigned O_EPOCH = 0;
static constexpr unsigned O_FP = 1024;
static constexpr unsigned O_FR = O_FP + 16 * 128;
static constexpr unsigned O_F1 = O_FR + 32 * 128;
static constexpr unsigned O_F2 = O_F1 + 32 * 128;
static constexpr unsigned O_F3 = O_F2 + 16 * 128;
static constexpr unsigned O_F4 = O_F3 + 16 * 512;
static constexpr unsigned O_CNT = O_F4 + 16 * 512;
static constexpr unsigned O_FW = O_CNT + 64 * 128;
static constexpr unsigned O_FB = O_FW + 8192;
static constexpr unsigned O_MOD = 65536;
static constexpr unsigned O_STAT = O_MOD + 16384;
static constexpr unsigned O_KVT = O_STAT + 131072;
static constexpr unsigned SD2 = S_ * D_ * 2;
static constexpr unsigned KVP = 4096 + 128, ZP = 20480 + 128;
static constexpr unsigned O_ZT = O_KVT + D_ * KVP;
static constexpr unsigned O_A1 = O_ZT + 128 * ZP;
static constexpr unsigned O_HP = O_A1 + SD2;
static constexpr unsigned O_X = O_HP + SD2;
static constexpr unsigned O_H1 = O_X + SD2;
static constexpr unsigned O_X1 = O_H1 + SD2;
static constexpr unsigned O_H = O_X1 + SD2;
static constexpr unsigned O_W1 = O_H + 128 * 56 * 1024;
static constexpr unsigned O_W2 = O_W1 + 800 * 1024;
static constexpr unsigned O_W3 = O_W2 + 800 * 1024;
static constexpr unsigned O_W4 = O_W3 + 2240 * 1024;
static constexpr unsigned O_END = O_W4 + 2240 * 1024;

static constexpr int CS = 4160;
static constexpr int UOFF = 8 * CS;
static constexpr int WR = 8 * CS + 4608;
static constexpr int LDS_BYTES = 160 * 1024;
static constexpr int RED_OFF = 100 * 1024;
static constexpr int CSUM_OFF = 140 * 1024;
static constexpr unsigned SPIN_CAP = 200000;

template <int V> struct IC { static constexpr int v = V; };
template <int I, int N, class F> DI void sfor(F&& f) {
  if constexpr (I < N) {
    f(IC<I>{});
    sfor<I + 1, N>(f);
  }
}
DI float lo2f(unsigned w) { return __builtin_bit_cast(float, w << 16); }
DI float hi2f(unsigned w) { return __builtin_bit_cast(float, w & 0xffff0000u); }
DI f2 up2(unsigned w) { return f2{lo2f(w), hi2f(w)}; }
DI unsigned pk2(float a, float b) { return __builtin_bit_cast(unsigned, __builtin_convertvector(f2{a, b}, bf2)); }
DI f4 mfma(v4u a, v4u b, f4 c) {
  return __builtin_amdgcn_mfma_f32_16x16x32_bf16(__builtin_bit_cast(bfx8, a), __builtin_bit_cast(bfx8, b), c, 0, 0, 0);
}
DI void mfma_v(f4& c, v4u a, v4u b) { asm volatile("v_mfma_f32_16x16x32_bf16 %0, %1, %2, %0" : "+v"(c) : "v"(a), "v"(b)); }
DI void mfma_v0(f4& c, v4u a, v4u b) { asm volatile("v_mfma_f32_16x16x32_bf16 %0, %1, %2, 0" : "=&v"(c) : "v"(a), "v"(b)); }
DI void mfma_va(f4& c, v4u a, v4u b) { asm volatile("v_mfma_f32_16x16x32_bf16 %0, %1, %2, %0" : "+v"(c) : "v"(a), "a"(b)); }
DI void mfma_va0(f4& c, v4u a, v4u b) { asm volatile("v_mfma_f32_16x16x32_bf16 %0, %1, %2, 0" : "=&v"(c) : "v"(a), "a"(b)); }
DI void mfma_av(f4& c, v4u a, v4u b) { asm volatile("v_mfma_f32_16x16x32_bf16 %0, %1, %2, %0" : "+v"(c) : "a"(a), "v"(b)); }
DI void mfma_av0(f4& c, v4u a, v4u b) { asm volatile("v_mfma_f32_16x16x32_bf16 %0, %1, %2, 0" : "=&v"(c) : "a"(a), "v"(b)); }
DI void mfma_aa(f4& c, v4u a, v4u b) { asm volatile("v_mfma_f32_16x16x32_bf16 %0, %1, %2, %0" : "+v"(c) : "a"(a), "a"(b)); }
DI void mfma_aa0(f4& c, v4u a, v4u b) { asm volatile("v_mfma_f32_16x16x32_bf16 %0, %1, %2, 0" : "=&v"(c) : "a"(a), "a"(b)); }
#define KEEP(x) asm volatile("" : "+v"(x))
#define MFENCE() asm volatile("s_nop 7\n\ts_nop 1" ::: "memory")
DI float add1(float a, float b) { float r; asm("v_add_f32 %0, %1, %2" : "=v"(r) : "v"(a), "v"(b)); return r; }
DI rsrc_t mkrs(const void* p, unsigned bytes) { return __builtin_amdgcn_make_buffer_rsrc((void*)p, 0, bytes, 0x00020000); }
DI v4u ld128(rsrc_t r, unsigned v, unsigned s) { return __builtin_amdgcn_raw_buffer_load_b128(r, v, s, 2); }
DI unsigned ld32(rsrc_t r, unsigned v, unsigned s) { return __builtin_amdgcn_raw_buffer_load_b32(r, v, s, 2); }
DI v4u hld128(rsrc_t r, unsigned v, unsigned s) { return __builtin_amdgcn_raw_buffer_load_b128(r, v, s, 16); }
DI v2u hld64(rsrc_t r, unsigned v, unsigned s) { return __builtin_amdgcn_raw_buffer_load_b64(r, v, s, 16); }
DI void hst128(v4u x, rsrc_t r, unsigned v, unsigned s) { __builtin_amdgcn_raw_buffer_store_b128(x, r, v + s, 0, 16); }
DI void hst64(v2u x, rsrc_t r, unsigned v, unsigned s) { __builtin_amdgcn_raw_buffer_store_b64(x, r, v, s, 16); }
DI void hst32(unsigned x, rsrc_t r, unsigned v, unsigned s) { __builtin_amdgcn_raw_buffer_store_b32(x, r, v, s, 16); }
#define DRAIN() do { asm volatile("s_waitcnt vmcnt(0)" ::: "memory"); __builtin_amdgcn_s_waitcnt(0x0F70); } while (0)
#define WGBAR() asm volatile("s_waitcnt lgkmcnt(0)\n\ts_barrier" ::: "memory")
#define PIN() __builtin_amdgcn_sched_barrier(0)

template <int CTRL> DI float dpp(float x) {
  return __builtin_bit_cast(float, __builtin_amdgcn_mov_dpp(__builtin_bit_cast(int, x), CTRL, 0xf, 0xf, true));
}
DI float xrow_sum(float x) {
  auto s = __builtin_amdgcn_permlane16_swap(__builtin_bit_cast(unsigned, x), __builtin_bit_cast(unsigned, x), false, false);
  x = __builtin_bit_cast(float, (unsigned)s[0]) + __builtin_bit_cast(float, (unsigned)s[1]);
  auto t = __builtin_amdgcn_permlane32_swap(__builtin_bit_cast(unsigned, x), __builtin_bit_cast(unsigned, x), false, false);
  return __builtin_bit_cast(float, (unsigned)t[0]) + __builtin_bit_cast(float, (unsigned)t[1]);
}
DI float swap32_add(float a, float b) {
  auto t = __builtin_amdgcn_permlane32_swap(__builtin_bit_cast(unsigned, a), __builtin_bit_cast(unsigned, b), false, false);
  return __builtin_bit_cast(float, (unsigned)t[0]) + __builtin_bit_cast(float, (unsigned)t[1]);
}
DI float swap16_add(float a, float b) {
  auto t = __builtin_amdgcn_permlane16_swap(__builtin_bit_cast(unsigned, a), __builtin_bit_cast(unsigned, b), false, false);
  return __builtin_bit_cast(float, (unsigned)t[0]) + __builtin_bit_cast(float, (unsigned)t[1]);
}
DI float rdl(float v, int l) { return __builtin_bit_cast(float, __builtin_amdgcn_readlane(__builtin_bit_cast(int, v), l)); }
DI void wave_sum4(f2& s, f2& ss) {
  float v = swap16_add(swap32_add(s.x, s.y), swap32_add(ss.x, ss.y));
  v += dpp<0xB1>(v);
  v += dpp<0x4E>(v);
  v += dpp<0x141>(v);
  v += dpp<0x128>(v);
  s = f2{rdl(v, 0), rdl(v, 32)};
  ss = f2{rdl(v, 16), rdl(v, 48)};
}
DI bool hit64(v2u v, v2u want, unsigned long long mask) {
  return __builtin_amdgcn_ballot_w64(__builtin_bit_cast(unsigned long long, v) == __builtin_bit_cast(unsigned long long, want)) == mask;
}
DI void poll64(rsrc_t ws, unsigned voff, v2u want) {
  const unsigned long long mask = __builtin_amdgcn_ballot_w64(voff < OOB);
  for (unsigned spin = 0; spin < SPIN_CAP; ++spin) {
    asm volatile("" ::: "memory");
    if (hit64(hld64(ws, voff, 0), want, mask)) break;
  }
  __builtin_amdgcn_fence(__ATOMIC_ACQUIRE, "wavefront");
}
DI bool wflags_ok(v4u a, int lane, int nl, v2u want) {
  return __all(lane >= nl || (a.x == want.x && a.y == want.y && a.z == want.x && a.w == want.y));
}
DI void pollw(rsrc_t ws, unsigned voff, int lane, int nl, v2u want) {
  for (unsigned spin = 0; spin < SPIN_CAP; ++spin) {
    asm volatile("" ::: "memory");
    if (wflags_ok(hld128(ws, voff, 0), lane, nl, want)) break;
    __builtin_amdgcn_s_sleep(4);
  }
  __builtin_amdgcn_fence(__ATOMIC_ACQUIRE, "wavefront");
}
DI float rbf(float x) { return lo2f(pk2(x, 0.f)); }
DI void ln_scal(f2 s, f2 ss, f2& r, f2& mr, f2* pmean = nullptr, f2* pden = nullptr) {
  const f2 m = s * (1.f / D_);
  const f2 var = ss * (1.f / D_) - m * m;
  const f2 v = f2{fmaxf(var.x, 0.f), fmaxf(var.y, 0.f)} + 1e-5f;
  r = f2{__builtin_amdgcn_rsqf(v.x), __builtin_amdgcn_rsqf(v.y)};
  mr = -m * r;
  if (pmean) *pmean = m;
  if (pden) *pden = v * r;
}
static constexpr float GC0 = -2.f * 0.7978845608028654f * 1.4426950408889634f, GC1 = GC0 * 0.044715f;
template <int N, class Mid> DI void gelu_pack(const f2 (&x)[N], float c0, float c1, float den, unsigned* o, Mid mid) {
  f2 a[N];
#define STAGE(expr) _Pragma("unroll") for (int i = 0; i < N; ++i) a[i] = expr; _Pragma("unroll") for (int i = 0; i < N; ++i) KEEP(a[i]); PIN()
  STAGE(x[i] * x[i]);
  mid(0);
  STAGE(a[i] * c1 + c0);
  STAGE(a[i] * x[i]);
  mid(1);
  STAGE((f2{__builtin_amdgcn_exp2f(a[i].x), __builtin_amdgcn_exp2f(a[i].y)}));
  mid(2);
  STAGE(a[i] * den + den);
  STAGE((f2{__builtin_amdgcn_rcpf(a[i].x), __builtin_amdgcn_rcpf(a[i].y)}));
  mid(3);
  STAGE(a[i] * x[i]);
#undef STAGE
#pragma unroll
  for (int i = 0; i < N; ++i) o[i] = pk2(a[i].x, a[i].y);
}


static constexpr int RT_BYTES = 32 * 896;
template <bool WIDE> struct Job {
  static constexpr int NL = WIDE ? 7 : 5, RB = WIDE ? 896 : 640, PITCH = WIDE ? F_ * 2 : D_ * 2;
  v4u r[NL];
  unsigned dst;
  DI void load(int job, int w, const unsigned (&vo)[NL], const void* w1, const void* w2, const void* wx) {
    const bool big = WIDE || job >= 80;
    const int jj = WIDE ? job : (job >= 80 ? job - 80 : (job >= 40 ? job - 40 : job));
    const int gq = WIDE ? jj & 3 : jj & 1, ks = WIDE ? jj >> 2 : jj >> 1;
    const rsrc_t rs = mkrs(big ? wx : (job >= 40 ? w2 : w1), big ? D_ * F_ * 2 : D_ * D_ * 2);
    const unsigned src = (32 * ks + 8 * w) * PITCH + gq * RB;
    dst = (WIDE ? O_W3 : (job >= 80 ? O_W4 : (job >= 40 ? O_W2 : O_W1))) + ((gq * 4 + w) * (!WIDE && job >= 80 ? 56 : 20) + ks) * (NL * 1024);
#pragma unroll
    for (int i = 0; i < NL; ++i)
      r[i] = ld128(rs, (WIDE && gq == 3 && (vo[i] & 1)) ? OOB : (vo[i] & ~1u) + src, 0);
  }
  DI void put(int w, int lane, LAS char* L) {
#pragma unroll
    for (int i = 0; i < NL; ++i) *(LAS v4u*)(L + w * (8 * RB) + lane * 16 + i * 1024) = r[i];
  }
  DI void emit(int w, int lane, LAS char* L, rsrc_t ws) {
    const int g = lane >> 4, q = (lane & 15) >> 2, p = lane & 3;
    LAS char* row = L + (8 * g + q) * RB + w * (RB / 4);
    LAS char* pr = row + 32 * (w & 1) + 16 * p;
    LAS char* sg = row + ((w & 1) ? 0 : RB / 4 - 32) + 8 * p;
    v2u lo[NL], hi[NL];
#pragma unroll
    for (int f = 0; f < NL; ++f) {
      LAS char* c = f < NL - 1 ? pr + 64 * (f >> 1) + 8 * (f & 1) : sg;
      lo[f] = __builtin_bit_cast(v2u, __builtin_amdgcn_ds_read_tr16_b64_v4i16((LAS s4v*)c));
      hi[f] = __builtin_bit_cast(v2u, __builtin_amdgcn_ds_read_tr16_b64_v4i16((LAS s4v*)(c + 4 * RB)));
    }
#pragma unroll
    for (int f = 0; f < NL; ++f) hst128(v4u{lo[f].x, lo[f].y, hi[f].x, hi[f].y}, ws, lane * 16 + dst, f * 1024);
  }
};

template <class Extra, class Gap>
DI void gemm_ks(rsrc_t ws, unsigned abase, const unsigned (&so)[4], const v4u* b, LAS char* L, int w, int lane, f4 (&acc)[5],
                Extra extra, Gap gap) {
  constexpr int KS = 5, WIN = 12, NA = 4 * KS, RING = WIN + 1;
  v4u a[RING];
  auto lda = [&](int n) { a[n % RING] = hld128(ws, abase + (n % KS) * 1024, so[n / KS]); };
#pragma unroll
  for (int n = 0; n < WIN; ++n) {
    lda(n);
    PIN();
  }
  f4 ac[2][5], p[15];
  unsigned lr = RED_OFF + lane * 16;
  KEEP(lr);
  sfor<0, NA>([&](auto ic) {
    constexpr int n = decltype(ic)::v, i = n / KS, k = n % KS;
    if (n == NA - 3) {
      WGBAR();
    }
#pragma unroll
    for (int t = 0; t < 5; ++t) {
      if (k == 0) mfma_av0(ac[i & 1][t], b[t], a[n % RING]);
      else mfma_av(ac[i & 1][t], b[k * 5 + t], a[n % RING]);
      if (t == 1) {
        if constexpr (n + WIN < NA) lda(n + WIN);
      }
      if (t == (n + WIN < NA ? 4 : 1) && n >= NA - 12) extra(n - (NA - 12));
      if (k == 0 && i > 0 && t > 0) *(LAS f4*)(L + (lr + ((w + i) & 3) * 15360) + ((i - 1) * 5 + t - 1) * 1024) = ac[(i - 1) & 1][t - 1];
      if (k == 1 && i > 0 && t == 0) *(LAS f4*)(L + (lr + ((w + i) & 3) * 15360) + ((i - 1) * 5 + 4) * 1024) = ac[(i - 1) & 1][4];
      if (n == NA - 3) {
        p[t] = *(LAS f4*)(L + (lr + w * 15360) + t * 1024);
        p[5 + t] = *(LAS f4*)(L + (lr + w * 15360) + (5 + t) * 1024);
      }
      if (n == NA - 2) p[10 + t] = *(LAS f4*)(L + (lr + w * 15360) + (10 + t) * 1024);
      if (n == NA - 1) {
        p[t][0] = add1(p[t][0], p[5 + t][0]);
        p[t][1] = add1(p[t][1], p[5 + t][1]);
        KEEP(p[t]);
      }
      gap(n, t);
      PIN();
    }
  });
#pragma unroll
  for (int t = 0; t < 5; ++t) {
    p[t][2] += p[5 + t][2];
    p[t][3] += p[5 + t][3];
    p[t] += p[10 + t];
    KEEP(p[t]);
  }
  PIN();
#pragma unroll
  for (int t = 0; t < 5; ++t) acc[t] = ac[1][t] + p[t];
}

#define YOUNG() do { asm volatile("; YOUNG" ::: "memory"); PIN(); } while (0)
#define WAITV(n) asm volatile("s_waitcnt vmcnt(" #n ") ; WAITV" ::: "memory")
__global__ __launch_bounds__(256, 1) void fused(const void*, const void*, const void*, const void*, const void*, const void*,
                                                const void*, const void*, const void*, const void*, const void*, const void*,
                                                void*, void*) {
  extern __shared__ __attribute__((aligned(16))) char smem[];
  LAS char* L = (LAS char*)smem;
  unsigned pf = 0;
  if (blockIdx.x >= 160) {
    unsigned long long pc;
    unsigned off = (((blockIdx.x >> 3) & 1) * 256 + threadIdx.x) * 128;
    asm volatile("s_getpc_b64 %1\n.Lfused_pc:\n\tv_min_u32 %2, .Lfused_end-.Lfused_pc, %2\n\tglobal_load_dword %0, %2, %1"
                 : "=&v"(pf), "=&s"(pc), "+v"(off));
  }
  PIN();
  typedef void* const __attribute__((address_space(4))) * karg_t;
  const karg_t ka = (karg_t)__builtin_amdgcn_kernarg_segment_ptr();
  void *gq = ka[0], *gk = ka[1], *gv = ka[2], *gsk = ka[3], *gssm = ka[4], *gD = ka[5], *gw1 = ka[6], *gw2 = ka[7], *gw3 = ka[8],
       *gw4 = ka[9], *gc = ka[10], *gwm = ka[11], *gout = ka[12], *gws = ka[13];
  asm volatile("" ::"s"(gq), "s"(gk), "s"(gv), "s"(gsk), "s"(gssm), "s"(gD), "s"(gw1), "s"(gw2), "s"(gw3), "s"(gw4),
               "s"(gc), "s"(gwm), "s"(gout), "s"(gws));
  const int tid = threadIdx.x, lane = tid & 63;
  const int w = __builtin_amdgcn_readfirstlane(tid >> 6);
  const int b = blockIdx.x, x = b & 7, j = b >> 3;
  const int tm = 4 * x + (j >> 3), tn = j & 7;
  const int rg = 2 * x + (j >> 4), cg = j & 15;
  const bool conv_wg = j < 20;
  const rsrc_t ws = mkrs(gws, O_END);
  const int g = lane >> 4, l15 = lane & 15;

  const int rt0 = 64 * tm + 8 * tn + 2 * w;
  const int rcm = 8 * lane, rco = 512 + 2 * lane;
  v4u qm[2];
  unsigned qo[2];
  auto load_q = [&]() {
    const rsrc_t rq = mkrs(gq, SD2);
#pragma unroll
    for (int t = 0; t < 2; ++t) {
      qm[t] = ld128(rq, rcm * 2, (rt0 + t) * D_ * 2);
      qo[t] = ld32(rq, rco * 2, (rt0 + t) * D_ * 2);
    }
  };
  v2u tag;
  auto make_tag = [&](v2u ep) {
    tag.x = __builtin_amdgcn_readfirstlane(ep.x) * 0x9E3779B1u + 0x7F4A7C15u;
    tag.y = (__builtin_amdgcn_readfirstlane(ep.y) ^ tag.x) * 0x85EBCA77u + 0x2545F491u;
    tag.x &= 0xffffff00u;
    tag.y |= 1u;
  };
  v4u b1[25], b2[25];
  const unsigned sb1 = O_W1 + (tn * 100 + w * 25) * 1024, sb2 = O_W2 + (tn * 100 + w * 25) * 1024;
  auto zero_h1 = [&](int i) {
    hst128(v4u{0u, 0u, 0u, 0u}, ws, (i < 2 || tid < 128) ? tid * 16 : OOB, O_H1 + b * 10240 + i * 4096);
  };
  v4u mm[5];
  unsigned mo[5];
  auto load_mod = [&](bool ok) {
    unsigned o1 = (ok ? 0u : OOB) | (lane * 16), o2 = (ok ? 0u : OOB) | (lane * 4 + 1024);
    asm volatile("" : "+v"(o1), "+v"(o2));
#pragma unroll
    for (int i = 0; i < 5; ++i) {
      mm[i] = hld128(ws, o1, O_MOD + i * D_ * 2);
      mo[i] = __builtin_amdgcn_raw_buffer_load_b32(ws, o2, O_MOD + i * D_ * 2, 16);
    }
  };
  f2 qv[10], n1[10];
  auto ln1 = [&]() {
#pragma unroll
    for (int i = 0; i < 4; ++i) {
      qv[2 * i] = f2{lo2f(qm[0][i]), lo2f(qm[1][i])};
      qv[2 * i + 1] = f2{hi2f(qm[0][i]), hi2f(qm[1][i])};
    }
    qv[8] = f2{lo2f(qo[0]), lo2f(qo[1])};
    qv[9] = f2{hi2f(qo[0]), hi2f(qo[1])};
    f2 s = qv[0], ss = qv[0] * qv[0];
#pragma unroll
    for (int i = 1; i < 10; ++i) { s += qv[i]; ss += qv[i] * qv[i]; }
    wave_sum4(s, ss);
    f2 r, mr;
    ln_scal(s, ss, r, mr);
#pragma unroll
    for (int i = 0; i < 10; ++i) n1[i] = qv[i] * r + mr;
  };
  unsigned z3off, im0, io0, roff, roff3, own_r, x1m, x1s, stoff, h1m, h1s, f1pub, f1wait, frpub, frwait, f2pub, f2wait, f3pub, f3wait;
  const unsigned own_m = ((4 * tm + w) * 20 + 5 * (tn >> 1) + 3 * (tn & 1)) * 1024;
  const unsigned own_s = ((4 * tm + w) * 20 + 5 * (tn >> 1) + 2) * 1024 + (tn & 1) * 512;
  auto offsets = [&]() {
    z3off = tid < 128 ? tid * 32 + 16384 : OOB;
    im0 = RED_OFF + 16384 + lane * 128 + (((2 * w) ^ (lane & 7)) << 4);
    io0 = RED_OFF + 16384 + (64 + (lane >> 2)) * 128 + (((2 * w) ^ ((lane >> 2) & 7)) << 4) + 4 * (lane & 3);
    roff = O_A1 + ((4 * tm + (tn >> 1)) * 20 + (tid >> 5)) * 1024 + (((tid ^ (tid >> 3)) & 7) + 8 * (tn & 1) + 16 * ((tid >> 3) & 3)) * 16;
    roff3 = tid < 128 ? roff : OOB;
    own_r = (l15 + 16 * (g >> 1)) * 16 + 8 * (g & 1);
    x1m = O_X1 + own_m + lane * 16;
    x1s = (g & 1) ? OOB + SD2 : O_X1 + own_s + (l15 + 16 * (g >> 1)) * 16;
    stoff = (g & 1) ? OOB : O_STAT + ((4 * tm + w) * 8 + tn) * 128 + l15 * 8 + (g >> 1) * 4;
    f3pub = lane == 0 ? O_F3 + rg * 512 + (w >> 1) * 256 + cg * 16 + (w & 1) * 8 : OOB;
    f3wait = lane < 8 ? O_F3 + rg * 512 + (tm & 1) * 256 + w * 64 + lane * 8 : OOB;
    const int sh = (tn >> 1) == 0 ? 1 : ((tn >> 1) == 1 ? -1 : ((tn >> 1) == 2 ? 2 : -2));
    const int t2 = 64 * tm + 16 * w + l15 + sh;
    const bool ok = t2 >= 0 && t2 < S_;
    const unsigned d = O_H1 + ((t2 >> 4) * 20 + 5 * (tn >> 1)) * 1024 + (t2 & 15) * 16;
    h1m = ok ? d + 3072 * (tn & 1) + 256 * g : OOB;
    h1s = (ok && !(g & 1)) ? d + 2048 + 512 * (tn & 1) + 256 * (g >> 1) : OOB;
    const int nb = tm + (sh > 0 ? 1 : -1);
    f1pub = OOB;
    if (lane == 0) f1pub = O_F1 + tm * 128 + tn * 8;
    if (lane == 1 && nb >= 0 && nb < 32) f1pub = O_F1 + nb * 128 + 64 + tn * 8;
    const bool up = ((lane >> 1) & 1) == 0;
    f1wait = (lane < 8 || (lane < 16 && (up ? tm > 0 : tm < 31))) ? O_F1 + tm * 128 + lane * 8 : OOB;
    frpub = lane == 0 ? O_FR + tm * 128 + tn * 8 : OOB;
    frwait = lane < 8 ? O_FR + tm * 128 + lane * 8 : OOB;
    f2pub = lane == 0 ? O_F2 + (tm >> 1) * 128 + ((tm & 1) * 8 + tn) * 8 : OOB;
    f2wait = lane < 16 ? O_F2 + rg * 128 + lane * 8 : OOB;
    asm volatile("" : "+v"(frpub), "+v"(frwait), "+v"(f2pub), "+v"(f2wait));
    asm volatile("" : "+v"(z3off), "+v"(im0), "+v"(io0), "+v"(roff), "+v"(roff3), "+v"(own_r), "+v"(f1pub), "+v"(f1wait));
    asm volatile("" : "+v"(x1m), "+v"(x1s), "+v"(stoff), "+v"(f3pub), "+v"(f3wait));
    asm volatile("" : "+v"(h1m), "+v"(h1s));
  };
  constexpr int NB2 = 19;
  bool wok = false, bok = false;
  if (conv_wg) {
    const int cgp = j < 16 ? x : 8 + (x >> 2), ci = j < 16 ? j : (x & 3) * 4 + (j - 16);
    const int pT0 = 128 * ci + 32 * w + 8 * ((lane >> 3) & 3) + 4 * (lane >> 5), pC0 = 64 * cgp + 8 * (lane & 7);
    const int ch = 64 * cgp + 4 * ci + w;
    v4u kw[7], vw[4], skw[4], kr[4];
    {
      const rsrc_t rk = mkrs(gk, SD2), rv = mkrs(gv, SD2), rs = mkrs(gsk, D_ * 8);
      const unsigned o = (unsigned)(pT0 * D_ + pC0) * 2;
#pragma unroll
      for (int i = 0; i < 7; ++i) kw[i] = __builtin_amdgcn_raw_buffer_load_b128(rk, o + (i - 3) * D_ * 2, 0, 0);
#pragma unroll
      for (int i = 0; i < 4; ++i) vw[i] = ld128(rv, o + i * D_ * 2, 0);
#pragma unroll
      for (int i = 0; i < 4; ++i) skw[i] = __builtin_amdgcn_raw_buffer_load_b128(rs, pC0 * 8 + i * 16, 0, 0);
      PIN();
      offsets();
      PIN();
      asm volatile("s_waitcnt vmcnt(4)" ::: "memory");
      const rsrc_t rK = mkrs((const char*)gssm + (size_t)ch * 4096, 4096);
#pragma unroll
      for (int i = 0; i < 4; ++i) kr[i] = ld128(rK, lane * 16 + i * 1024, 0);
    }
    const v2u ep = hld64(ws, lane == 0 ? O_EPOCH : O_FP + x * 256 + (j >= 16) * 128, 0);
    const unsigned dvb = __builtin_amdgcn_raw_buffer_load_b16(mkrs(gD, D_ * 2), ch * 2, 0, 0);
    PIN();
    {
      unsigned pe[8][2];
#pragma unroll
      for (int pr = 0; pr < 4; ++pr) {
        const f2 w0 = {lo2f(skw[pr].x), lo2f(skw[pr].z)}, w1 = {hi2f(skw[pr].x), hi2f(skw[pr].z)};
        const f2 w2 = {lo2f(skw[pr].y), lo2f(skw[pr].w)}, w3 = {hi2f(skw[pr].y), hi2f(skw[pr].w)};
        f2 kf[7], o[4];
#pragma unroll
        for (int i = 0; i < 7; ++i) kf[i] = up2(kw[i][pr]);
#pragma unroll
        for (int i = 0; i < 4; ++i) o[i] = (kf[i + 3] * w0 + kf[i + 2] * w1 + kf[i + 1] * w2 + kf[i] * w3) * up2(vw[i][pr]);
        pe[2 * pr][0] = pk2(o[3].x, o[2].x);
        pe[2 * pr][1] = pk2(o[1].x, o[0].x);
        pe[2 * pr + 1][0] = pk2(o[3].y, o[2].y);
        pe[2 * pr + 1][1] = pk2(o[1].y, o[0].y);
      }
      const unsigned so = (pC0 + 4 * (lane >> 5)) * KVP + (pT0 & ~7) * 2;
#pragma unroll
      for (int e = 0; e < 4; ++e) {
        const auto s0 = __builtin_amdgcn_permlane32_swap(pe[e][0], pe[e + 4][0], false, false);
        const auto s1 = __builtin_amdgcn_permlane32_swap(pe[e][1], pe[e + 4][1], false, false);
        hst128(v4u{s0[1], s1[1], s0[0], s1[0]}, ws, so, O_KVT + e * KVP);
      }
    }
    PIN();
    LAS char* T = L + w * WR;
    if (lane < 8) *(LAS v4u*)(T + lane * CS) = v4u{0u, 0u, 0u, 0u};
#pragma unroll
    for (int i = 0; i < 4; ++i) *(LAS v4u*)(T + 7 * CS + (i * 64 + lane + 1) * 16) = kr[i];
    asm volatile("s_waitcnt lgkmcnt(0)" ::: "memory");
    v4u kl[4];
#pragma unroll
    for (int i = 0; i < 4; ++i) kl[i] = *(LAS v4u*)(T + 7 * CS + (i * 64 + lane) * 16);
    auto tbuild = [&](int i) {
      const unsigned d[8] = {kl[i].x, kl[i].y, kl[i].z, kl[i].w, kr[i].x, kr[i].y, kr[i].z, kr[i].w};
      unsigned a[7];
#pragma unroll
      for (int e = 0; e < 7; ++e) a[e] = __builtin_amdgcn_alignbit(d[e + 1], d[e], 16);
#pragma unroll
      for (int m = 0; m < 7; ++m) {
        const int h = (m + 1) >> 1;
        const v4u o = (m & 1) ? v4u{d[h], d[h + 1], d[h + 2], d[h + 3]} : v4u{a[h], a[h + 1], a[h + 2], a[h + 3]};
        *(LAS v4u*)(T + m * CS + (i * 64 + lane + 1) * 16) = o;
      }
    };
    tbuild(0);
    make_tag(ep);
    DRAIN();
    asm volatile("s_barrier" ::: "memory");
    if (w == 0) {
      unsigned o = OOB;
      if (j < 16 && lane == 0) o = O_FP + x * 256 + ci * 8;
      if (j >= 16 && lane < 4) o = O_FP + ((x & 4) + lane) * 256 + 128 + ci * 8;
      hst64(tag, ws, o, 0);
    }
    tbuild(1);
    tbuild(2);
    tbuild(3);
    if (lane < 32) *(LAS v4u*)(T + UOFF + lane * 16) = v4u{0u, 0u, 0u, 0u};
    const LAS char* pa = T + (l15 & 7) * CS + (3 - g + (l15 >> 3)) * 16;
    const LAS char* pv = T + UOFF + 512 + (l15 - 1) * 32 + g * 16;
    v4u fa[2][8], fv[2][8];
    asm volatile("s_waitcnt lgkmcnt(0)" ::: "memory");
#pragma unroll
    for (int a = 0; a < 8; ++a) fa[0][a] = *(const LAS v4u*)(pa + 8 * a * 64);
    f4 acc[8];
    v4u ad;
    {
      const int jd = 8 * g - 9 - l15;
#pragma unroll
      for (int e = 0; e < 4; ++e) ad[e] = jd == 2 * e ? dvb : (jd == 2 * e + 1 ? dvb << 16 : 0u);
      asm volatile("" : "+v"(ad));
    }
    const rsrc_t ru = mkrs((const char*)gws + O_KVT + ch * KVP, 4096);
    unsigned v0off = (l15 - 1) * 32 + g * 16, v1off = v0off + 512;
    asm volatile("" : "+v"(v0off), "+v"(v1off));
    if (w == 0) poll64(ws, lane < 16 ? O_FP + x * 256 + (j >= 16) * 128 + lane * 8 : OOB, tag);
    WGBAR();
#pragma unroll
    for (int a = 0; a < 8; ++a) {
      fv[0][a] = hld128(ru, a == 0 ? v0off : v1off + (a - 1) * 512, 0);
      PIN();
    }
    v4u ur[4];
#pragma unroll
    for (int i = 0; i < 4; ++i) ur[i] = hld128(ru, lane * 16 + i * 1024, 0);
    const v4u wf = hld128(ws, O_FW + x * 1024 + lane * 16, 0);
    PIN();
#pragma unroll
    for (int q = 0; q < 8; ++q) mfma_v0(acc[q], ad, fv[0][q]);
    PIN();
#pragma unroll
    for (int i = 0; i < 4; ++i) *(LAS v4u*)(T + UOFF + 512 + i * 1024 + lane * 16) = ur[i];
    unsigned boff = OOB;
    PIN();
#pragma unroll
    for (int g0 = 0; g0 < 8; ++g0) {
      int n = 0;
      if (g0 == 0) load_q();
      if (g0 == 1) {
        wok = wflags_ok(wf, lane, 40, tag);
        __builtin_amdgcn_fence(__ATOMIC_ACQUIRE, "wavefront");
        boff = wok ? lane * 16 : OOB;
      }
#pragma unroll
      for (int a = 0; a < 8; ++a)
#pragma unroll
        for (int q = a; q < 8; ++q) {
          mfma_v(acc[q], fa[g0 & 1][a], fv[g0 & 1][q - a]);
          ++n;
          if ((n & 1) == 0) {
            const int e = n / 2 - 1;
            if (g0 < 7 && e < 16) {
              if (e < 8) fv[(g0 + 1) & 1][e] = *(const LAS v4u*)(pv + e * 512 - (g0 + 1) * 64);
              else fa[(g0 + 1) & 1][e - 8] = *(const LAS v4u*)(pa + (g0 + 1 + 8 * (e - 8)) * 64);
            }
            PIN();
          }
          if (n % 5 == 3) {
            const int i = g0 * 7 + n / 5 - 12;
            if (i < -9) zero_h1(i + 12);
            if (i >= 0 && i < 25) b1[i] = hld128(ws, boff + (i & 3) * 1024, sb1 + (i >> 2) * 4096);
            if (i >= 25 && i < 25 + NB2) b2[i - 25] = hld128(ws, boff + ((i - 25) & 3) * 1024, sb2 + ((i - 25) >> 2) * 4096);
            PIN();
          }
        }
    }
    const v4u bfl = hld128(ws, O_FB + x * 1024 + lane * 16, 0);
    MFENCE();
    PIN();
#pragma unroll
    for (int q = 0; q < 8; q += 2) {
      const auto s0 = __builtin_amdgcn_permlane16_swap(pk2(acc[q][0], acc[q][1]), pk2(acc[q + 1][0], acc[q + 1][1]), false, false);
      const auto s1 = __builtin_amdgcn_permlane16_swap(pk2(acc[q][2], acc[q][3]), pk2(acc[q + 1][2], acc[q + 1][3]), false, false);
      *(LAS v4u*)(T + 64 * w + (32 * (q + (g & 1)) + 2 * l15 + (g >> 1)) * 16) = v4u{s0[0], s1[0], s0[1], s1[1]};
    }
    WGBAR();
    {
      v4u zo[4];
#pragma unroll
      for (int i = 0; i < 4; ++i) zo[i] = *(LAS v4u*)(L + ((lane >> 1) & 3) * (WR + 64) + (64 * w + 16 * i + 2 * (lane >> 3) + (lane & 1)) * 16);
#pragma unroll
      for (int i = 0; i < 4; ++i) hst128(zo[i], ws, (lane >> 3) * ZP + (lane & 7) * 16, O_ZT + (32 * w + 8 * i) * ZP + (ch - w) * 32);
    }
    YOUNG();
    bok = wflags_ok(bfl, lane, 48, tag);
    __builtin_amdgcn_fence(__ATOMIC_ACQUIRE, "wavefront");
    load_mod(bok);
    PIN();
    ln1();
    PIN();
    WAITV(10);
  } else {
    const int hb = (j - 20) * 8 + x;
    unsigned vn[5];
#pragma unroll
    for (int i = 0; i < 5; ++i) vn[i] = (i * 64 + lane) * 16 + (((i * 64 + lane) * 1639u) >> 16) * 640;
    Job<false> j0, j1;
    j0.load(hb, w, vn, gw1, gw2, gw4);
    const v2u ep = hld64(ws, O_EPOCH, 0);
    const v2u cw = __builtin_amdgcn_raw_buffer_load_b64(mkrs(gc, D_ * 2), tid * 8, 0, 0);
    PIN();
    unsigned vw[7];
#pragma unroll
    for (int i = 0; i < 7; ++i) {
      const unsigned c = i * 64 + lane, row = (c * 1171u) >> 16;
      vw[i] = c * 16 + row * 2560 + (c - row * 56 >= 48);
      KEEP(vw[i]);
    }
    offsets();
    LAS float* sc = (LAS float*)(L + 4 * RT_BYTES);
    {
      const float c4[4] = {lo2f(cw.x), hi2f(cw.x), lo2f(cw.y), hi2f(cw.y)};
      f4 sv;
#pragma unroll
      for (int e = 0; e < 4; ++e) sv[e] = rbf(c4[e] * __builtin_amdgcn_rcpf(1.f + __builtin_amdgcn_exp2f(-1.4426950408889634f * c4[e])));
      *(LAS f4*)(sc + tid * 4) = sv;
    }
    j0.put(w, lane, L);
    make_tag(ep);
    WGBAR();
    j0.emit(w, lane, L, ws);
    if (j == 20 && w == 0) hst128(v4u{tag.x, tag.y, 0u, 0u}, ws, (lane >> 3) * 1024 + (lane & 7) * 16, O_CNT + x * 128);
    asm volatile("" ::"v"(pf) : "memory");
    auto first_pub = [&]() {
      WGBAR();
      if (w == 0) hst64(tag, ws, lane < 8 ? O_FW + lane * 1024 + hb * 8 : OOB, 0);
    };
    if (hb < 60) {
      const rsrc_t rm = mkrs(gwm, D_ * 6 * D_ * 2);
      v4u wm[20];
      YOUNG();
#pragma unroll
      for (int i = 0; i < 8; ++i) wm[i] = ld128(rm, (tid >> 3) * (6 * D_ * 2) + (tid & 7) * 16, hb * 128 + i * 32 * (6 * D_ * 2));
      PIN();
      WAITV(8);
      first_pub();
#pragma unroll
      for (int i = 8; i < 20; ++i) wm[i] = ld128(rm, (tid >> 3) * (6 * D_ * 2) + (tid & 7) * 16, hb * 128 + i * 32 * (6 * D_ * 2));
      j1.load(132 + hb, w, vn, gw1, gw2, gw4);
      load_q();
      float sl[20];
#pragma unroll
      for (int i = 0; i < 20; ++i) sl[i] = sc[(tid >> 3) + 32 * i];
      PIN();
      f2 a[4] = {f2{0.f, 0.f}, f2{0.f, 0.f}, f2{0.f, 0.f}, f2{0.f, 0.f}};
#pragma unroll
      for (int i = 0; i < 20; ++i)
#pragma unroll
        for (int e = 0; e < 4; ++e) a[e] += up2(wm[i][e]) * sl[i];
      LAS char* mred = L + 4 * RT_BYTES + 4096;
      *(LAS f4*)(mred + (tid >> 3) * 272 + (tid & 7) * 32) = f4{a[0].x, a[0].y, a[1].x, a[1].y};
      *(LAS f4*)(mred + (tid >> 3) * 272 + (tid & 7) * 32 + 16) = f4{a[2].x, a[2].y, a[3].x, a[3].y};
      WGBAR();
      {
        const int col = tid >> 2, part = tid & 3;
        float v[8];
#pragma unroll
        for (int r = 0; r < 8; ++r) v[r] = *(LAS float*)(mred + (part * 8 + r) * 272 + col * 4);
        float s = ((v[0] + v[1]) + (v[2] + v[3])) + ((v[4] + v[5]) + (v[6] + v[7]));
        s += dpp<0xB1>(s);
        s += dpp<0x4E>(s);
        s = rbf(s);
        if ((hb >= 10 && hb < 20) || (hb >= 40 && hb < 50)) s = rbf(1.f + s);
        if (part == 0) *((LAS unsigned short*)sc + 8 + col) = (unsigned short)(__builtin_bit_cast(unsigned, s) >> 16);
      }
      j1.put(w, lane, L);
      WGBAR();
      if (w == 0) {
        hst128(*(LAS v4u*)((LAS char*)sc + 16 + (lane & 7) * 16), ws, lane < 8 ? lane * 16 : OOB, O_MOD + hb * 128);
        DRAIN();
        hst64(tag, ws, lane < 8 ? O_FB + lane * 1024 + hb * 8 : OOB, 0);
      }
    } else {
      Job<true> ja, jb, jc;
      YOUNG();
      ja.load(hb - 60, w, vw, gw1, gw2, gw3);
      PIN();
      WAITV(7);
      first_pub();
      jb.load(hb - 24, w, vw, gw1, gw2, gw3);
      if (hb < 68) jc.load(hb + 12, w, vw, gw1, gw2, gw3);
      j1.load(36 + hb, w, vn, gw1, gw2, gw4);
      load_q();
      PIN();
      ja.put(w, lane, L + RT_BYTES);
      jb.put(w, lane, L + 2 * RT_BYTES);
      if (hb < 68) jc.put(w, lane, L + 3 * RT_BYTES);
      WGBAR();
      ja.emit(w, lane, L + RT_BYTES, ws);
      jb.emit(w, lane, L + 2 * RT_BYTES, ws);
      if (hb < 68) jc.emit(w, lane, L + 3 * RT_BYTES, ws);
      j1.put(w, lane, L);
      DRAIN();
      WGBAR();
      if (w == 0) hst64(tag, ws, lane < 8 ? O_FB + lane * 1024 + hb * 8 : OOB, 0);
    }
    pollw(ws, O_FW + x * 1024 + lane * 16, lane, 40, tag);
    j1.emit(w, lane, L, ws);
    zero_h1(0);
    zero_h1(1);
    zero_h1(2);
    if (hb >= 34 && hb < 60) hst128(v4u{0u, 0u, 0u, 0u}, ws, w == 0 ? lane * 16 : OOB, O_FR + (hb - 34) * 1024);
    YOUNG();
#pragma unroll
    for (int i = 0; i < 10; ++i) b1[i] = hld128(ws, lane * 16, sb1 + i * 1024);
    PIN();
    ln1();
    PIN();
    WAITV(10);
  }
  if (conv_wg && !wok) pollw(ws, O_FW + x * 1024 + lane * 16, lane, 40, tag);
  WGBAR();
  if (w == 0 && lane < 8)
    __hip_atomic_fetch_add((GAS unsigned*)((GAS char*)gws + O_CNT + (lane * 8 + x) * 128), 1u, __ATOMIC_RELAXED, __HIP_MEMORY_SCOPE_AGENT);
  if (!wok) {
    if (conv_wg) {
#pragma unroll
      for (int i = 0; i < 10; ++i) b1[i] = hld128(ws, lane * 16, sb1 + i * 1024);
    }
#pragma unroll
    for (int i = 10; i < 25; ++i) b1[i] = hld128(ws, lane * 16, sb1 + i * 1024);
#pragma unroll
    for (int i = 0; i < NB2; ++i) b2[i] = hld128(ws, lane * 16, sb2 + i * 1024);
  }
#pragma unroll
  for (int i = NB2; i < 25; ++i) b2[i] = hld128(ws, lane * 16, sb2 + i * 1024);
  v4u b3[35], b4[70];
  unsigned sb3 = O_W3 + (cg * 140 + w * 35) * 1024, sb4 = O_W4 + (tn * 280 + w * 70) * 1024;
  unsigned sa3 = O_X1 + (8 * rg + 2 * w) * 20 * 1024, som[4];
#pragma unroll
  for (int i = 0; i < 4; ++i) som[i] = ((4 * tm + ((w + 1 + i) & 3)) * 20 + 5 * w) * 1024;
  unsigned va1 = O_A1 + lane * 16, vh1 = O_H1 + lane * 16;
  asm volatile("" : "+s"(sb3), "+s"(sb4), "+s"(sa3), "+s"(som[0]), "+s"(som[1]), "+s"(som[2]), "+s"(som[3]));
  asm volatile("" : "+v"(va1), "+v"(vh1));
#define LB3(lo, hi) _Pragma("unroll") for (int i_ = lo; i_ < hi; ++i_) b3[i_] = hld128(ws, lane * 16 + (i_ & 3) * 1024, sb3 + (i_ >> 2) * 4096)
  auto ks4 = [](int s) { return s < 8 ? 7 * (s >> 2) + (s & 1) + 4 * ((s >> 1) & 1) : 7 * ((s - 8) / 3) + ((s - 8) % 3 == 2 ? 6 : 2 + (s - 8) % 3); };
  auto lb4 = [&](int i) {
    const int f = ks4(i / 5) * 5 + i % 5;
    b4[i] = hld128(ws, lane * 16 + (f & 3) * 1024, sb4 + (f >> 2) * 4096);
  };
#define LB4(lo, hi) _Pragma("unroll") for (int i_ = lo; i_ < hi; ++i_) lb4(i_)
  if (!bok) {
    pollw(ws, O_FB + x * 1024 + lane * 16, lane, 48, tag);
    load_mod(true);
  }
  constexpr int NB3C = 10;
  f2 gh[10];
  float cb1[10], sbv[10];
  auto md = [&](int v, int i) { const unsigned u = i < 8 ? mm[v][(i >> 1) & 3] : mo[v]; return (i & 1) ? hi2f(u) : lo2f(u); };
  auto use_mod = [&]() {
    LB3(0, NB3C);
#pragma unroll
    for (int i = 0; i < 10; ++i) {
      gh[i] = (n1[i] * md(1, i) + md(0, i)) * md(2, i);
      cb1[i] = md(4, i);
      sbv[i] = md(3, i);
      asm volatile("" : "+v"(gh[i]), "+v"(cb1[i]), "+v"(sbv[i]));
    }
  };
  use_mod();
  PIN();
  if (w == 0) {
    poll64(ws, lane < 8 ? O_CNT + (x * 8 + lane) * 128 : OOB, v2u{tag.x + 32, tag.y});
    if (b == 0) hst64(tag, ws, lane == 0 ? O_EPOCH : OOB, 0);
  }
  WGBAR();

  v4u hx[6];
  f4 csp[7];
  auto img3 = [&](int lo, int hi, int hc) {
    const v4u ones = {0x3f803f80u, 0x3f803f80u, 0x3f803f80u, 0x3f803f80u};
    unsigned la = w * 25600 + lane * 16, lb = (75 + w * 10) * 1024 + lane * 16;
    asm volatile("" : "+v"(la), "+v"(lb));
#pragma unroll
    for (int i = lo; i < 35; ++i) {
      if (i < hi) *(LAS v4u*)(L + (i < 25 ? la : lb) + i * 1024) = b3[i];
      if (i < hc) csp[i % 7] = mfma(b3[i], ones, i < 7 ? f4{0.f, 0.f, 0.f, 0.f} : csp[i % 7]);
      PIN();
    }
  };
  {
    v4u zc[3];
#pragma unroll
    for (int i = 0; i < 3; ++i) zc[i] = hld128(ws, i < 2 ? tid * 32 + i * 8192 : z3off, O_ZT + (4 * tm + (tn >> 1)) * ZP + (tn & 1) * 16);
    PIN();
#pragma unroll
    for (int i = 0; i < 3; ++i)
#pragma unroll
      for (int e = 0; e < 4; ++e) *(LAS unsigned*)(L + RED_OFF + e * 3072 + (tid + 256 * i) * 4) = zc[i][e];
    WGBAR();
    const v4u z0 = *(LAS v4u*)(L + RED_OFF + w * 3072 + rcm * 4), z1 = *(LAS v4u*)(L + RED_OFF + w * 3072 + rcm * 4 + 16);
    const v2u z2 = *(LAS v2u*)(L + RED_OFF + w * 3072 + rco * 4);
    LB3(10, 19);
    f2 xv[10], hv[10], av[10], s, ss, r, mr;
#pragma unroll
    for (int i = 0; i < 10; ++i) {
      xv[i] = gh[i] * up2(i < 4 ? z0[i & 3] : (i < 8 ? z1[i & 3] : z2[i & 1])) + qv[i];
      s = i ? s + xv[i] : xv[i];
      ss = i ? ss + xv[i] * xv[i] : xv[i] * xv[i];
    }
    wave_sum4(s, ss);
    ln_scal(s, ss, r, mr);
#pragma unroll
    for (int i = 0; i < 10; ++i) {
      hv[i] = (xv[i] * r + mr) * cb1[i] + sbv[i];
      s = i ? s + hv[i] : hv[i];
      ss = i ? ss + hv[i] * hv[i] : hv[i] * hv[i];
    }
    wave_sum4(s, ss);
    ln_scal(s, ss, r, mr);
#pragma unroll
    for (int i = 0; i < 10; ++i) av[i] = hv[i] * r + mr;
    auto put = [&](const f2(&v)[10], int img) {
      *(LAS v4u*)(L + im0 + img * 10240) = v4u{pk2(v[0].x, v[1].x), pk2(v[2].x, v[3].x), pk2(v[4].x, v[5].x), pk2(v[6].x, v[7].x)};
      *(LAS v4u*)(L + (im0 ^ 16) + img * 10240) = v4u{pk2(v[0].y, v[1].y), pk2(v[2].y, v[3].y), pk2(v[4].y, v[5].y), pk2(v[6].y, v[7].y)};
      *(LAS unsigned*)(L + io0 + img * 10240) = pk2(v[8].x, v[9].x);
      *(LAS unsigned*)(L + (io0 ^ 16) + img * 10240) = pk2(v[8].y, v[9].y);
    };
    put(av, 0);
    WGBAR();
    {
      v4u oc[3];
#pragma unroll
      for (int i = 0; i < 3; ++i) oc[i] = *(LAS v4u*)(L + RED_OFF + 16384 + i * 4096 + tid * 16);
#pragma unroll
      for (int i = 0; i < 3; ++i) hst128(oc[i], ws, i < 2 ? roff : roff3, i * 8192);
    }
    PIN();
    put(hv, 1);
    put(xv, 2);
    DRAIN();
    WGBAR();
    if (w == 0) hst64(tag, ws, frpub, 0);
    LB3(19, 28);
    PIN();
#pragma unroll
    for (int i = 0; i < 6; ++i) hx[i] = *(LAS v4u*)(L + RED_OFF + 26624 + (i / 3) * 10240 + (i % 3) * 4096 + tid * 16);
    img3(0, 16, 16);
    if (w == 0) poll64(ws, frwait, tag);
    WGBAR();
  }

  {
    f4 acc[5];
    gemm_ks(ws, va1, som, b1, L, w, lane, acc,
            [&](int n) { if (n < 7) { LB3(28 + n, 29 + n); } else lb4(16 + n); },
            [&](int n, int t) {
              if (t == 3 && n >= 8 && n < 14) hst128(hx[n - 8], ws, ((n - 8) % 3) < 2 ? roff : roff3, (1 + (n - 8) / 3) * SD2 + ((n - 8) % 3) * 8192);
            });
    f2 v[10];
    unsigned o[10];
#pragma unroll
    for (int t = 0; t < 5; ++t) {
      v[2 * t] = f2{acc[t][0], acc[t][1]};
      v[2 * t + 1] = f2{acc[t][2], acc[t][3]};
    }
    gelu_pack<10>(v, GC0, GC1, 1.f, o, [](int) {});
    const auto s0 = __builtin_amdgcn_permlane16_swap(o[8], o[8], false, false);
    const auto s1 = __builtin_amdgcn_permlane16_swap(o[9], o[9], false, false);
    hst128(v4u{o[0], o[1], o[2], o[3]}, ws, h1m, 0);
    hst128(v4u{o[4], o[5], o[6], o[7]}, ws, h1m + 1024, 0);
    hst128(v4u{s0[0], s1[0], s0[1], s1[1]}, ws, h1s, 0);
    DRAIN();
    WGBAR();
    if (w == 0) hst64(tag, ws, f1pub, 0);
    LB4(0, 9);
    PIN();
    img3(16, 24, 35);
    if (l15 == 0) {
#pragma unroll
      for (int t = 0; t < 7; ++t) *(LAS f4*)(L + (w * 25 + 24) * 1024 + (t * 4 + g) * 16) = csp[t];
    }
    if (w == 0) poll64(ws, f1wait, tag);
    WGBAR();
  }
  v4u xr[2];
  v2u xro;
  unsigned x1p[10];
  {
    v4u hr[2];
    v2u hro;
    f4 acc[5];
    f2 hf[10];
    gemm_ks(ws, vh1, som, b2, L, w, lane, acc, [&](int n) {
      lb4(n < 5 ? 9 + n : 28 + n);
    }, [&](int n, int t) {
      if (t == 3 && n == 2) hr[0] = hld128(ws, lane * 16, O_HP + own_m);
      if (t == 3 && n == 4) hr[1] = hld128(ws, lane * 16 + 1024, O_HP + own_m);
      if (t == 3 && n == 6) hro = hld64(ws, own_r, O_HP + own_s);
      if (n == 18) {
        unsigned h0 = t < 4 ? hr[t >> 1][2 * (t & 1)] : hro.x;
        KEEP(h0);
        hf[2 * t] = up2(h0);
        KEEP(hf[2 * t]);
      }
    });
#pragma unroll
    for (int t = 0; t < 5; ++t) {
      const unsigned h1 = t < 4 ? hr[t >> 1][2 * (t & 1) + 1] : hro.y;
      const f2 p = f2{acc[t][0], acc[t][1]} + hf[2 * t], q = f2{acc[t][2], acc[t][3]} + up2(h1);
      x1p[2 * t] = pk2(p.x, p.y);
      x1p[2 * t + 1] = pk2(q.x, q.y);
    }
    const auto s0 = __builtin_amdgcn_permlane16_swap(x1p[8], x1p[8], false, false);
    const auto s1 = __builtin_amdgcn_permlane16_swap(x1p[9], x1p[9], false, false);
    hst128(v4u{x1p[0], x1p[1], x1p[2], x1p[3]}, ws, x1m, 0);
    hst128(v4u{x1p[4], x1p[5], x1p[6], x1p[7]}, ws, x1m + 1024, 0);
    hst128(v4u{s0[0], s1[0], s0[1], s1[1]}, ws, x1s, 0);
    float t1 = 0.f, t2 = 0.f;
#pragma unroll
    for (int i = 0; i < 10; ++i) {
      t1 = __builtin_amdgcn_fdot2_f32_bf16(__builtin_bit_cast(bf2, x1p[i]), __builtin_bit_cast(bf2, 0x3f803f80u), t1, false);
      t2 = __builtin_amdgcn_fdot2_f32_bf16(__builtin_bit_cast(bf2, x1p[i]), __builtin_bit_cast(bf2, x1p[i]), t2, false);
    }
    t1 = swap32_add(t1, t2);
    t1 = swap16_add(t1, t1);
    hst32(__builtin_bit_cast(unsigned, t1), ws, stoff, 0);
    f4 cst;
    {
      f4 cp[4];
#pragma unroll
      for (int v = 0; v < 4; ++v) cp[v] = *(LAS f4*)(L + (v * 25 + 24) * 1024 + lane * 16);
      cst = (cp[0] + cp[1]) + (cp[2] + cp[3]);
      KEEP(cst);
    }
    PIN();
    DRAIN();
    WGBAR();
    if (w == 0) hst64(tag, ws, f2pub, 0);
    LB4(14, 23);
    PIN();
    img3(24, 35, 0);
    if (w == 1) *(LAS f4*)(L + CSUM_OFF + lane * 16) = cst;
    if (w == 0) poll64(ws, f2wait, tag);
    WGBAR();
  }
  constexpr int WIN4 = 8, RING4 = WIN4 + 1, PUBK = 10, PUBN = 9, SMPN = 20, CHKN = 26;
  v4u a4[RING4];
  auto lda4 = [&](int n) {
    const int i = n < 32 ? n >> 3 : (n - 32) / 6, k = ks4(n < 32 ? n & 7 : 8 + (n - 32) % 6);
    a4[n % RING4] = hld128(ws, lane * 16 + (k & 3) * 1024, O_H + ((4 * tm + ((w + 1 + i) & 3)) * 56 + 14 * w + (k & ~3)) * 1024);
  };
  {
    constexpr int W3 = 6;
    v4u a[20][2];
    v2u sv[2][2];
    const unsigned sa = sa3;
    auto lda = [&](int k, int i) { a[k][i] = hld128(ws, lane * 16 + (k & 3) * 1024, sa + i * 20480 + (k >> 2) * 4096); };
#pragma unroll
    for (int k = 0; k < W3; ++k)
#pragma unroll
      for (int i = 0; i < 2; ++i) {
        lda(k, i);
        if (k == 1) {
          sv[i][0] = hld64(ws, l15 * 8 + g * 256, O_STAT + (8 * rg + 2 * w + i) * 1024);
          sv[i][1] = hld64(ws, l15 * 8 + g * 256 + 128, O_STAT + (8 * rg + 2 * w + i) * 1024);
        }
        PIN();
      }
    f4 acc[2][7];
    f2 gc0, gc1, den, mean;
    unsigned hs, hs1, lcs;
    auto bslot = [](int k, int t) { const int v = k / 5, i = (k % 5) * 7 + t; return (i < 25 ? v * 25 + i : 75 + v * 10 + i) * 1024; };
    const LAS char* Lb = L + lane * 16;
    const LAS char* Lb2 = L + lane * 16 + 65536;
    const LAS char* Lb3 = L + lane * 16 + 131072;
    auto rdb = [&](int k, int t) { const int o = bslot(k, t); return *(const LAS v4u*)(o < 65536 ? Lb + o : (o < 131072 ? Lb2 + (o - 65536) : Lb3 + (o - 131072))); };
    v4u bf[3][4];
#pragma unroll
    for (int t = 0; t < 8; ++t) {
      bf[t >> 2][t & 3] = rdb(t >> 2, t & 3);
      PIN();
    }
    sfor<0, 20>([&](auto ic) {
      constexpr int k = decltype(ic)::v;
      __builtin_amdgcn_s_waitcnt(k < 19 ? 0xC47F : 0xC37F);
#pragma unroll
      for (int t = 0; t < 4; ++t) {
        if (k == 0) mfma_va0(acc[0][t], bf[0][t], a[0][0]);
        else mfma_va(acc[0][t], bf[k % 3][t], a[k][0]);
        if (k < 18) bf[(k + 2) % 3][t] = rdb(k + 2, t);
        else if (t < 3) bf[(k + 2) % 3][t] = rdb(k - 18, 4 + t);
        PIN();
      }
#pragma unroll
      for (int t = 0; t < 4; ++t) {
        if (k == 0) mfma_va0(acc[1][t], bf[0][t], a[0][1]);
        else mfma_va(acc[1][t], bf[k % 3][t], a[k][1]);
        if (t < 2) {
          if constexpr (k + W3 < 20) lda(k + W3, t);
        }
        if (t >= (k + W3 < 20 ? 2 : 0) && t < (k + W3 < 20 ? 4 : 2)) {
          if constexpr (k >= 14 && k < 18) lb4(40 + 2 * (k - 14) + (t & 1));
        }
        if (k == 4 && t == 3) {
          asm volatile("" : "+v"(sv[0][0]), "+v"(sv[0][1]), "+v"(sv[1][0]), "+v"(sv[1][1]));
          const f2 p0 = __builtin_bit_cast(f2, sv[0][0]) + __builtin_bit_cast(f2, sv[0][1]);
          const f2 p1 = __builtin_bit_cast(f2, sv[1][0]) + __builtin_bit_cast(f2, sv[1][1]);
          f2 r, mr;
          ln_scal(f2{xrow_sum(p0.x), xrow_sum(p1.x)}, f2{xrow_sum(p0.y), xrow_sum(p1.y)}, r, mr, &mean, &den);
          gc0 = r * GC0;
          gc1 = r * r * r * GC1;
          hs = O_H + ((8 * rg + 2 * w) * 56 + 7 * (cg >> 1) + 4 * (cg & 1)) * 1024 + lane * 16;
          hs1 = O_H + ((8 * rg + 2 * w) * 56 + 7 * (cg >> 1) + 3) * 1024 + (cg & 1) * 512 + (g & 1) * 57344 + (l15 + 16 * (g >> 1)) * 16;
          lcs = CSUM_OFF + g * 16;
          asm volatile("" : "+v"(gc0), "+v"(gc1), "+v"(mean), "+v"(den), "+v"(hs), "+v"(hs1), "+v"(lcs));
        }
        if (k == 19) csp[t] = *(LAS f4*)(L + lcs + t * 64);
        PIN();
      }
    });
    MFENCE();
    PIN();
#pragma unroll
    for (int i = 0; i < 2; ++i) {
      f2 v[8];
      unsigned o[8];
#pragma unroll
      for (int e = 0; e < 8; ++e) {
        const int t = e >> 1, h = 2 * (e & 1);
        v[e] = f2{acc[i][t][h], acc[i][t][h + 1]} - f2{csp[t][h], csp[t][h + 1]} * mean[i];
      }
      gelu_pack<8>(v, gc0[i], gc1[i], den[i], o, [&](int m) {
        if (i == 0) lb4(28 + m);
        else if (m == 0) lb4(32);
      });
      hst128(v4u{o[0], o[1], o[2], o[3]}, ws, hs, i * 57344);
      hst128(v4u{o[4], o[5], o[6], o[7]}, ws, hs, i * 57344 + 1024);
    }
    YOUNG();
    sfor<0, 20>([&](auto ic) {
      constexpr int k = decltype(ic)::v;
      __builtin_amdgcn_s_waitcnt(k < 19 ? 0xC37F : 0xC07F);
      if (k == PUBK) {
        WAITV(10);
        hst64(tag, ws, f3pub, 0);
      }
#pragma unroll
      for (int e = 0; e < 6; ++e) {
        const int i = e / 3, t = e % 3;
        if (k == 0) mfma_va0(acc[i][4 + t], bf[2][t], a[0][i]);
        else mfma_va(acc[i][4 + t], bf[(k + 2) % 3][t], a[k][i]);
        if (i == 0 && k < 18) bf[(k + 1) % 3][t] = rdb(k + 2, 4 + t);
        if (k == 19 && i == 0) csp[4 + t] = *(LAS f4*)(L + lcs + (4 + t) * 64);
        if (k >= 1 && k < 5 && e == 5) lb4(47 + k);
        if (k >= 1 && k < 19 && e == 3) lb4(51 + k);
        PIN();
      }
    });
    v2u fl;
    MFENCE();
    PIN();
    unsigned os[2][2];
#pragma unroll
    for (int i = 0; i < 2; ++i) {
      f2 v[6];
      unsigned o[6];
#pragma unroll
      for (int e = 0; e < 6; ++e) {
        const int t = 4 + (e >> 1), h = 2 * (e & 1);
        v[e] = f2{acc[i][t][h], acc[i][t][h + 1]} - f2{csp[t][h], csp[t][h + 1]} * mean[i];
      }
      gelu_pack<6>(v, gc0[i], gc1[i], den[i], o, [&](int m) {
        if (i == 0 && m == 0) fl = hld64(ws, f3wait, 0);
      });
      hst128(v4u{o[0], o[1], o[2], o[3]}, ws, hs, i * 57344 + 2048);
      os[i][0] = o[4];
      os[i][1] = o[5];
      if (i == 0) {
        PIN();
        KEEP(fl);
        if (!hit64(fl, tag, 0xffull)) poll64(ws, f3wait, tag);
        __builtin_amdgcn_fence(__ATOMIC_ACQUIRE, "wavefront");
#pragma unroll
        for (int n = 0; n < WIN4; ++n) {
          lda4(n);
          PIN();
        }
      }
    }
    {
      const auto s0 = __builtin_amdgcn_permlane16_swap(os[0][0], os[1][0], false, false);
      const auto s1 = __builtin_amdgcn_permlane16_swap(os[0][1], os[1][1], false, false);
      hst128(v4u{s0[0], s1[0], s0[1], s1[1]}, ws, hs1, 0);
    }
    YOUNG();
  }
  {
    v4u gb[2];
    v2u gbo, fl;
    f4 ac[4][5], p[15];
    float gf[20], cf[20];
    unsigned lq = lane;
    KEEP(lq);
    const unsigned lr = RED_OFF + lq * 16, gq4 = lq >> 4;
    unsigned dm = ((64 * tm + 16 * w + (lq & 15)) * D_ + 80 * tn) * 2, ds = dm + 8 * gq4 + ((tn & 1) ? 0 : 128);
    dm += 16 * gq4 + 32 * (tn & 1);
    unsigned xro_off = ((lq & 15) + 16 * (gq4 >> 1)) * 16 + 8 * (gq4 & 1);
    sfor<0, 56>([&](auto ic) {
      constexpr int n = decltype(ic)::v, i = n < 32 ? n >> 3 : (n - 32) / 6, sl = n < 32 ? n & 7 : 8 + (n - 32) % 6;
      if (n == 54) {
        WGBAR();
      }
      if (n == PUBN) {
        WAITV(7);
        hst64(tag, ws, f3pub + (O_F4 - O_F3), 0);
      }
      if (n == 23) asm volatile("" : "+v"(dm), "+v"(ds), "+v"(xro_off));
      if (n == 37) asm volatile("s_barrier" ::: "memory");
      if (n == CHKN) {
        KEEP(fl);
        if (!hit64(fl, tag, 0xffull)) poll64(ws, f3wait + (O_F4 - O_F3), tag);
        __builtin_amdgcn_fence(__ATOMIC_ACQUIRE, "wavefront");
#pragma unroll
        for (int m = 32; m < CHKN + WIN4; ++m) lda4(m);
        PIN();
      }
#pragma unroll
      for (int t = 0; t < 5; ++t) {
        const int f = sl * 5 + t;
        if (sl == 0) mfma_aa0(ac[i][t], b4[f], a4[n % RING4]);
        else if (f >= 22 && f < 48) mfma_va(ac[i][t], b4[f], a4[n % RING4]);
        else mfma_aa(ac[i][t], b4[f], a4[n % RING4]);
        if (t == 1) {
          if constexpr (n + WIN4 < 56) {
            if (n < 32 - WIN4 || n >= CHKN) lda4(n + WIN4);
          }
        }
        if (t == 3) {
          if (n == 30 || n == 31) gb[n & 1] = hld128(ws, gq4 * 16 + (n & 1) * 64, O_MOD + (5 * D_ + 80 * tn + 16 * (tn & 1)) * 2);
          if (n == 32 || n == 33) xr[n & 1] = hld128(ws, lq * 16 + (n & 1) * 1024, O_X + own_m);
          if (n == 34) gbo = hld64(ws, gq4 * 8, O_MOD + (5 * D_ + 80 * tn + ((tn & 1) ? 0 : 64)) * 2);
          if (n == 35) xro = hld64(ws, xro_off, O_X + own_s);
        }
        if constexpr (n >= 44 && n < 54) {
          constexpr int q = n - 44;
          const unsigned gw = q < 8 ? gb[(q >> 2) & 1][q & 3] : gbo[q & 1], xw = q < 8 ? xr[(q >> 2) & 1][q & 3] : xro[q & 1];
          float tmp;
          if (t == 1) asm volatile("v_lshlrev_b32 %0, 16, %2\n\tv_and_b32 %1, 0xffff0000, %2" : "=&v"(gf[2 * q]), "=v"(gf[2 * q + 1]) : "v"(gw));
          if (t == 2) asm volatile("v_lshlrev_b32 %0, 16, %2\n\tv_and_b32 %1, 0xffff0000, %2" : "=&v"(cf[2 * q]), "=v"(cf[2 * q + 1]) : "v"(xw));
          if (t == 3) asm volatile("v_lshlrev_b32 %1, 16, %2\n\tv_fmac_f32 %0, %1, %3" : "+v"(cf[2 * q]), "=&v"(tmp) : "v"(x1p[q]), "v"(gf[2 * q]));
          if (t == 4) asm volatile("v_and_b32 %1, 0xffff0000, %2\n\tv_fmac_f32 %0, %1, %3" : "+v"(cf[2 * q + 1]), "=&v"(tmp) : "v"(x1p[q]), "v"(gf[2 * q + 1]));
        }
        if (n == SMPN && t == 0) fl = hld64(ws, f3wait + (O_F4 - O_F3), 0);
        if (n >= 38 && sl < 11 && (t == 0 || t == 2) && (sl - 8) * 2 + (t == 2) > 0) {
          const int u = (sl - 8) * 2 + (t == 2) - 1;
          *(LAS f4*)(L + (lr + ((w + i) & 3) * 15360) + ((i - 1) * 5 + u) * 1024) = ac[i > 0 ? i - 1 : 0][u];
        }
        if (n == 54) {
          p[t] = *(LAS f4*)(L + (lr + w * 15360) + t * 1024);
          p[5 + t] = *(LAS f4*)(L + (lr + w * 15360) + (5 + t) * 1024);
        }
        if (n == 55) p[10 + t] = *(LAS f4*)(L + (lr + w * 15360) + (10 + t) * 1024);
        PIN();
      }
    });
    MFENCE();
    PIN();
    unsigned o[10];
#pragma unroll
    for (int t = 0; t < 5; ++t) {
      const f4 y = (ac[3][t] + p[t]) + (p[5 + t] + p[10 + t]);
      const f2 u = f2{y[0], y[1]} * f2{gf[4 * t], gf[4 * t + 1]} + f2{cf[4 * t], cf[4 * t + 1]};
      const f2 v = f2{y[2], y[3]} * f2{gf[4 * t + 2], gf[4 * t + 3]} + f2{cf[4 * t + 2], cf[4 * t + 3]};
      o[2 * t] = pk2(u.x, u.y);
      o[2 * t + 1] = pk2(v.x, v.y);
    }
    const rsrc_t ro = mkrs(gout, SD2);
    __builtin_amdgcn_raw_buffer_store_b128(v4u{o[0], o[1], o[2], o[3]}, ro, dm, 0, 0);
    __builtin_amdgcn_raw_buffer_store_b128(v4u{o[4], o[5], o[6], o[7]}, ro, dm + 64, 0, 0);
    __builtin_amdgcn_raw_buffer_store_b64(v2u{o[8], o[9]}, ro, ds, 0, 0);
  }
  asm volatile(".Lfused_end:" ::: "memory");
}

extern "C" void kernel_launch(void* const* d_in, const int* in_sizes, int n_in, void* d_out, int out_size,
                              void* d_ws, size_t ws_size, hipStream_t stream) {
  (void)hipFuncSetAttribute((const void*)fused, hipFuncAttributeMaxDynamicSharedMemorySize, LDS_BYTES);
  fused<<<256, 256, LDS_BYTES, stream>>>(d_in[0], d_in[1], d_in[2], d_in[3], d_in[4], d_in[5], d_in[7], d_in[8], d_in[9],
                                         d_in[10], d_in[11], d_in[12], d_out, d_ws);
}
```
